```python
import torch

def _softmax(x, axis=-1):
    return torch.softmax(x, dim=axis)
def _gelu(x):
    return torch.nn.functional.gelu(x, approximate="tanh")
def _silu(x):
    return torch.nn.functional.silu(x)
def _sigmoid(x):
    return torch.sigmoid(x)
def _layernorm(x, axis=-1, eps=1e-5):
    m = x.mean(dim=axis, keepdim=True)
    v = x.var(dim=axis, keepdim=True, unbiased=False)
    return (x - m) / torch.sqrt(v + eps)
def _scatter_add(tgt, idx, src, axis=0):
    return tgt.clone().index_add_(axis, idx, src)
def _segment_sum(src, seg, n):
    out = torch.zeros((n,) + src.shape[1:], dtype=src.dtype)
    return out.index_add_(0, seg, src)
def _pad(x, pw):
    flat = tuple(v for pair in reversed(pw) for v in pair)
    return torch.nn.functional.pad(x, flat)

kernel_name = 'concat_heads__compressed_kv_cla__sliding_local_dilated__prompt'

def setup_inputs():

    _w = (lambda a: (lambda t: t.to(torch.bfloat16) if t.is_floating_point() else t)(torch.as_tensor(a)))
    def _A_setup():
        B, H, Sq, Sk, D, M = 1, 32, 2048, 2048, 128, 640
        Dc = 4 * D
        return {
            "x": _w((lambda *s: torch.randn(*s, dtype=torch.bfloat16) * 0.2)(B, Sq, M)),
            "c_kv": _w((lambda *s: torch.randn(*s, dtype=torch.bfloat16) * 1.6)(B, Sk, Dc)),
            "w_q": _w((lambda *s: torch.randn(*s, dtype=torch.bfloat16) * 3.2)(M, H, D) * (M ** -0.5)),
            "w_uk": _w((lambda *s: torch.randn(*s, dtype=torch.bfloat16) * 3.2)(Dc, H, D) * (Dc ** -0.5)),
            "w_uv": _w((lambda *s: torch.randn(*s, dtype=torch.bfloat16) * 1.6)(Dc, H, D) * (Dc ** -0.5)),
            "w_o": _w((lambda *s: torch.randn(*s, dtype=torch.bfloat16) * 1.6)(H, D, M) * ((H * D) ** -0.5)),
        }
    _A_in = _A_setup()

    def _B_setup():
        B, S, D = 1, 2048, 640
        return {
            "x": _w((lambda *s: torch.randn(*s, dtype=torch.bfloat16) * 0.4)(B, S, D)),
            "wqkv": _w((lambda *s: torch.randn(*s, dtype=torch.bfloat16) * 0.4)(D, 3 * D)),
            "wo": _w((lambda *s: torch.randn(*s, dtype=torch.bfloat16) * 0.05)(D, D)),
        }
    _B_in = _B_setup()

    return {
        "A_x": _A_in["x"],
        "A_c_kv": _A_in["c_kv"],
        "A_w_q": _A_in["w_q"],
        "A_w_uk": _A_in["w_uk"],
        "A_w_uv": _A_in["w_uv"],
        "A_w_o": _A_in["w_o"],
        "B_x": _B_in["x"],
        "B_wqkv": _B_in["wqkv"],
        "B_wo": _B_in["wo"],
    }

def reference(A_x, A_c_kv, A_w_q, A_w_uk, A_w_uv, A_w_o, B_x, B_wqkv, B_wo):
    def _A_ref(x, c_kv, w_q, w_uk, w_uv, w_o):
        B, Sq, M = x.shape[0], x.shape[1], x.shape[2]
        Sk = c_kv.shape[1]
        H, D = w_q.shape[1], w_q.shape[2]
        q = torch.einsum("bqm,mhd->bhqd", x, w_q)
        k = torch.einsum("bkc,chd->bhkd", c_kv, w_uk)
        v = torch.einsum("bkc,chd->bhkd", c_kv, w_uv)
        scores = torch.einsum("bhqd,bhkd->bhqk", q, k) * (D ** -0.5)
        off = Sk - Sq
        qi = torch.arange(Sq)[:, None]
        kj = torch.arange(Sk)[None, :]
        cmask = kj <= (qi + off)
        scores = torch.where(cmask[None, None], scores, -1e9)
        p = _softmax(scores, axis=-1)
        ctx = torch.einsum("bhqk,bhkd->bhqd", p, v)
        return torch.einsum("bhqd,hdm->bqm", ctx, w_o)

    def _B_ref(x, wqkv, wo):
        B, S, D = x.shape
        H = 32; Dh = D // H
        W, dil = 8, 2
        qkv = (lambda x, *d: x.permute(*d))((x @ wqkv).reshape(B, S, 3, H, Dh), 2, 0, 3, 1, 4)
        q, k, v = qkv[0], qkv[1], qkv[2]
        scale = Dh ** -0.5
        pos = torch.arange(S)
        off = torch.arange(W) * dil
        idx = pos[:, None] - off[None, :]
        mask = idx >= 0
        idx_c = torch.where(mask, idx, 0).reshape(-1)
        kg = k[:, :, idx_c, :].reshape(B, H, S, W, Dh)
        vg = v[:, :, idx_c, :].reshape(B, H, S, W, Dh)
        logits = torch.einsum("bhsd,bhswd->bhsw", q, kg) * scale
        logits = torch.where(mask[None, None, :, :], logits, -1e9)
        attn = _softmax(logits, axis=-1)
        o = torch.einsum("bhsw,bhswd->bhsd", attn, vg)
        return (lambda x, *d: x.permute(*d))(o, 0, 2, 1, 3).reshape(B, S, D) @ wo

    a = _A_ref(A_x, A_c_kv, A_w_q, A_w_uk, A_w_uv, A_w_o)
    b = _B_ref(B_x, B_wqkv, B_wo)
    return torch.concatenate([a, b], axis=-1)
```

```cpp
#include <hip/hip_runtime.h>
#include <hip/hip_bf16.h>
typedef float f32x4 __attribute__((ext_vector_type(4)));
typedef float f32x2 __attribute__((ext_vector_type(2)));
typedef __bf16 bf16x8 __attribute__((ext_vector_type(8)));
typedef __bf16 bf16x2 __attribute__((ext_vector_type(2)));
typedef unsigned u32x4 __attribute__((ext_vector_type(4)));
typedef unsigned u32x2 __attribute__((ext_vector_type(2)));
#define DI __device__ __forceinline__
constexpr size_t WS_KV = 0, WS_CTX = 32u << 20, WS_QKV = 48u << 20, WS_OB = 56u << 20, WS_WA = 60u << 20, WS_WB = 66u << 20, WS_SYNC = 70u << 20, WS_QS = 84u << 20;
DI unsigned cvtpk(float lo, float hi) { f32x2 v = {lo, hi}; return __builtin_bit_cast(unsigned, __builtin_convertvector(v, bf16x2)); }
DI float rbf(float x) { return __builtin_bit_cast(float, cvtpk(x, 0.f) << 16); }
DI float dot2(unsigned a, unsigned b, float c) { return __builtin_amdgcn_fdot2_f32_bf16(__builtin_bit_cast(bf16x2, a), __builtin_bit_cast(bf16x2, b), c, false); }

typedef __amdgpu_buffer_rsrc_t rsrc_t;
constexpr int SC1 = 16;

#define PFS0 "s_lshr_b32 s12, %7, 9\n s_add_u32 s12, s12, 0\n s_and_b32 s12, s12, 3\n s_mul_i32 s12, s12, 20480\n s_add_u32 s12, s12, %1\n s_add_u32 s12, s12, 16384\n buffer_load_dwordx4 a[128:131], %2, %3, s12 offen offset:0 sc1\n buffer_load_dwordx4 a[132:135], %2, %3, s12 offen offset:1024 sc1\n buffer_load_dwordx4 a[136:139], %2, %3, s12 offen offset:2048 sc1\n buffer_load_dwordx4 a[140:143], %2, %3, s12 offen offset:3072 sc1"
#define PFS1 "s_lshr_b32 s12, %7, 9\n s_add_u32 s12, s12, 1\n s_and_b32 s12, s12, 3\n s_mul_i32 s12, s12, 20480\n s_add_u32 s12, s12, %1\n s_add_u32 s12, s12, 0\n buffer_load_dwordx4 a[144:147], %2, %3, s12 offen offset:0 sc1\n buffer_load_dwordx4 a[148:151], %2, %3, s12 offen offset:1024 sc1\n buffer_load_dwordx4 a[152:155], %2, %3, s12 offen offset:2048 sc1\n buffer_load_dwordx4 a[156:159], %2, %3, s12 offen offset:3072 sc1"
#define PFS2 "s_waitcnt vmcnt(8)\n buffer_atomic_add %4, %5, %3, %6 offen sc1\n s_lshr_b32 s12, %7, 9\n s_add_u32 s12, s12, 1\n s_and_b32 s12, s12, 3\n s_mul_i32 s12, s12, 20480\n s_add_u32 s12, s12, %1\n s_add_u32 s12, s12, 4096\n buffer_load_dwordx4 a[160:163], %2, %3, s12 offen offset:0 sc1\n buffer_load_dwordx4 a[164:167], %2, %3, s12 offen offset:1024 sc1\n buffer_load_dwordx4 a[168:171], %2, %3, s12 offen offset:2048 sc1\n buffer_load_dwordx4 a[172:175], %2, %3, s12 offen offset:3072 sc1"
#define PFS3 "s_lshr_b32 s12, %7, 9\n s_add_u32 s12, s12, 1\n s_and_b32 s12, s12, 3\n s_mul_i32 s12, s12, 20480\n s_add_u32 s12, s12, %1\n s_add_u32 s12, s12, 8192\n buffer_load_dwordx4 a[176:179], %2, %3, s12 offen offset:0 sc1\n buffer_load_dwordx4 a[180:183], %2, %3, s12 offen offset:1024 sc1\n buffer_load_dwordx4 a[184:187], %2, %3, s12 offen offset:2048 sc1\n buffer_load_dwordx4 a[188:191], %2, %3, s12 offen offset:3072 sc1"
#define PFS4 "s_lshr_b32 s12, %7, 9\n s_add_u32 s12, s12, 1\n s_and_b32 s12, s12, 3\n s_mul_i32 s12, s12, 20480\n s_add_u32 s12, s12, %1\n s_add_u32 s12, s12, 12288\n buffer_load_dwordx4 a[192:195], %2, %3, s12 offen offset:0 sc1\n buffer_load_dwordx4 a[196:199], %2, %3, s12 offen offset:1024 sc1\n buffer_load_dwordx4 a[200:203], %2, %3, s12 offen offset:2048 sc1\n buffer_load_dwordx4 a[204:207], %2, %3, s12 offen offset:3072 sc1"
#define PFS5 "s_lshr_b32 s12, %7, 9\n s_add_u32 s12, s12, 1\n s_and_b32 s12, s12, 3\n s_mul_i32 s12, s12, 20480\n s_add_u32 s12, s12, %1\n s_add_u32 s12, s12, 16384\n buffer_load_dwordx4 a[208:211], %2, %3, s12 offen offset:0 sc1\n buffer_load_dwordx4 a[212:215], %2, %3, s12 offen offset:1024 sc1\n buffer_load_dwordx4 a[216:219], %2, %3, s12 offen offset:2048 sc1\n buffer_load_dwordx4 a[220:223], %2, %3, s12 offen offset:3072 sc1\n s_lshr_b32 s12, %7, 9\n s_add_u32 s12, s12, 2\n s_and_b32 s12, s12, 3\n s_mul_i32 s12, s12, 20480\n s_add_u32 s12, s12, %1\n s_add_u32 s12, s12, 0\n buffer_load_dwordx4 a[224:227], %2, %3, s12 offen offset:0 sc1\n buffer_load_dwordx4 a[228:231], %2, %3, s12 offen offset:1024 sc1\n buffer_load_dwordx4 a[232:235], %2, %3, s12 offen offset:2048 sc1\n buffer_load_dwordx4 a[236:239], %2, %3, s12 offen offset:3072 sc1"
#define PFS6 "s_lshr_b32 s12, %7, 9\n s_add_u32 s12, s12, 2\n s_and_b32 s12, s12, 3\n s_mul_i32 s12, s12, 20480\n s_add_u32 s12, s12, %1\n s_add_u32 s12, s12, 4096\n buffer_load_dwordx4 a[240:243], %2, %3, s12 offen offset:0 sc1\n buffer_load_dwordx4 a[244:247], %2, %3, s12 offen offset:1024 sc1\n buffer_load_dwordx4 a[248:251], %2, %3, s12 offen offset:2048 sc1\n buffer_load_dwordx4 a[252:255], %2, %3, s12 offen offset:3072 sc1"
#define PFS7 "s_lshr_b32 s12, %7, 9\n s_add_u32 s12, s12, 2\n s_and_b32 s12, s12, 3\n s_mul_i32 s12, s12, 20480\n s_add_u32 s12, s12, %1\n s_add_u32 s12, s12, 8192\n buffer_load_dwordx4 a[0:3], %2, %3, s12 offen offset:0 sc1\n buffer_load_dwordx4 a[4:7], %2, %3, s12 offen offset:1024 sc1\n buffer_load_dwordx4 a[8:11], %2, %3, s12 offen offset:2048 sc1\n buffer_load_dwordx4 a[12:15], %2, %3, s12 offen offset:3072 sc1"
#define PFS8 "s_lshr_b32 s12, %7, 9\n s_add_u32 s12, s12, 2\n s_and_b32 s12, s12, 3\n s_mul_i32 s12, s12, 20480\n s_add_u32 s12, s12, %1\n s_add_u32 s12, s12, 12288\n buffer_load_dwordx4 a[16:19], %2, %3, s12 offen offset:0 sc1\n buffer_load_dwordx4 a[20:23], %2, %3, s12 offen offset:1024 sc1\n buffer_load_dwordx4 a[24:27], %2, %3, s12 offen offset:2048 sc1\n buffer_load_dwordx4 a[28:31], %2, %3, s12 offen offset:3072 sc1"
#define PFS9 "s_lshr_b32 s12, %7, 9\n s_add_u32 s12, s12, 2\n s_and_b32 s12, s12, 3\n s_mul_i32 s12, s12, 20480\n s_add_u32 s12, s12, %1\n s_add_u32 s12, s12, 16384\n buffer_load_dwordx4 a[32:35], %2, %3, s12 offen offset:0 sc1\n buffer_load_dwordx4 a[36:39], %2, %3, s12 offen offset:1024 sc1\n buffer_load_dwordx4 a[40:43], %2, %3, s12 offen offset:2048 sc1\n buffer_load_dwordx4 a[44:47], %2, %3, s12 offen offset:3072 sc1"
#define PFS10 "s_lshr_b32 s12, %7, 9\n s_add_u32 s12, s12, 3\n s_and_b32 s12, s12, 3\n s_mul_i32 s12, s12, 20480\n s_add_u32 s12, s12, %1\n s_add_u32 s12, s12, 0\n buffer_load_dwordx4 a[48:51], %2, %3, s12 offen offset:0 sc1\n buffer_load_dwordx4 a[52:55], %2, %3, s12 offen offset:1024 sc1\n buffer_load_dwordx4 a[56:59], %2, %3, s12 offen offset:2048 sc1\n buffer_load_dwordx4 a[60:63], %2, %3, s12 offen offset:3072 sc1"
#define PFS11 "s_add_u32 m0, %0, 0\n s_lshr_b32 s12, %7, 9\n s_add_u32 s12, s12, 3\n s_and_b32 s12, s12, 3\n s_mul_i32 s12, s12, 20480\n s_add_u32 s12, s12, %1\n s_add_u32 s12, s12, 4096\n buffer_load_dwordx4 %2, %3, s12 offen offset:0 sc1 lds\n buffer_load_dwordx4 %2, %3, s12 offen offset:1024 sc1 lds\n buffer_load_dwordx4 %2, %3, s12 offen offset:2048 sc1 lds\n buffer_load_dwordx4 %2, %3, s12 offen offset:3072 sc1 lds\n s_add_u32 m0, %0, 4096\n s_lshr_b32 s12, %7, 9\n s_add_u32 s12, s12, 3\n s_and_b32 s12, s12, 3\n s_mul_i32 s12, s12, 20480\n s_add_u32 s12, s12, %1\n s_add_u32 s12, s12, 8192\n buffer_load_dwordx4 %2, %3, s12 offen offset:0 sc1 lds\n buffer_load_dwordx4 %2, %3, s12 offen offset:1024 sc1 lds\n buffer_load_dwordx4 %2, %3, s12 offen offset:2048 sc1 lds\n buffer_load_dwordx4 %2, %3, s12 offen offset:3072 sc1 lds"
#define PFW(g) { __builtin_amdgcn_sched_barrier(0); \
  asm volatile(PFS##g :: "s"((h & 3) << 15), "s"(wo), "v"((row & 63) * 16), "s"(rs), "v"(1), "v"((row & 63) ? 0x7f000000 : 0), \
               "s"((int)WS_SYNC + 128 + 128 * (64 + (h >> 2)) + (bid & 4) * 3), "s"(bid) : "s12", "scc"); \
  __builtin_amdgcn_sched_barrier(0); }
DI void local_attn(const char* st, const u32x2* q, rsrc_t rs, int h, int row, int bid) {
  u32x2 kk[8][5], vv[8][5];
  typedef const __attribute__((address_space(3))) char* lds_t;
  typedef const __attribute__((address_space(3))) u32x2* lds2_t;
  lds_t pp[5];
  int wo = WS_WA + ((h >> 2) * 8 + (bid & 4) + (h & 3)) * 81920;
  PFW(0)
#pragma unroll
  for (int i = 0; i < 5; ++i) {
    int g = (h & 4) + 5 * (h & 3) + i;
    pp[i] = (lds_t)st + ((g >> 3) * 4 + (g & 3)) * 1280 + (g & 4) * 2 + ((row & 63) + 2) * 16;
    asm("" : "+v"(pp[i]));
#pragma unroll
    for (int w = 0; w < 8; ++w) kk[w][i] = *(lds2_t)(pp[i] + (7 - w) * 32);
  }
  PFW(1)
  float s[8], m = -3e38f;
#pragma unroll
  for (int w = 0; w < 8; ++w) {
    float a = 0.f;
#pragma unroll
    for (int i = 0; i < 5; ++i)
#pragma unroll
      for (int j = 0; j < 2; ++j)
        a = dot2(q[i][j], kk[w][i][j], a);
    a = rbf(rbf(a) * 0.22360679774997896f);
    s[w] = (row - 2 * w >= 0) ? a : -1e9f;
    m = fmaxf(m, s[w]);
    if (w == 1) PFW(2) if (w == 3) PFW(3) if (w == 5) PFW(4)
    if (w >= 3) {
#pragma unroll
      for (int u = 0; u < 8; ++u) vv[u][w - 3] = *(lds2_t)(pp[w - 3] + 15360 + (7 - u) * 32);
    }
  }
  PFW(5)
  float l = 0.f;
#pragma unroll
  for (int w = 0; w < 8; ++w) { s[w] = __builtin_amdgcn_exp2f((s[w] - m) * 1.4426950408889634f); l += s[w]; }
  PFW(6)
  l = 1.f / l;
  unsigned p[4];
  u32x2 ov[5];
#pragma unroll
  for (int w = 0; w < 4; ++w) p[w] = cvtpk(s[2 * w] * l, s[2 * w + 1] * l);
#pragma unroll
  for (int i = 0; i < 5; ++i) {
    float o[4] = {0.f, 0.f, 0.f, 0.f};
    if (i == 0) PFW(7) if (i == 1) PFW(8) if (i == 2) PFW(9) if (i == 3) PFW(10) if (i == 4) PFW(11)
#pragma unroll
    for (int w = 0; w < 4; ++w)
#pragma unroll
      for (int j = 0; j < 4; ++j) {
        unsigned pr = __builtin_amdgcn_perm(vv[2 * w + 1][i][j >> 1], vv[2 * w][i][j >> 1], (j & 1) ? 0x07060302u : 0x05040100u);
        o[j] = dot2(p[w], pr, o[j]);
      }
    ov[i] = u32x2{cvtpk(o[0], o[1]), cvtpk(o[2], o[3])};
  }
  int base = WS_OB + ((row >> 6) * 80 + ((row >> 4) & 3)) * 1024 + (row & 15) * 16, od = h & 1;
#pragma unroll
  for (int t = 0; t < 2; ++t) {
    int g = (5 * h + 1) / 2 + t;
    u32x2 a = od ? ov[2 * t + 1] : ov[2 * t], b = od ? ov[2 * t + 2] : ov[2 * t + 1];
    __builtin_amdgcn_raw_buffer_store_b128(u32x4{a[0], a[1], b[0], b[1]}, rs, base, (g >> 2) * 4096 + (g & 3) * 256, SC1);
  }
  int g = 5 * (h >> 1) + 2;
  __builtin_amdgcn_raw_buffer_store_b64(od ? ov[0] : ov[4], rs, base, (g >> 2) * 4096 + (g & 3) * 256 + od * 8, SC1);
}

DI void slow_rows(const char* qf, char* ws, rsrc_t rs, int h, int blk, int w, int lane) {
  bf16x8 q[8];
#pragma unroll
  for (int f = 0; f < 8; ++f) q[f] = *(const bf16x8*)(qf + f * 1024 + lane * 16);
  f32x4 o[2][8] = {};
  float m[2] = {-3e38f, -3e38f}, l[2] = {0.f, 0.f};
  int last = 4 * blk + w, thr = (lane & 15) - 4 * (lane >> 4);
#pragma unroll 1
  for (int u = 0; u <= last; ++u) {
    const char* kv = ws + WS_KV + ((size_t)h << 20) + (u >> 1) * 32768 + (u & 1) * 8192 + lane * 16;
#pragma unroll
    for (int t = 0; t < 2; ++t) {
      f32x4 s[2] = {};
#pragma unroll
      for (int f = 0; f < 8; ++f) s[f & 1] = __builtin_amdgcn_mfma_f32_16x16x32_bf16(*(const bf16x8*)(kv + f * 1024), q[4 * t + (f >> 1)], s[f & 1], 0, 0, 0);
      float mx = m[t];
#pragma unroll
      for (int e = 0; e < 8; ++e) { if (u == last && 16 * (e >> 2) + (e & 3) - 16 * t > thr) s[e >> 2][e & 3] = -3e38f; mx = fmaxf(mx, s[e >> 2][e & 3]); }
      mx = fmaxf(mx, __shfl_xor(mx, 16));
      mx = fmaxf(mx, __shfl_xor(mx, 32));
      float al = __builtin_amdgcn_exp2f(m[t] - mx);
      m[t] = mx; l[t] *= al;
      u32x4 p;
#pragma unroll
      for (int e = 0; e < 8; e += 2) {
        float a = __builtin_amdgcn_exp2f(s[e >> 2][e & 3] - mx), b = __builtin_amdgcn_exp2f(s[e >> 2][(e & 3) + 1] - mx);
        l[t] += a + b; p[e >> 1] = cvtpk(a, b);
      }
#pragma unroll
      for (int d = 0; d < 8; ++d)
        o[t][d] = __builtin_amdgcn_mfma_f32_16x16x32_bf16(*(const bf16x8*)(kv + 16384 + d * 1024), __builtin_bit_cast(bf16x8, p), o[t][d] * al, 0, 0, 0);
    }
  }
  int dst = WS_CTX + (last >> 1) * 524288 + (last & 1) * 2048 + h * 16384 + lane * 16;
#pragma unroll
  for (int t = 0; t < 2; ++t) {
    float r = l[t] + __shfl_xor(l[t], 16);
    r = 1.f / (r + __shfl_xor(r, 32));
#pragma unroll
    for (int f = 0; f < 4; ++f) {
      f32x4 a = o[t][2 * f] * r, b = o[t][2 * f + 1] * r;
      u32x4 st = {cvtpk(a[0], a[1]), cvtpk(a[2], a[3]), cvtpk(b[0], b[1]), cvtpk(b[2], b[3])};
      __builtin_amdgcn_raw_buffer_store_b128(st, rs, dst + f * 4096 + t * 1024, 0, SC1);
    }
  }
}

__global__ __launch_bounds__(256, 1) void fused(const char* x, const char* ckv, const char* wq, const char* wuk, const char* wuv, const char* wo,
                                                const char* bx, const char* bw, const char* wb, char* ws, char* out) {
  __shared__ __attribute__((aligned(1024))) char lds[163840];
  int tid = threadIdx.x, bid = blockIdx.x;
  unsigned ret;
  u32x2 q[5];
  auto ka = (const __attribute__((address_space(4))) unsigned long long*)__builtin_amdgcn_kernarg_segment_ptr();
  {
    asm volatile(
  "v_lshrrev_b32 v54, 6, %1\n"
  "s_nop 0\n"
  "v_readfirstlane_b32 s20, v54\n"
  "v_and_b32 v54, 63, %1\n"
  "s_lshr_b32 s53, %2, 3\n"
  "s_getpc_b64 s[22:23]\n"
  "pf%=:\n"
  "s_lshl2_add_u32 s61, s53, s20\n"
  "s_lshl_b32 s61, s61, 10\n"
  "v_lshl_add_u32 v1, v54, 4, s61\n"
  "v_min_u32 v1, p3_fin-pf%=-8, v1\n"
  "global_load_dwordx2 v[126:127], v1, s[22:23]\n"
  "s_load_dwordx2 s[24:25], %3, 8\n"
  "s_load_dwordx2 s[28:29], %3, 24\n"
  "s_load_dwordx2 s[36:37], %3, 32\n"
  "s_load_dwordx2 s[40:41], %3, 0\n"
  "s_load_dwordx2 s[44:45], %3, 16\n"
  "s_load_dwordx2 s[48:49], %3, 48\n"
  "s_load_dwordx2 s[84:85], %3, 56\n"
  "s_load_dwordx2 s[88:89], %3, 72\n"
  "s_load_dwordx2 s[92:93], %3, 72\n"
  "s_load_dwordx2 s[8:9], %3, 40\n"
  "s_load_dwordx2 s[12:13], %3, 64\n"
  "s_mov_b32 s26, 2097152\n"
  "s_mov_b32 s27, 0x20000\n"
  "s_mov_b32 s30, 4194304\n"
  "s_mov_b32 s31, 0x20000\n"
  "s_and_b32 s52, %2, 7\n"
  "s_lshr_b32 s53, %2, 3\n"
  "s_lshr_b32 s54, s53, 3\n"
  "s_lshl2_add_u32 s54, s52, s54\n"
  "s_and_b32 s55, s53, 7\n"
  "s_sub_u32 s56, 15, s55\n"
  "s_lshl2_add_u32 s58, s55, s20\n"
  "s_lshl_b32 s58, s58, 16\n"
  "s_add_u32 s59, s58, 0x8000\n"
  "s_lshl_b32 s60, s20, 13\n"
  "s_lshl_b32 s62, s20, 12\n"
  "v_lshrrev_b32 v55, 4, v54\n"
  "v_and_b32 v56, 7, v54\n"
  "v_xor_b32 v55, v55, v56\n"
  "v_lshlrev_b32 v55, 4, v55\n"
  "v_lshrrev_b32 v56, 3, v54\n"
  "v_mul_u32_u24 v56, 1024, v56\n"
  "v_add_u32 v55, v55, v56\n"
  "v_xor_b32 v56, 64, v55\n"
  "v_add_u32 v16, s58, v55\n"
  "v_add_u32 v17, s58, v56\n"
  "v_add_u32 v17, 7168, v17\n"
  "v_add_u32 v18, s58, v55\n"
  "v_add_u32 v18, 14336, v18\n"
  "v_add_u32 v19, s58, v56\n"
  "v_add_u32 v19, 21504, v19\n"
  "v_add_u32 v20, s59, v55\n"
  "v_add_u32 v21, s59, v56\n"
  "v_add_u32 v21, 7168, v21\n"
  "v_add_u32 v22, s59, v55\n"
  "v_add_u32 v22, 14336, v22\n"
  "v_add_u32 v23, s59, v56\n"
  "v_add_u32 v23, 21504, v23\n"
  "s_lshl_b32 s58, s20, 17\n"
  "s_lshl_b32 s59, s54, 8\n"
  "s_add_u32 s58, s58, s59\n"
  "v_lshrrev_b32 v55, 4, v54\n"
  "v_mul_u32_u24 v56, 8192, v55\n"
  "v_lshlrev_b32 v55, 2, v55\n"
  "v_xor_b32 v55, v55, v54\n"
  "v_and_b32 v55, 15, v55\n"
  "v_lshl_add_u32 v55, v55, 4, v56\n"
  "v_add_u32 v24, s58, v55\n"
  "v_xor_b32 v55, 32, v24\n"
  "v_add_u32 v25, 31744, v24\n"
  "v_add_u32 v26, 63488, v55\n"
  "v_add_u32 v27, 95232, v55\n"
  "s_mov_b32 s38, 4194304\n"
  "s_mov_b32 s39, 0x20000\n"
  "s_waitcnt lgkmcnt(0)\n"
  "s_add_u32 m0, s60, 65536\n"
  "s_mov_b32 s64, 0\n"
  "s_nop 0\n"
  "buffer_load_dwordx4 v16, s[24:27], s64 offen offset:0 lds\n"
  "buffer_load_dwordx4 v17, s[24:27], s64 offen offset:1024 lds\n"
  "buffer_load_dwordx4 v18, s[24:27], s64 offen offset:2048 lds\n"
  "buffer_load_dwordx4 v19, s[24:27], s64 offen offset:3072 lds\n"
  "s_add_u32 m0, s62, 98304\n"
  "s_mov_b32 s64, 0\n"
  "s_nop 0\n"
  "buffer_load_dwordx4 v24, s[28:31], s64 offen offset:0 lds\n"
  "buffer_load_dwordx4 v25, s[28:31], s64 offen offset:1024 lds\n"
  "buffer_load_dwordx4 v26, s[28:31], s64 offen offset:2048 lds\n"
  "buffer_load_dwordx4 v27, s[28:31], s64 offen offset:3072 lds\n"
  "s_add_u32 m0, s60, 69632\n"
  "s_mov_b32 s64, 0\n"
  "s_nop 0\n"
  "buffer_load_dwordx4 v20, s[24:27], s64 offen offset:0 lds\n"
  "buffer_load_dwordx4 v21, s[24:27], s64 offen offset:1024 lds\n"
  "buffer_load_dwordx4 v22, s[24:27], s64 offen offset:2048 lds\n"
  "buffer_load_dwordx4 v23, s[24:27], s64 offen offset:3072 lds\n"
  "s_add_u32 m0, s62, 114688\n"
  "s_mov_b32 s64, 0\n"
  "s_nop 0\n"
  "buffer_load_dwordx4 v24, s[36:39], s64 offen offset:0 lds\n"
  "buffer_load_dwordx4 v25, s[36:39], s64 offen offset:1024 lds\n"
  "buffer_load_dwordx4 v26, s[36:39], s64 offen offset:2048 lds\n"
  "buffer_load_dwordx4 v27, s[36:39], s64 offen offset:3072 lds\n"
  "s_mov_b32 s38, 4194304\n"
  "s_mov_b32 s39, 0x20000\n"
  "s_mov_b32 s42, 2621440\n"
  "s_mov_b32 s43, 0x20000\n"
  "s_mov_b32 s46, 5242880\n"
  "s_mov_b32 s47, 0x20000\n"
  "s_mov_b32 s50, 2621440\n"
  "s_mov_b32 s51, 0x20000\n"
  "s_mov_b32 s86, 2457600\n"
  "s_mov_b32 s87, 0x20000\n"
  "s_mov_b32 s90, 75497472\n"
  "s_mov_b32 s91, 0x20000\n"
  "s_mov_b32 s94, 75497472\n"
  "s_mov_b32 s95, 0x20000\n"
  "s_mov_b32 s10, 5242880\n"
  "s_mov_b32 s11, 0x20000\n"
  "s_mov_b32 s14, 5242880\n"
  "s_mov_b32 s15, 0x20000\n"
  "s_or_b32 s58, %2, s20\n"
  "s_cmp_eq_u32 s58, 0\n"
  "s_cselect_b32 s94, s94, 0\n"
  "s_mov_b32 s59, 73400320\n"
  "v_mov_b32 v96, 0\n"
  "v_mov_b32 v97, 0\n"
  "v_mov_b32 v98, 0\n"
  "v_mov_b32 v99, 0\n"
  "v_lshlrev_b32 v6, 7, v54\n"
  "buffer_load_dword v52, off, s[88:91], s59 sc1\n"
  "buffer_store_dwordx4 v[96:99], v6, s[92:95], s59 offen offset:128 sc1\n"
  "v_add_u32 v6, 0x2000, v6\n"
  "buffer_store_dwordx4 v[96:99], v6, s[92:95], s59 offen offset:128 sc1\n"
  "v_lshrrev_b32 v69, 3, %1\n"
  "v_and_b32 v70, 7, %1\n"
  "v_lshlrev_b32 v70, 7, v70\n"
  "v_lshl_add_u32 v70, v69, 18, v70\n"
  "s_lshl3_add_u32 s58, s53, s52\n"
  "s_lshl_b32 s58, s58, 10\n"
  "v_add_u32 v69, s58, v70\n"
  "v_xor_b32 v70, 8126464, v69\n"
  "s_lshl_b32 s58, s53, 15\n"
  "v_lshl_add_u32 v71, %1, 7, s58\n"
  "v_add_u32 v74, 1048576, v71\n"
  "v_add_u32 v75, 2097152, v71\n"
  "v_lshrrev_b32 v55, 1, v54\n"
  "v_and_b32 v55, 7, v55\n"
  "v_lshrrev_b32 v56, 4, v54\n"
  "v_xor_b32 v55, v55, v56\n"
  "v_lshlrev_b32 v55, 4, v55\n"
  "v_and_b32 v56, 15, v54\n"
  "v_lshl_add_u32 v55, v56, 7, v55\n"
  "v_add_u32 v2, s60, v55\n"
  "v_xor_b32 v3, 64, v2\n"
  "v_lshrrev_b32 v55, 2, v54\n"
  "v_and_b32 v55, 3, v55\n"
  "v_mul_u32_u24 v55, 0x140, v55\n"
  "v_lshrrev_b32 v56, 4, v54\n"
  "v_lshl_add_u32 v55, v56, 11, v55\n"
  "v_bfe_u32 v56, v54, 4, 1\n"
  "v_lshl_add_u32 v55, v56, 5, v55\n"
  "v_and_b32 v56, 3, v54\n"
  "v_lshl_add_u32 v55, v56, 3, v55\n"
  "v_xor_b32 v8, 0, v55\n"
  "v_xor_b32 v9, 32, v8\n"
  "v_xor_b32 v10, 64, v8\n"
  "v_xor_b32 v11, 96, v8\n"
  "v_xor_b32 v12, 128, v8\n"
  "v_xor_b32 v13, 160, v8\n"
  "v_xor_b32 v14, 192, v8\n"
  "v_xor_b32 v15, 224, v8\n"
  "s_add_u32 m0, s60, 0\n"
  "s_mov_b32 s64, 128\n"
  "s_nop 0\n"
  "buffer_load_dwordx4 v16, s[24:27], s64 offen offset:0 lds\n"
  "buffer_load_dwordx4 v17, s[24:27], s64 offen offset:1024 lds\n"
  "buffer_load_dwordx4 v18, s[24:27], s64 offen offset:2048 lds\n"
  "buffer_load_dwordx4 v19, s[24:27], s64 offen offset:3072 lds\n"
  "s_lshl2_add_u32 s58, s56, s20\n"
  "s_mul_i32 s58, s58, 40960\n"
  "s_lshl2_add_u32 s59, s55, s20\n"
  "s_mul_i32 s59, s59, 40960\n"
  "v_lshrrev_b32 v55, 4, v54\n"
  "v_and_b32 v56, 7, v54\n"
  "v_xor_b32 v55, v55, v56\n"
  "v_lshlrev_b32 v55, 4, v55\n"
  "v_lshrrev_b32 v56, 3, v54\n"
  "v_mul_u32_u24 v56, 1280, v56\n"
  "v_add_u32 v55, v55, v56\n"
  "v_xor_b32 v56, 64, v55\n"
  "v_add_u32 v28, s58, v55\n"
  "v_add_u32 v29, s58, v56\n"
  "v_add_u32 v29, 9216, v29\n"
  "v_add_u32 v30, s58, v55\n"
  "v_add_u32 v30, 18432, v30\n"
  "v_add_u32 v31, s58, v56\n"
  "v_add_u32 v31, 27648, v31\n"
  "v_add_u32 v32, s59, v55\n"
  "v_add_u32 v33, s59, v56\n"
  "v_add_u32 v33, 9216, v33\n"
  "v_add_u32 v34, s59, v55\n"
  "v_add_u32 v34, 18432, v34\n"
  "v_add_u32 v35, s59, v56\n"
  "v_add_u32 v35, 27648, v35\n"
  "s_and_b32 s70, s52, 3\n"
  "s_and_b32 s58, s53, 3\n"
  "s_lshl2_add_u32 s70, s70, s58\n"
  "s_lshr_b32 s71, s52, 2\n"
  "s_lshr_b32 s58, s53, 2\n"
  "s_lshl3_add_u32 s71, s71, s58\n"
  "s_cmp_eq_u32 s71, 15\n"
  "s_cselect_b32 s50, 0, s50\n"
  "s_cselect_b32 s86, 0, s86\n"
  "s_lshl2_add_u32 s58, s70, s20\n"
  "s_mul_i32 s58, s58, 40960\n"
  "v_lshrrev_b32 v55, 4, v54\n"
  "v_and_b32 v56, 7, v54\n"
  "v_xor_b32 v55, v55, v56\n"
  "v_lshlrev_b32 v55, 4, v55\n"
  "v_lshrrev_b32 v56, 3, v54\n"
  "v_mul_u32_u24 v56, 1280, v56\n"
  "v_add_u32 v55, v55, v56\n"
  "v_xor_b32 v56, 64, v55\n"
  "v_add_u32 v42, s58, v55\n"
  "v_add_u32 v43, s58, v56\n"
  "v_add_u32 v43, 9216, v43\n"
  "v_add_u32 v44, s58, v55\n"
  "v_add_u32 v44, 18432, v44\n"
  "v_add_u32 v45, s58, v56\n"
  "v_add_u32 v45, 27648, v45\n"
  "s_add_u32 m0, s62, 32768\n"
  "s_mov_b32 s64, 524288\n"
  "s_nop 0\n"
  "buffer_load_dwordx4 v24, s[28:31], s64 offen offset:0 lds\n"
  "buffer_load_dwordx4 v25, s[28:31], s64 offen offset:1024 lds\n"
  "buffer_load_dwordx4 v26, s[28:31], s64 offen offset:2048 lds\n"
  "buffer_load_dwordx4 v27, s[28:31], s64 offen offset:3072 lds\n"
  "s_mul_i32 s58, s20, 61440\n"
  "s_lshl_b32 s59, s71, 8\n"
  "s_add_u32 s58, s58, s59\n"
  "v_lshrrev_b32 v55, 4, v54\n"
  "v_mul_u32_u24 v56, 3840, v55\n"
  "v_lshlrev_b32 v55, 2, v55\n"
  "v_xor_b32 v55, v55, v54\n"
  "v_and_b32 v55, 15, v55\n"
  "v_lshl_add_u32 v55, v55, 4, v56\n"
  "v_add_u32 v48, s58, v55\n"
  "v_xor_b32 v55, 32, v48\n"
  "v_add_u32 v49, 14336, v48\n"
  "v_add_u32 v50, 28672, v55\n"
  "v_add_u32 v51, 43008, v55\n"
  "s_lshr_b32 s72, s20, 1\n"
  "s_and_b32 s73, s20, 1\n"
  "s_lshl_b32 s58, s72, 13\n"
  "v_lshrrev_b32 v55, 1, v54\n"
  "v_and_b32 v55, 7, v55\n"
  "v_lshrrev_b32 v56, 4, v54\n"
  "v_xor_b32 v55, v55, v56\n"
  "v_lshlrev_b32 v55, 4, v55\n"
  "v_and_b32 v56, 15, v54\n"
  "v_lshl_add_u32 v55, v56, 7, v55\n"
  "v_add_u32 v36, s58, v55\n"
  "v_xor_b32 v37, 64, v36\n"
  "s_lshl_b32 s58, s73, 7\n"
  "v_xor_b32 v38, s58, v8\n"
  "v_xor_b32 v39, s58, v9\n"
  "v_xor_b32 v40, s58, v10\n"
  "v_xor_b32 v41, s58, v11\n"
  "s_add_u32 m0, s60, 4096\n"
  "s_mov_b32 s64, 128\n"
  "s_nop 0\n"
  "buffer_load_dwordx4 v20, s[24:27], s64 offen offset:0 lds\n"
  "buffer_load_dwordx4 v21, s[24:27], s64 offen offset:1024 lds\n"
  "buffer_load_dwordx4 v22, s[24:27], s64 offen offset:2048 lds\n"
  "buffer_load_dwordx4 v23, s[24:27], s64 offen offset:3072 lds\n"
  "v_lshlrev_b32 v57, 4, v54\n"
  "v_mov_b32 v58, v57\n"
  "v_add_u32 v59, s60, v57\n"
  "v_add_u32 v60, 0x1000, v59\n"
  "s_lshl1_add_u32 s58, s56, 2\n"
  "v_subrev_u32 v55, s58, v54\n"
  "v_cmp_gt_i32 vcc, 0, v55\n"
  "s_lshl_b32 s57, s54, 20\n"
  "v_mov_b32 v56, 2130706432\n"
  "v_cndmask_b32 v55, v55, v54, vcc\n"
  "v_cmp_gt_i32 vcc, 34, v54\n"
  "v_lshl_add_u32 v61, v55, 15, s57\n"
  "s_lshl_b32 s58, s20, 5\n"
  "v_cndmask_b32 v61, v56, v61, vcc\n"
  "v_cmp_eq_u32 vcc, 0, %1\n"
  "s_lshl_b32 s59, s54, 7\n"
  "v_mov_b32 v55, s59\n"
  "v_cndmask_b32 v6, v56, v55, vcc\n"
  "s_cmp_eq_u32 %2, 0\n"
  "s_cselect_b32 s66, 0, s54\n"
  "s_cselect_b32 s67, 32, 1\n"
  "v_add_u32 v55, s66, v54\n"
  "v_cmp_gt_u32 vcc, s67, v54\n"
  "v_lshlrev_b32 v55, 7, v55\n"
  "v_mov_b32 v7, 8\n"
  "v_cndmask_b32 v4, v56, v55, vcc\n"
  "v_cndmask_b32 v5, 0, v7, vcc\n"
  "v_mov_b32 v7, 1\n"
  "v_and_b32 v55, 15, v54\n"
  "v_add_u32 v55, s58, v55\n"
  "v_lshrrev_b32 v56, 4, v54\n"
  "v_lshlrev_b32 v63, 2, v56\n"
  "v_sub_u32 v55, v55, v63\n"
  "v_cvt_f32_i32 v55, v55\n"
  "v_add_f32 v55, 0.5, v55\n"
  "v_mul_f32 v63, 0x71800000, v55\n"
  "v_lshlrev_b32 v46, 4, v54\n"
  "s_mov_b32 s76, 3\n"
  "s_mov_b32 s83, 0\n"
  "s_add_u32 s78, s60, 0x18000\n"
  "s_mov_b32 s80, 0\n"
  "s_mov_b32 s82, 0\n"
  "s_mov_b32 s79, s56\n"
  "s_lshl3_add_u32 s68, s54, s73\n"
  "s_lshl_b32 s68, s68, 11\n"
  "s_lshl_b32 s81, s72, 19\n"
  "s_add_u32 s68, s68, s81\n"
  "s_add_u32 s68, s68, 33554432\n"
  "s_lshl_b32 s81, s56, 20\n"
  "s_add_u32 s81, s81, s68\n"
  "s_lshl2_add_u32 s74, s55, s20\n"
  "s_lshl_b32 s74, s74, 15\n"
  "s_add_u32 s74, s74, s57\n"
  "s_mov_b32 s69, 73400320\n"
  "s_add_u32 s75, s69, 0x1000\n"
  "v_add_u32 v2, 0x10000, v2\n"
  "v_add_u32 v3, 0x10000, v3\n"
  "v_add_u32 v8, 0x10000, v8\n"
  "v_add_u32 v9, 0x10000, v9\n"
  "v_add_u32 v10, 0x10000, v10\n"
  "v_add_u32 v11, 0x10000, v11\n"
  "v_add_u32 v12, 0x10000, v12\n"
  "v_add_u32 v13, 0x10000, v13\n"
  "v_add_u32 v14, 0x10000, v14\n"
  "v_add_u32 v15, 0x10000, v15\n"
  "s_waitcnt vmcnt(15)\n"
  "s_barrier\n"
  "ds_read_b64_tr_b16 a[48:49], v8 offset:32768\n"
  "ds_read_b64_tr_b16 a[50:51], v8 offset:33792\n"
  "ds_read_b128 a[32:35], v2 offset:0\n"
  "ds_read_b64_tr_b16 a[52:53], v9 offset:32768\n"
  "ds_read_b64_tr_b16 a[54:55], v9 offset:33792\n"
  "ds_read_b128 a[36:39], v2 offset:2048\n"
  "ds_read_b64_tr_b16 a[56:57], v10 offset:32768\n"
  "ds_read_b64_tr_b16 a[58:59], v10 offset:33792\n"
  "ds_read_b128 a[40:43], v2 offset:4096\n"
  "ds_read_b64_tr_b16 a[60:61], v11 offset:32768\n"
  "ds_read_b64_tr_b16 a[62:63], v11 offset:33792\n"
  "ds_read_b128 a[44:47], v2 offset:6144\n"
  "ds_read_b64_tr_b16 a[64:65], v12 offset:32768\n"
  "ds_read_b64_tr_b16 a[66:67], v12 offset:33792\n"
  "ds_read_b64_tr_b16 a[68:69], v13 offset:32768\n"
  "ds_read_b64_tr_b16 a[70:71], v13 offset:33792\n"
  "ds_read_b64_tr_b16 a[72:73], v14 offset:32768\n"
  "ds_read_b64_tr_b16 a[74:75], v14 offset:33792\n"
  "ds_read_b64_tr_b16 a[76:77], v15 offset:32768\n"
  "ds_read_b64_tr_b16 a[78:79], v15 offset:33792\n"
  "s_waitcnt lgkmcnt(0)\n"
  "v_mfma_f32_16x16x32_bf16 v[128:131], a[48:51], a[32:35], 0\n"
  "ds_read_b64_tr_b16 a[96:97], v8 offset:49152\n"
  "s_add_u32 m0, s62, 49152\n"
  "s_mov_b32 s64, 524288\n"
  "v_mfma_f32_16x16x32_bf16 v[160:163], a[48:51], a[36:39], 0\n"
  "ds_read_b64_tr_b16 a[98:99], v8 offset:50176\n"
  "buffer_load_dwordx4 v24, s[36:39], s64 offen offset:0 lds\n"
  "v_mfma_f32_16x16x32_bf16 v[192:195], a[48:51], a[40:43], 0\n"
  "v_mfma_f32_16x16x32_bf16 v[224:227], a[48:51], a[44:47], 0\n"
  "ds_read_b64_tr_b16 a[100:101], v9 offset:49152\n"
  "v_mfma_f32_16x16x32_bf16 v[132:135], a[52:55], a[32:35], 0\n"
  "ds_read_b64_tr_b16 a[102:103], v9 offset:50176\n"
  "v_mfma_f32_16x16x32_bf16 v[164:167], a[52:55], a[36:39], 0\n"
  "v_mfma_f32_16x16x32_bf16 v[196:199], a[52:55], a[40:43], 0\n"
  "ds_read_b64_tr_b16 a[104:105], v10 offset:49152\n"
  "v_mfma_f32_16x16x32_bf16 v[228:231], a[52:55], a[44:47], 0\n"
  "ds_read_b64_tr_b16 a[106:107], v10 offset:50176\n"
  "buffer_load_dwordx4 v25, s[36:39], s64 offen offset:1024 lds\n"
  "v_mfma_f32_16x16x32_bf16 v[136:139], a[56:59], a[32:35], 0\n"
  "v_mfma_f32_16x16x32_bf16 v[168:171], a[56:59], a[36:39], 0\n"
  "ds_read_b64_tr_b16 a[108:109], v11 offset:49152\n"
  "v_mfma_f32_16x16x32_bf16 v[200:203], a[56:59], a[40:43], 0\n"
  "ds_read_b64_tr_b16 a[110:111], v11 offset:50176\n"
  "v_mfma_f32_16x16x32_bf16 v[232:235], a[56:59], a[44:47], 0\n"
  "v_mfma_f32_16x16x32_bf16 v[140:143], a[60:63], a[32:35], 0\n"
  "ds_read_b64_tr_b16 a[112:113], v12 offset:49152\n"
  "v_mfma_f32_16x16x32_bf16 v[172:175], a[60:63], a[36:39], 0\n"
  "ds_read_b64_tr_b16 a[114:115], v12 offset:50176\n"
  "buffer_load_dwordx4 v26, s[36:39], s64 offen offset:2048 lds\n"
  "v_mfma_f32_16x16x32_bf16 v[204:207], a[60:63], a[40:43], 0\n"
  "v_mfma_f32_16x16x32_bf16 v[236:239], a[60:63], a[44:47], 0\n"
  "ds_read_b64_tr_b16 a[116:117], v13 offset:49152\n"
  "v_mfma_f32_16x16x32_bf16 v[144:147], a[64:67], a[32:35], 0\n"
  "ds_read_b64_tr_b16 a[118:119], v13 offset:50176\n"
  "v_mfma_f32_16x16x32_bf16 v[176:179], a[64:67], a[36:39], 0\n"
  "v_mfma_f32_16x16x32_bf16 v[208:211], a[64:67], a[40:43], 0\n"
  "ds_read_b64_tr_b16 a[120:121], v14 offset:49152\n"
  "v_mfma_f32_16x16x32_bf16 v[240:243], a[64:67], a[44:47], 0\n"
  "ds_read_b64_tr_b16 a[122:123], v14 offset:50176\n"
  "buffer_load_dwordx4 v27, s[36:39], s64 offen offset:3072 lds\n"
  "v_mfma_f32_16x16x32_bf16 v[148:151], a[68:71], a[32:35], 0\n"
  "v_mfma_f32_16x16x32_bf16 v[180:183], a[68:71], a[36:39], 0\n"
  "ds_read_b64_tr_b16 a[124:125], v15 offset:49152\n"
  "v_mfma_f32_16x16x32_bf16 v[212:215], a[68:71], a[40:43], 0\n"
  "ds_read_b64_tr_b16 a[126:127], v15 offset:50176\n"
  "v_mfma_f32_16x16x32_bf16 v[244:247], a[68:71], a[44:47], 0\n"
  "v_mfma_f32_16x16x32_bf16 v[152:155], a[72:75], a[32:35], 0\n"
  "v_mfma_f32_16x16x32_bf16 v[184:187], a[72:75], a[36:39], 0\n"
  "v_mfma_f32_16x16x32_bf16 v[216:219], a[72:75], a[40:43], 0\n"
  "v_mfma_f32_16x16x32_bf16 v[248:251], a[72:75], a[44:47], 0\n"
  "v_mfma_f32_16x16x32_bf16 v[156:159], a[76:79], a[32:35], 0\n"
  "v_mfma_f32_16x16x32_bf16 v[188:191], a[76:79], a[36:39], 0\n"
  "v_mfma_f32_16x16x32_bf16 v[220:223], a[76:79], a[40:43], 0\n"
  "v_mfma_f32_16x16x32_bf16 v[252:255], a[76:79], a[44:47], 0\n"
  "s_waitcnt lgkmcnt(0)\n"
  "v_mfma_f32_16x16x32_bf16 a[128:131], a[32:35], a[96:99], 0\n"
  "ds_read_b64_tr_b16 a[48:49], v8 offset:40960\n"
  "v_mfma_f32_16x16x32_bf16 a[132:135], a[36:39], a[96:99], 0\n"
  "ds_read_b64_tr_b16 a[50:51], v8 offset:41984\n"
  "v_mfma_f32_16x16x32_bf16 a[192:195], a[40:43], a[96:99], 0\n"
  "ds_read_b128 a[80:83], v3 offset:0\n"
  "v_mfma_f32_16x16x32_bf16 a[196:199], a[44:47], a[96:99], 0\n"
  "ds_read_b64_tr_b16 a[52:53], v9 offset:40960\n"
  "v_mfma_f32_16x16x32_bf16 a[136:139], a[32:35], a[100:103], 0\n"
  "ds_read_b64_tr_b16 a[54:55], v9 offset:41984\n"
  "v_mfma_f32_16x16x32_bf16 a[140:143], a[36:39], a[100:103], 0\n"
  "v_mfma_f32_16x16x32_bf16 a[200:203], a[40:43], a[100:103], 0\n"
  "ds_read_b128 a[84:87], v3 offset:2048\n"
  "v_mfma_f32_16x16x32_bf16 a[204:207], a[44:47], a[100:103], 0\n"
  "ds_read_b64_tr_b16 a[56:57], v10 offset:40960\n"
  "v_mfma_f32_16x16x32_bf16 a[144:147], a[32:35], a[104:107], 0\n"
  "ds_read_b64_tr_b16 a[58:59], v10 offset:41984\n"
  "v_mfma_f32_16x16x32_bf16 a[148:151], a[36:39], a[104:107], 0\n"
  "ds_read_b128 a[88:91], v3 offset:4096\n"
  "v_mfma_f32_16x16x32_bf16 a[208:211], a[40:43], a[104:107], 0\n"
  "ds_read_b64_tr_b16 a[60:61], v11 offset:40960\n"
  "v_mfma_f32_16x16x32_bf16 a[212:215], a[44:47], a[104:107], 0\n"
  "v_mfma_f32_16x16x32_bf16 a[152:155], a[32:35], a[108:111], 0\n"
  "ds_read_b64_tr_b16 a[62:63], v11 offset:41984\n"
  "v_mfma_f32_16x16x32_bf16 a[156:159], a[36:39], a[108:111], 0\n"
  "ds_read_b128 a[92:95], v3 offset:6144\n"
  "v_mfma_f32_16x16x32_bf16 a[216:219], a[40:43], a[108:111], 0\n"
  "ds_read_b64_tr_b16 a[64:65], v12 offset:40960\n"
  "v_mfma_f32_16x16x32_bf16 a[220:223], a[44:47], a[108:111], 0\n"
  "ds_read_b64_tr_b16 a[66:67], v12 offset:41984\n"
  "v_mfma_f32_16x16x32_bf16 a[160:163], a[32:35], a[112:115], 0\n"
  "ds_read_b64_tr_b16 a[68:69], v13 offset:40960\n"
  "v_mfma_f32_16x16x32_bf16 a[164:167], a[36:39], a[112:115], 0\n"
  "v_mfma_f32_16x16x32_bf16 a[224:227], a[40:43], a[112:115], 0\n"
  "ds_read_b64_tr_b16 a[70:71], v13 offset:41984\n"
  "v_mfma_f32_16x16x32_bf16 a[228:231], a[44:47], a[112:115], 0\n"
  "ds_read_b64_tr_b16 a[72:73], v14 offset:40960\n"
  "v_mfma_f32_16x16x32_bf16 a[168:171], a[32:35], a[116:119], 0\n"
  "ds_read_b64_tr_b16 a[74:75], v14 offset:41984\n"
  "s_waitcnt vmcnt(18)\n"
  "v_mfma_f32_16x16x32_bf16 a[172:175], a[36:39], a[116:119], 0\n"
  "ds_read_b64_tr_b16 a[76:77], v15 offset:40960\n"
  "v_mfma_f32_16x16x32_bf16 a[232:235], a[40:43], a[116:119], 0\n"
  "ds_read_b64_tr_b16 a[78:79], v15 offset:41984\n"
  "v_readfirstlane_b32 s21, v52\n"
  "v_mfma_f32_16x16x32_bf16 a[236:239], a[44:47], a[116:119], 0\n"
  "v_mfma_f32_16x16x32_bf16 a[176:179], a[32:35], a[120:123], 0\n"
  "v_nop\n"
  "v_mfma_f32_16x16x32_bf16 a[180:183], a[36:39], a[120:123], 0\n"
  "v_lshlrev_b32 v52, 4, v54\n"
  "v_mfma_f32_16x16x32_bf16 a[240:243], a[40:43], a[120:123], 0\n"
  "v_mfma_f32_16x16x32_bf16 a[244:247], a[44:47], a[120:123], 0\n"
  "v_add_u32 v53, 0x1000, v52\n"
  "v_mfma_f32_16x16x32_bf16 a[184:187], a[32:35], a[124:127], 0\n"
  "v_mfma_f32_16x16x32_bf16 a[188:191], a[36:39], a[124:127], 0\n"
  "v_mfma_f32_16x16x32_bf16 a[248:251], a[40:43], a[124:127], 0\n"
  "v_mfma_f32_16x16x32_bf16 a[252:255], a[44:47], a[124:127], 0\n"
  "s_waitcnt lgkmcnt(0)\n"
  "s_barrier\n"
  "v_mfma_f32_16x16x32_bf16 v[128:131], a[48:51], a[80:83], v[128:131]\n"
  "ds_read_b64_tr_b16 a[96:97], v8 offset:57344\n"
  "s_add_u32 m0, s60, 65536\n"
  "s_mov_b32 s64, 256\n"
  "v_mfma_f32_16x16x32_bf16 v[160:163], a[48:51], a[84:87], v[160:163]\n"
  "ds_read_b64_tr_b16 a[98:99], v8 offset:58368\n"
  "buffer_load_dwordx4 v16, s[24:27], s64 offen offset:0 lds\n"
  "v_mfma_f32_16x16x32_bf16 v[192:195], a[48:51], a[88:91], v[192:195]\n"
  "v_mfma_f32_16x16x32_bf16 v[224:227], a[48:51], a[92:95], v[224:227]\n"
  "ds_read_b64_tr_b16 a[100:101], v9 offset:57344\n"
  "v_mfma_f32_16x16x32_bf16 v[132:135], a[52:55], a[80:83], v[132:135]\n"
  "ds_read_b64_tr_b16 a[102:103], v9 offset:58368\n"
  "v_mfma_f32_16x16x32_bf16 v[164:167], a[52:55], a[84:87], v[164:167]\n"
  "v_mfma_f32_16x16x32_bf16 v[196:199], a[52:55], a[88:91], v[196:199]\n"
  "ds_read_b64_tr_b16 a[104:105], v10 offset:57344\n"
  "v_mfma_f32_16x16x32_bf16 v[228:231], a[52:55], a[92:95], v[228:231]\n"
  "ds_read_b64_tr_b16 a[106:107], v10 offset:58368\n"
  "buffer_load_dwordx4 v17, s[24:27], s64 offen offset:1024 lds\n"
  "v_mfma_f32_16x16x32_bf16 v[136:139], a[56:59], a[80:83], v[136:139]\n"
  "v_mfma_f32_16x16x32_bf16 v[168:171], a[56:59], a[84:87], v[168:171]\n"
  "ds_read_b64_tr_b16 a[108:109], v11 offset:57344\n"
  "v_mfma_f32_16x16x32_bf16 v[200:203], a[56:59], a[88:91], v[200:203]\n"
  "ds_read_b64_tr_b16 a[110:111], v11 offset:58368\n"
  "v_mfma_f32_16x16x32_bf16 v[232:235], a[56:59], a[92:95], v[232:235]\n"
  "v_mfma_f32_16x16x32_bf16 v[140:143], a[60:63], a[80:83], v[140:143]\n"
  "ds_read_b64_tr_b16 a[112:113], v12 offset:57344\n"
  "v_mfma_f32_16x16x32_bf16 v[172:175], a[60:63], a[84:87], v[172:175]\n"
  "ds_read_b64_tr_b16 a[114:115], v12 offset:58368\n"
  "buffer_load_dwordx4 v18, s[24:27], s64 offen offset:2048 lds\n"
  "v_mfma_f32_16x16x32_bf16 v[204:207], a[60:63], a[88:91], v[204:207]\n"
  "v_mfma_f32_16x16x32_bf16 v[236:239], a[60:63], a[92:95], v[236:239]\n"
  "ds_read_b64_tr_b16 a[116:117], v13 offset:57344\n"
  "v_mfma_f32_16x16x32_bf16 v[144:147], a[64:67], a[80:83], v[144:147]\n"
  "ds_read_b64_tr_b16 a[118:119], v13 offset:58368\n"
  "v_mfma_f32_16x16x32_bf16 v[176:179], a[64:67], a[84:87], v[176:179]\n"
  "v_mfma_f32_16x16x32_bf16 v[208:211], a[64:67], a[88:91], v[208:211]\n"
  "ds_read_b64_tr_b16 a[120:121], v14 offset:57344\n"
  "v_mfma_f32_16x16x32_bf16 v[240:243], a[64:67], a[92:95], v[240:243]\n"
  "ds_read_b64_tr_b16 a[122:123], v14 offset:58368\n"
  "buffer_load_dwordx4 v19, s[24:27], s64 offen offset:3072 lds\n"
  "v_mfma_f32_16x16x32_bf16 v[148:151], a[68:71], a[80:83], v[148:151]\n"
  "s_add_u32 m0, s62, 98304\n"
  "s_mov_b32 s64, 1048576\n"
  "v_mfma_f32_16x16x32_bf16 v[180:183], a[68:71], a[84:87], v[180:183]\n"
  "ds_read_b64_tr_b16 a[124:125], v15 offset:57344\n"
  "v_mfma_f32_16x16x32_bf16 v[212:215], a[68:71], a[88:91], v[212:215]\n"
  "ds_read_b64_tr_b16 a[126:127], v15 offset:58368\n"
  "v_mfma_f32_16x16x32_bf16 v[244:247], a[68:71], a[92:95], v[244:247]\n"
  "v_mfma_f32_16x16x32_bf16 v[152:155], a[72:75], a[80:83], v[152:155]\n"
  "v_add_u32 v2, 0xffff0000, v2\n"
  "v_mfma_f32_16x16x32_bf16 v[184:187], a[72:75], a[84:87], v[184:187]\n"
  "buffer_load_dwordx4 v24, s[28:31], s64 offen offset:0 lds\n"
  "v_add_u32 v3, 0xffff0000, v3\n"
  "v_mfma_f32_16x16x32_bf16 v[216:219], a[72:75], a[88:91], v[216:219]\n"
  "v_add_u32 v8, 0xffff0000, v8\n"
  "v_mfma_f32_16x16x32_bf16 v[248:251], a[72:75], a[92:95], v[248:251]\n"
  "v_add_u32 v9, 0xffff0000, v9\n"
  "v_add_u32 v10, 0xffff0000, v10\n"
  "v_mfma_f32_16x16x32_bf16 v[156:159], a[76:79], a[80:83], v[156:159]\n"
  "v_add_u32 v11, 0xffff0000, v11\n"
  "v_mfma_f32_16x16x32_bf16 v[188:191], a[76:79], a[84:87], v[188:191]\n"
  "v_add_u32 v12, 0xffff0000, v12\n"
  "v_mfma_f32_16x16x32_bf16 v[220:223], a[76:79], a[88:91], v[220:223]\n"
  "v_add_u32 v13, 0xffff0000, v13\n"
  "v_mfma_f32_16x16x32_bf16 v[252:255], a[76:79], a[92:95], v[252:255]\n"
  "buffer_load_dwordx4 v25, s[28:31], s64 offen offset:1024 lds\n"
  "v_add_u32 v14, 0xffff0000, v14\n"
  "v_add_u32 v15, 0xffff0000, v15\n"
  "s_waitcnt vmcnt(6) lgkmcnt(0)\n"
  "s_barrier\n"
  "buffer_atomic_add v7, v6, s[92:95], s69 offen sc1\n"
  "v_mfma_f32_16x16x32_bf16 a[128:131], a[80:83], a[96:99], a[128:131]\n"
  "ds_read_b64_tr_b16 a[48:49], v8 offset:32768\n"
  "v_mfma_f32_16x16x32_bf16 a[132:135], a[84:87], a[96:99], a[132:135]\n"
  "ds_read_b64_tr_b16 a[50:51], v8 offset:33792\n"
  "v_mfma_f32_16x16x32_bf16 a[192:195], a[88:91], a[96:99], a[192:195]\n"
  "ds_read_b128 a[32:35], v2 offset:0\n"
  "v_mfma_f32_16x16x32_bf16 a[196:199], a[92:95], a[96:99], a[196:199]\n"
  "ds_read_b64_tr_b16 a[52:53], v9 offset:32768\n"
  "v_mfma_f32_16x16x32_bf16 a[136:139], a[80:83], a[100:103], a[136:139]\n"
  "ds_read_b64_tr_b16 a[54:55], v9 offset:33792\n"
  "v_mfma_f32_16x16x32_bf16 a[140:143], a[84:87], a[100:103], a[140:143]\n"
  "buffer_load_dwordx4 v26, s[28:31], s64 offen offset:2048 lds\n"
  "v_mfma_f32_16x16x32_bf16 a[200:203], a[88:91], a[100:103], a[200:203]\n"
  "ds_read_b128 a[36:39], v2 offset:2048\n"
  "v_mfma_f32_16x16x32_bf16 a[204:207], a[92:95], a[100:103], a[204:207]\n"
  "ds_read_b64_tr_b16 a[56:57], v10 offset:32768\n"
  "v_mfma_f32_16x16x32_bf16 a[144:147], a[80:83], a[104:107], a[144:147]\n"
  "ds_read_b64_tr_b16 a[58:59], v10 offset:33792\n"
  "v_mfma_f32_16x16x32_bf16 a[148:151], a[84:87], a[104:107], a[148:151]\n"
  "ds_read_b128 a[40:43], v2 offset:4096\n"
  "v_mfma_f32_16x16x32_bf16 a[208:211], a[88:91], a[104:107], a[208:211]\n"
  "ds_read_b64_tr_b16 a[60:61], v11 offset:32768\n"
  "v_mfma_f32_16x16x32_bf16 a[212:215], a[92:95], a[104:107], a[212:215]\n"
  "buffer_load_dwordx4 v27, s[28:31], s64 offen offset:3072 lds\n"
  "v_mfma_f32_16x16x32_bf16 a[152:155], a[80:83], a[108:111], a[152:155]\n"
  "ds_read_b64_tr_b16 a[62:63], v11 offset:33792\n"
  "s_add_u32 m0, s60, 69632\n"
  "s_mov_b32 s64, 256\n"
  "v_mfma_f32_16x16x32_bf16 a[156:159], a[84:87], a[108:111], a[156:159]\n"
  "ds_read_b128 a[44:47], v2 offset:6144\n"
  "v_mfma_f32_16x16x32_bf16 a[216:219], a[88:91], a[108:111], a[216:219]\n"
  "ds_read_b64_tr_b16 a[64:65], v12 offset:32768\n"
  "v_mfma_f32_16x16x32_bf16 a[220:223], a[92:95], a[108:111], a[220:223]\n"
  "ds_read_b64_tr_b16 a[66:67], v12 offset:33792\n"
  "v_mfma_f32_16x16x32_bf16 a[160:163], a[80:83], a[112:115], a[160:163]\n"
  "ds_read_b64_tr_b16 a[68:69], v13 offset:32768\n"
  "v_mfma_f32_16x16x32_bf16 a[164:167], a[84:87], a[112:115], a[164:167]\n"
  "buffer_load_dwordx4 v20, s[24:27], s64 offen offset:0 lds\n"
  "v_mfma_f32_16x16x32_bf16 a[224:227], a[88:91], a[112:115], a[224:227]\n"
  "ds_read_b64_tr_b16 a[70:71], v13 offset:33792\n"
  "v_mfma_f32_16x16x32_bf16 a[228:231], a[92:95], a[112:115], a[228:231]\n"
  "ds_read_b64_tr_b16 a[72:73], v14 offset:32768\n"
  "v_mfma_f32_16x16x32_bf16 a[168:171], a[80:83], a[116:119], a[168:171]\n"
  "ds_read_b64_tr_b16 a[74:75], v14 offset:33792\n"
  "v_mfma_f32_16x16x32_bf16 a[172:175], a[84:87], a[116:119], a[172:175]\n"
  "ds_read_b64_tr_b16 a[76:77], v15 offset:32768\n"
  "v_mfma_f32_16x16x32_bf16 a[232:235], a[88:91], a[116:119], a[232:235]\n"
  "ds_read_b64_tr_b16 a[78:79], v15 offset:33792\n"
  "v_mfma_f32_16x16x32_bf16 a[236:239], a[92:95], a[116:119], a[236:239]\n"
  "buffer_load_dwordx4 v21, s[24:27], s64 offen offset:1024 lds\n"
  "v_mfma_f32_16x16x32_bf16 a[176:179], a[80:83], a[120:123], a[176:179]\n"
  "v_mfma_f32_16x16x32_bf16 a[180:183], a[84:87], a[120:123], a[180:183]\n"
  "v_mfma_f32_16x16x32_bf16 a[240:243], a[88:91], a[120:123], a[240:243]\n"
  "v_mfma_f32_16x16x32_bf16 a[244:247], a[92:95], a[120:123], a[244:247]\n"
  "v_mfma_f32_16x16x32_bf16 a[184:187], a[80:83], a[124:127], a[184:187]\n"
  "v_mfma_f32_16x16x32_bf16 a[188:191], a[84:87], a[124:127], a[188:191]\n"
  "buffer_load_dwordx4 v22, s[24:27], s64 offen offset:2048 lds\n"
  "v_mfma_f32_16x16x32_bf16 a[248:251], a[88:91], a[124:127], a[248:251]\n"
  "v_mfma_f32_16x16x32_bf16 a[252:255], a[92:95], a[124:127], a[252:255]\n"
  "s_waitcnt lgkmcnt(0)\n"
  "v_mfma_f32_16x16x32_bf16 v[128:131], a[48:51], a[32:35], v[128:131]\n"
  "ds_read_b64_tr_b16 a[96:97], v8 offset:49152\n"
  "v_mfma_f32_16x16x32_bf16 v[160:163], a[48:51], a[36:39], v[160:163]\n"
  "ds_read_b64_tr_b16 a[98:99], v8 offset:50176\n"
  "v_mfma_f32_16x16x32_bf16 v[192:195], a[48:51], a[40:43], v[192:195]\n"
  "v_mfma_f32_16x16x32_bf16 v[224:227], a[48:51], a[44:47], v[224:227]\n"
  "ds_read_b64_tr_b16 a[100:101], v9 offset:49152\n"
  "buffer_load_dwordx4 v23, s[24:27], s64 offen offset:3072 lds\n"
  "v_mfma_f32_16x16x32_bf16 v[132:135], a[52:55], a[32:35], v[132:135]\n"
  "ds_read_b64_tr_b16 a[102:103], v9 offset:50176\n"
  "s_add_u32 m0, s62, 114688\n"
  "s_mov_b32 s64, 1048576\n"
  "v_mfma_f32_16x16x32_bf16 v[164:167], a[52:55], a[36:39], v[164:167]\n"
  "v_mfma_f32_16x16x32_bf16 v[196:199], a[52:55], a[40:43], v[196:199]\n"
  "ds_read_b64_tr_b16 a[104:105], v10 offset:49152\n"
  "v_mfma_f32_16x16x32_bf16 v[228:231], a[52:55], a[44:47], v[228:231]\n"
  "ds_read_b64_tr_b16 a[106:107], v10 offset:50176\n"
  "v_mfma_f32_16x16x32_bf16 v[136:139], a[56:59], a[32:35], v[136:139]\n"
  "v_mfma_f32_16x16x32_bf16 v[168:171], a[56:59], a[36:39], v[168:171]\n"
  "ds_read_b64_tr_b16 a[108:109], v11 offset:49152\n"
  "buffer_load_dwordx4 v24, s[36:39], s64 offen offset:0 lds\n"
  "v_mfma_f32_16x16x32_bf16 v[200:203], a[56:59], a[40:43], v[200:203]\n"
  "ds_read_b64_tr_b16 a[110:111], v11 offset:50176\n"
  "v_mfma_f32_16x16x32_bf16 v[232:235], a[56:59], a[44:47], v[232:235]\n"
  "v_mfma_f32_16x16x32_bf16 v[140:143], a[60:63], a[32:35], v[140:143]\n"
  "ds_read_b64_tr_b16 a[112:113], v12 offset:49152\n"
  "v_mfma_f32_16x16x32_bf16 v[172:175], a[60:63], a[36:39], v[172:175]\n"
  "ds_read_b64_tr_b16 a[114:115], v12 offset:50176\n"
  "v_mfma_f32_16x16x32_bf16 v[204:207], a[60:63], a[40:43], v[204:207]\n"
  "v_mfma_f32_16x16x32_bf16 v[236:239], a[60:63], a[44:47], v[236:239]\n"
  "ds_read_b64_tr_b16 a[116:117], v13 offset:49152\n"
  "buffer_load_dwordx4 v25, s[36:39], s64 offen offset:1024 lds\n"
  "v_mfma_f32_16x16x32_bf16 v[144:147], a[64:67], a[32:35], v[144:147]\n"
  "ds_read_b64_tr_b16 a[118:119], v13 offset:50176\n"
  "v_mfma_f32_16x16x32_bf16 v[176:179], a[64:67], a[36:39], v[176:179]\n"
  "v_mfma_f32_16x16x32_bf16 v[208:211], a[64:67], a[40:43], v[208:211]\n"
  "ds_read_b64_tr_b16 a[120:121], v14 offset:49152\n"
  "v_mfma_f32_16x16x32_bf16 v[240:243], a[64:67], a[44:47], v[240:243]\n"
  "ds_read_b64_tr_b16 a[122:123], v14 offset:50176\n"
  "v_mfma_f32_16x16x32_bf16 v[148:151], a[68:71], a[32:35], v[148:151]\n"
  "v_mfma_f32_16x16x32_bf16 v[180:183], a[68:71], a[36:39], v[180:183]\n"
  "ds_read_b64_tr_b16 a[124:125], v15 offset:49152\n"
  "buffer_load_dwordx4 v26, s[36:39], s64 offen offset:2048 lds\n"
  "v_mfma_f32_16x16x32_bf16 v[212:215], a[68:71], a[40:43], v[212:215]\n"
  "ds_read_b64_tr_b16 a[126:127], v15 offset:50176\n"
  "v_mfma_f32_16x16x32_bf16 v[244:247], a[68:71], a[44:47], v[244:247]\n"
  "v_mfma_f32_16x16x32_bf16 v[152:155], a[72:75], a[32:35], v[152:155]\n"
  "v_mfma_f32_16x16x32_bf16 v[184:187], a[72:75], a[36:39], v[184:187]\n"
  "v_mfma_f32_16x16x32_bf16 v[216:219], a[72:75], a[40:43], v[216:219]\n"
  "v_mfma_f32_16x16x32_bf16 v[248:251], a[72:75], a[44:47], v[248:251]\n"
  "buffer_load_dwordx4 v27, s[36:39], s64 offen offset:3072 lds\n"
  "v_mfma_f32_16x16x32_bf16 v[156:159], a[76:79], a[32:35], v[156:159]\n"
  "v_mfma_f32_16x16x32_bf16 v[188:191], a[76:79], a[36:39], v[188:191]\n"
  "v_mfma_f32_16x16x32_bf16 v[220:223], a[76:79], a[40:43], v[220:223]\n"
  "v_mfma_f32_16x16x32_bf16 v[252:255], a[76:79], a[44:47], v[252:255]\n"
  "s_waitcnt lgkmcnt(0)\n"
  "v_mfma_f32_16x16x32_bf16 a[128:131], a[32:35], a[96:99], a[128:131]\n"
  "ds_read_b64_tr_b16 a[48:49], v8 offset:40960\n"
  "v_mfma_f32_16x16x32_bf16 a[132:135], a[36:39], a[96:99], a[132:135]\n"
  "ds_read_b64_tr_b16 a[50:51], v8 offset:41984\n"
  "v_mfma_f32_16x16x32_bf16 a[192:195], a[40:43], a[96:99], a[192:195]\n"
  "ds_read_b128 a[80:83], v3 offset:0\n"
  "v_mfma_f32_16x16x32_bf16 a[196:199], a[44:47], a[96:99], a[196:199]\n"
  "ds_read_b64_tr_b16 a[52:53], v9 offset:40960\n"
  "v_mfma_f32_16x16x32_bf16 a[136:139], a[32:35], a[100:103], a[136:139]\n"
  "ds_read_b64_tr_b16 a[54:55], v9 offset:41984\n"
  "v_mfma_f32_16x16x32_bf16 a[140:143], a[36:39], a[100:103], a[140:143]\n"
  "v_mfma_f32_16x16x32_bf16 a[200:203], a[40:43], a[100:103], a[200:203]\n"
  "ds_read_b128 a[84:87], v3 offset:2048\n"
  "v_mfma_f32_16x16x32_bf16 a[204:207], a[44:47], a[100:103], a[204:207]\n"
  "ds_read_b64_tr_b16 a[56:57], v10 offset:40960\n"
  "v_mfma_f32_16x16x32_bf16 a[144:147], a[32:35], a[104:107], a[144:147]\n"
  "ds_read_b64_tr_b16 a[58:59], v10 offset:41984\n"
  "v_mfma_f32_16x16x32_bf16 a[148:151], a[36:39], a[104:107], a[148:151]\n"
  "ds_read_b128 a[88:91], v3 offset:4096\n"
  "v_mfma_f32_16x16x32_bf16 a[208:211], a[40:43], a[104:107], a[208:211]\n"
  "ds_read_b64_tr_b16 a[60:61], v11 offset:40960\n"
  "v_mfma_f32_16x16x32_bf16 a[212:215], a[44:47], a[104:107], a[212:215]\n"
  "v_mfma_f32_16x16x32_bf16 a[152:155], a[32:35], a[108:111], a[152:155]\n"
  "ds_read_b64_tr_b16 a[62:63], v11 offset:41984\n"
  "v_mfma_f32_16x16x32_bf16 a[156:159], a[36:39], a[108:111], a[156:159]\n"
  "ds_read_b128 a[92:95], v3 offset:6144\n"
  "v_mfma_f32_16x16x32_bf16 a[216:219], a[40:43], a[108:111], a[216:219]\n"
  "ds_read_b64_tr_b16 a[64:65], v12 offset:40960\n"
  "v_mfma_f32_16x16x32_bf16 a[220:223], a[44:47], a[108:111], a[220:223]\n"
  "ds_read_b64_tr_b16 a[66:67], v12 offset:41984\n"
  "v_mfma_f32_16x16x32_bf16 a[160:163], a[32:35], a[112:115], a[160:163]\n"
  "ds_read_b64_tr_b16 a[68:69], v13 offset:40960\n"
  "v_mfma_f32_16x16x32_bf16 a[164:167], a[36:39], a[112:115], a[164:167]\n"
  "v_mfma_f32_16x16x32_bf16 a[224:227], a[40:43], a[112:115], a[224:227]\n"
  "ds_read_b64_tr_b16 a[70:71], v13 offset:41984\n"
  "v_mfma_f32_16x16x32_bf16 a[228:231], a[44:47], a[112:115], a[228:231]\n"
  "ds_read_b64_tr_b16 a[72:73], v14 offset:40960\n"
  "v_mfma_f32_16x16x32_bf16 a[168:171], a[32:35], a[116:119], a[168:171]\n"
  "ds_read_b64_tr_b16 a[74:75], v14 offset:41984\n"
  "v_mfma_f32_16x16x32_bf16 a[172:175], a[36:39], a[116:119], a[172:175]\n"
  "ds_read_b64_tr_b16 a[76:77], v15 offset:40960\n"
  "v_mfma_f32_16x16x32_bf16 a[232:235], a[40:43], a[116:119], a[232:235]\n"
  "ds_read_b64_tr_b16 a[78:79], v15 offset:41984\n"
  "v_mfma_f32_16x16x32_bf16 a[236:239], a[44:47], a[116:119], a[236:239]\n"
  "v_mfma_f32_16x16x32_bf16 a[176:179], a[32:35], a[120:123], a[176:179]\n"
  "v_mfma_f32_16x16x32_bf16 a[180:183], a[36:39], a[120:123], a[180:183]\n"
  "v_mfma_f32_16x16x32_bf16 a[240:243], a[40:43], a[120:123], a[240:243]\n"
  "v_mfma_f32_16x16x32_bf16 a[244:247], a[44:47], a[120:123], a[244:247]\n"
  "v_mfma_f32_16x16x32_bf16 a[184:187], a[32:35], a[124:127], a[184:187]\n"
  "v_mfma_f32_16x16x32_bf16 a[188:191], a[36:39], a[124:127], a[188:191]\n"
  "v_mfma_f32_16x16x32_bf16 a[248:251], a[40:43], a[124:127], a[248:251]\n"
  "v_mfma_f32_16x16x32_bf16 a[252:255], a[44:47], a[124:127], a[252:255]\n"
  "s_waitcnt lgkmcnt(0)\n"
  "s_barrier\n"
  "v_mfma_f32_16x16x32_bf16 v[128:131], a[48:51], a[80:83], v[128:131]\n"
  "ds_read_b64_tr_b16 a[96:97], v8 offset:57344\n"
  "s_add_u32 m0, s60, 0\n"
  "s_mov_b32 s64, 384\n"
  "v_mfma_f32_16x16x32_bf16 v[160:163], a[48:51], a[84:87], v[160:163]\n"
  "ds_read_b64_tr_b16 a[98:99], v8 offset:58368\n"
  "buffer_load_dwordx4 v16, s[24:27], s64 offen offset:0 lds\n"
  "v_mfma_f32_16x16x32_bf16 v[192:195], a[48:51], a[88:91], v[192:195]\n"
  "v_mfma_f32_16x16x32_bf16 v[224:227], a[48:51], a[92:95], v[224:227]\n"
  "ds_read_b64_tr_b16 a[100:101], v9 offset:57344\n"
  "v_mfma_f32_16x16x32_bf16 v[132:135], a[52:55], a[80:83], v[132:135]\n"
  "ds_read_b64_tr_b16 a[102:103], v9 offset:58368\n"
  "v_mfma_f32_16x16x32_bf16 v[164:167], a[52:55], a[84:87], v[164:167]\n"
  "v_mfma_f32_16x16x32_bf16 v[196:199], a[52:55], a[88:91], v[196:199]\n"
  "ds_read_b64_tr_b16 a[104:105], v10 offset:57344\n"
  "v_mfma_f32_16x16x32_bf16 v[228:231], a[52:55], a[92:95], v[228:231]\n"
  "ds_read_b64_tr_b16 a[106:107], v10 offset:58368\n"
  "buffer_load_dwordx4 v17, s[24:27], s64 offen offset:1024 lds\n"
  "v_mfma_f32_16x16x32_bf16 v[136:139], a[56:59], a[80:83], v[136:139]\n"
  "v_mfma_f32_16x16x32_bf16 v[168:171], a[56:59], a[84:87], v[168:171]\n"
  "ds_read_b64_tr_b16 a[108:109], v11 offset:57344\n"
  "v_mfma_f32_16x16x32_bf16 v[200:203], a[56:59], a[88:91], v[200:203]\n"
  "ds_read_b64_tr_b16 a[110:111], v11 offset:58368\n"
  "v_mfma_f32_16x16x32_bf16 v[232:235], a[56:59], a[92:95], v[232:235]\n"
  "v_mfma_f32_16x16x32_bf16 v[140:143], a[60:63], a[80:83], v[140:143]\n"
  "ds_read_b64_tr_b16 a[112:113], v12 offset:57344\n"
  "v_mfma_f32_16x16x32_bf16 v[172:175], a[60:63], a[84:87], v[172:175]\n"
  "ds_read_b64_tr_b16 a[114:115], v12 offset:58368\n"
  "buffer_load_dwordx4 v18, s[24:27], s64 offen offset:2048 lds\n"
  "v_mfma_f32_16x16x32_bf16 v[204:207], a[60:63], a[88:91], v[204:207]\n"
  "v_mfma_f32_16x16x32_bf16 v[236:239], a[60:63], a[92:95], v[236:239]\n"
  "ds_read_b64_tr_b16 a[116:117], v13 offset:57344\n"
  "v_mfma_f32_16x16x32_bf16 v[144:147], a[64:67], a[80:83], v[144:147]\n"
  "ds_read_b64_tr_b16 a[118:119], v13 offset:58368\n"
  "v_mfma_f32_16x16x32_bf16 v[176:179], a[64:67], a[84:87], v[176:179]\n"
  "v_mfma_f32_16x16x32_bf16 v[208:211], a[64:67], a[88:91], v[208:211]\n"
  "ds_read_b64_tr_b16 a[120:121], v14 offset:57344\n"
  "v_mfma_f32_16x16x32_bf16 v[240:243], a[64:67], a[92:95], v[240:243]\n"
  "ds_read_b64_tr_b16 a[122:123], v14 offset:58368\n"
  "buffer_load_dwordx4 v19, s[24:27], s64 offen offset:3072 lds\n"
  "v_mfma_f32_16x16x32_bf16 v[148:151], a[68:71], a[80:83], v[148:151]\n"
  "s_add_u32 m0, s62, 32768\n"
  "s_mov_b32 s64, 1572864\n"
  "v_mfma_f32_16x16x32_bf16 v[180:183], a[68:71], a[84:87], v[180:183]\n"
  "ds_read_b64_tr_b16 a[124:125], v15 offset:57344\n"
  "v_mfma_f32_16x16x32_bf16 v[212:215], a[68:71], a[88:91], v[212:215]\n"
  "ds_read_b64_tr_b16 a[126:127], v15 offset:58368\n"
  "v_mfma_f32_16x16x32_bf16 v[244:247], a[68:71], a[92:95], v[244:247]\n"
  "v_mfma_f32_16x16x32_bf16 v[152:155], a[72:75], a[80:83], v[152:155]\n"
  "v_add_u32 v2, 0x10000, v2\n"
  "v_mfma_f32_16x16x32_bf16 v[184:187], a[72:75], a[84:87], v[184:187]\n"
  "buffer_load_dwordx4 v24, s[28:31], s64 offen offset:0 lds\n"
  "v_add_u32 v3, 0x10000, v3\n"
  "v_mfma_f32_16x16x32_bf16 v[216:219], a[72:75], a[88:91], v[216:219]\n"
  "v_add_u32 v8, 0x10000, v8\n"
  "v_mfma_f32_16x16x32_bf16 v[248:251], a[72:75], a[92:95], v[248:251]\n"
  "v_add_u32 v9, 0x10000, v9\n"
  "v_add_u32 v10, 0x10000, v10\n"
  "v_mfma_f32_16x16x32_bf16 v[156:159], a[76:79], a[80:83], v[156:159]\n"
  "v_add_u32 v11, 0x10000, v11\n"
  "v_mfma_f32_16x16x32_bf16 v[188:191], a[76:79], a[84:87], v[188:191]\n"
  "v_add_u32 v12, 0x10000, v12\n"
  "v_mfma_f32_16x16x32_bf16 v[220:223], a[76:79], a[88:91], v[220:223]\n"
  "v_add_u32 v13, 0x10000, v13\n"
  "v_mfma_f32_16x16x32_bf16 v[252:255], a[76:79], a[92:95], v[252:255]\n"
  "buffer_load_dwordx4 v25, s[28:31], s64 offen offset:1024 lds\n"
  "v_add_u32 v14, 0x10000, v14\n"
  "v_add_u32 v15, 0x10000, v15\n"
  "s_waitcnt vmcnt(6) lgkmcnt(0)\n"
  "s_barrier\n"
  "v_mfma_f32_16x16x32_bf16 a[128:131], a[80:83], a[96:99], a[128:131]\n"
  "ds_read_b64_tr_b16 a[48:49], v8 offset:32768\n"
  "v_mfma_f32_16x16x32_bf16 a[132:135], a[84:87], a[96:99], a[132:135]\n"
  "ds_read_b64_tr_b16 a[50:51], v8 offset:33792\n"
  "v_mfma_f32_16x16x32_bf16 a[192:195], a[88:91], a[96:99], a[192:195]\n"
  "ds_read_b128 a[32:35], v2 offset:0\n"
  "v_mfma_f32_16x16x32_bf16 a[196:199], a[92:95], a[96:99], a[196:199]\n"
  "ds_read_b64_tr_b16 a[52:53], v9 offset:32768\n"
  "v_mfma_f32_16x16x32_bf16 a[136:139], a[80:83], a[100:103], a[136:139]\n"
  "ds_read_b64_tr_b16 a[54:55], v9 offset:33792\n"
  "v_mfma_f32_16x16x32_bf16 a[140:143], a[84:87], a[100:103], a[140:143]\n"
  "buffer_load_dwordx4 v26, s[28:31], s64 offen offset:2048 lds\n"
  "v_mfma_f32_16x16x32_bf16 a[200:203], a[88:91], a[100:103], a[200:203]\n"
  "ds_read_b128 a[36:39], v2 offset:2048\n"
  "v_mfma_f32_16x16x32_bf16 a[204:207], a[92:95], a[100:103], a[204:207]\n"
  "ds_read_b64_tr_b16 a[56:57], v10 offset:32768\n"
  "v_mfma_f32_16x16x32_bf16 a[144:147], a[80:83], a[104:107], a[144:147]\n"
  "ds_read_b64_tr_b16 a[58:59], v10 offset:33792\n"
  "v_mfma_f32_16x16x32_bf16 a[148:151], a[84:87], a[104:107], a[148:151]\n"
  "ds_read_b128 a[40:43], v2 offset:4096\n"
  "v_mfma_f32_16x16x32_bf16 a[208:211], a[88:91], a[104:107], a[208:211]\n"
  "ds_read_b64_tr_b16 a[60:61], v11 offset:32768\n"
  "v_mfma_f32_16x16x32_bf16 a[212:215], a[92:95], a[104:107], a[212:215]\n"
  "buffer_load_dwordx4 v27, s[28:31], s64 offen offset:3072 lds\n"
  "v_mfma_f32_16x16x32_bf16 a[152:155], a[80:83], a[108:111], a[152:155]\n"
  "ds_read_b64_tr_b16 a[62:63], v11 offset:33792\n"
  "s_add_u32 m0, s60, 4096\n"
  "s_mov_b32 s64, 384\n"
  "v_mfma_f32_16x16x32_bf16 a[156:159], a[84:87], a[108:111], a[156:159]\n"
  "ds_read_b128 a[44:47], v2 offset:6144\n"
  "v_mfma_f32_16x16x32_bf16 a[216:219], a[88:91], a[108:111], a[216:219]\n"
  "ds_read_b64_tr_b16 a[64:65], v12 offset:32768\n"
  "v_mfma_f32_16x16x32_bf16 a[220:223], a[92:95], a[108:111], a[220:223]\n"
  "ds_read_b64_tr_b16 a[66:67], v12 offset:33792\n"
  "v_mfma_f32_16x16x32_bf16 a[160:163], a[80:83], a[112:115], a[160:163]\n"
  "ds_read_b64_tr_b16 a[68:69], v13 offset:32768\n"
  "v_mfma_f32_16x16x32_bf16 a[164:167], a[84:87], a[112:115], a[164:167]\n"
  "buffer_load_dwordx4 v20, s[24:27], s64 offen offset:0 lds\n"
  "v_mfma_f32_16x16x32_bf16 a[224:227], a[88:91], a[112:115], a[224:227]\n"
  "ds_read_b64_tr_b16 a[70:71], v13 offset:33792\n"
  "v_mfma_f32_16x16x32_bf16 a[228:231], a[92:95], a[112:115], a[228:231]\n"
  "ds_read_b64_tr_b16 a[72:73], v14 offset:32768\n"
  "v_mfma_f32_16x16x32_bf16 a[168:171], a[80:83], a[116:119], a[168:171]\n"
  "ds_read_b64_tr_b16 a[74:75], v14 offset:33792\n"
  "v_mfma_f32_16x16x32_bf16 a[172:175], a[84:87], a[116:119], a[172:175]\n"
  "ds_read_b64_tr_b16 a[76:77], v15 offset:32768\n"
  "v_mfma_f32_16x16x32_bf16 a[232:235], a[88:91], a[116:119], a[232:235]\n"
  "ds_read_b64_tr_b16 a[78:79], v15 offset:33792\n"
  "v_mfma_f32_16x16x32_bf16 a[236:239], a[92:95], a[116:119], a[236:239]\n"
  "buffer_load_dwordx4 v21, s[24:27], s64 offen offset:1024 lds\n"
  "v_mfma_f32_16x16x32_bf16 a[176:179], a[80:83], a[120:123], a[176:179]\n"
  "v_mfma_f32_16x16x32_bf16 a[180:183], a[84:87], a[120:123], a[180:183]\n"
  "v_mfma_f32_16x16x32_bf16 a[240:243], a[88:91], a[120:123], a[240:243]\n"
  "v_mfma_f32_16x16x32_bf16 a[244:247], a[92:95], a[120:123], a[244:247]\n"
  "v_mfma_f32_16x16x32_bf16 a[184:187], a[80:83], a[124:127], a[184:187]\n"
  "v_mfma_f32_16x16x32_bf16 a[188:191], a[84:87], a[124:127], a[188:191]\n"
  "buffer_load_dwordx4 v22, s[24:27], s64 offen offset:2048 lds\n"
  "v_mfma_f32_16x16x32_bf16 a[248:251], a[88:91], a[124:127], a[248:251]\n"
  "v_mfma_f32_16x16x32_bf16 a[252:255], a[92:95], a[124:127], a[252:255]\n"
  "s_waitcnt lgkmcnt(0)\n"
  "v_mfma_f32_16x16x32_bf16 v[128:131], a[48:51], a[32:35], v[128:131]\n"
  "ds_read_b64_tr_b16 a[96:97], v8 offset:49152\n"
  "v_mfma_f32_16x16x32_bf16 v[160:163], a[48:51], a[36:39], v[160:163]\n"
  "ds_read_b64_tr_b16 a[98:99], v8 offset:50176\n"
  "v_mfma_f32_16x16x32_bf16 v[192:195], a[48:51], a[40:43], v[192:195]\n"
  "v_mfma_f32_16x16x32_bf16 v[224:227], a[48:51], a[44:47], v[224:227]\n"
  "ds_read_b64_tr_b16 a[100:101], v9 offset:49152\n"
  "buffer_load_dwordx4 v23, s[24:27], s64 offen offset:3072 lds\n"
  "v_mfma_f32_16x16x32_bf16 v[132:135], a[52:55], a[32:35], v[132:135]\n"
  "ds_read_b64_tr_b16 a[102:103], v9 offset:50176\n"
  "s_add_u32 m0, s62, 49152\n"
  "s_mov_b32 s64, 1572864\n"
  "v_mfma_f32_16x16x32_bf16 v[164:167], a[52:55], a[36:39], v[164:167]\n"
  "v_mfma_f32_16x16x32_bf16 v[196:199], a[52:55], a[40:43], v[196:199]\n"
  "ds_read_b64_tr_b16 a[104:105], v10 offset:49152\n"
  "v_mfma_f32_16x16x32_bf16 v[228:231], a[52:55], a[44:47], v[228:231]\n"
  "ds_read_b64_tr_b16 a[106:107], v10 offset:50176\n"
  "v_mfma_f32_16x16x32_bf16 v[136:139], a[56:59], a[32:35], v[136:139]\n"
  "v_mfma_f32_16x16x32_bf16 v[168:171], a[56:59], a[36:39], v[168:171]\n"
  "ds_read_b64_tr_b16 a[108:109], v11 offset:49152\n"
  "buffer_load_dwordx4 v24, s[36:39], s64 offen offset:0 lds\n"
  "v_mfma_f32_16x16x32_bf16 v[200:203], a[56:59], a[40:43], v[200:203]\n"
  "ds_read_b64_tr_b16 a[110:111], v11 offset:50176\n"
  "v_mfma_f32_16x16x32_bf16 v[232:235], a[56:59], a[44:47], v[232:235]\n"
  "v_mfma_f32_16x16x32_bf16 v[140:143], a[60:63], a[32:35], v[140:143]\n"
  "ds_read_b64_tr_b16 a[112:113], v12 offset:49152\n"
  "v_mfma_f32_16x16x32_bf16 v[172:175], a[60:63], a[36:39], v[172:175]\n"
  "ds_read_b64_tr_b16 a[114:115], v12 offset:50176\n"
  "v_mfma_f32_16x16x32_bf16 v[204:207], a[60:63], a[40:43], v[204:207]\n"
  "v_mfma_f32_16x16x32_bf16 v[236:239], a[60:63], a[44:47], v[236:239]\n"
  "ds_read_b64_tr_b16 a[116:117], v13 offset:49152\n"
  "buffer_load_dwordx4 v25, s[36:39], s64 offen offset:1024 lds\n"
  "v_mfma_f32_16x16x32_bf16 v[144:147], a[64:67], a[32:35], v[144:147]\n"
  "ds_read_b64_tr_b16 a[118:119], v13 offset:50176\n"
  "v_mfma_f32_16x16x32_bf16 v[176:179], a[64:67], a[36:39], v[176:179]\n"
  "v_mfma_f32_16x16x32_bf16 v[208:211], a[64:67], a[40:43], v[208:211]\n"
  "ds_read_b64_tr_b16 a[120:121], v14 offset:49152\n"
  "v_mfma_f32_16x16x32_bf16 v[240:243], a[64:67], a[44:47], v[240:243]\n"
  "ds_read_b64_tr_b16 a[122:123], v14 offset:50176\n"
  "v_mfma_f32_16x16x32_bf16 v[148:151], a[68:71], a[32:35], v[148:151]\n"
  "v_mfma_f32_16x16x32_bf16 v[180:183], a[68:71], a[36:39], v[180:183]\n"
  "ds_read_b64_tr_b16 a[124:125], v15 offset:49152\n"
  "buffer_load_dwordx4 v26, s[36:39], s64 offen offset:2048 lds\n"
  "v_mfma_f32_16x16x32_bf16 v[212:215], a[68:71], a[40:43], v[212:215]\n"
  "ds_read_b64_tr_b16 a[126:127], v15 offset:50176\n"
  "v_mfma_f32_16x16x32_bf16 v[244:247], a[68:71], a[44:47], v[244:247]\n"
  "v_mfma_f32_16x16x32_bf16 v[152:155], a[72:75], a[32:35], v[152:155]\n"
  "v_mfma_f32_16x16x32_bf16 v[184:187], a[72:75], a[36:39], v[184:187]\n"
  "v_mfma_f32_16x16x32_bf16 v[216:219], a[72:75], a[40:43], v[216:219]\n"
  "v_mfma_f32_16x16x32_bf16 v[248:251], a[72:75], a[44:47], v[248:251]\n"
  "buffer_load_dwordx4 v27, s[36:39], s64 offen offset:3072 lds\n"
  "v_mfma_f32_16x16x32_bf16 v[156:159], a[76:79], a[32:35], v[156:159]\n"
  "v_mfma_f32_16x16x32_bf16 v[188:191], a[76:79], a[36:39], v[188:191]\n"
  "v_mfma_f32_16x16x32_bf16 v[220:223], a[76:79], a[40:43], v[220:223]\n"
  "v_mfma_f32_16x16x32_bf16 v[252:255], a[76:79], a[44:47], v[252:255]\n"
  "s_waitcnt lgkmcnt(0)\n"
  "v_mfma_f32_16x16x32_bf16 a[128:131], a[32:35], a[96:99], a[128:131]\n"
  "ds_read_b64_tr_b16 a[48:49], v8 offset:40960\n"
  "v_mfma_f32_16x16x32_bf16 a[132:135], a[36:39], a[96:99], a[132:135]\n"
  "ds_read_b64_tr_b16 a[50:51], v8 offset:41984\n"
  "v_mfma_f32_16x16x32_bf16 a[192:195], a[40:43], a[96:99], a[192:195]\n"
  "ds_read_b128 a[80:83], v3 offset:0\n"
  "v_mfma_f32_16x16x32_bf16 a[196:199], a[44:47], a[96:99], a[196:199]\n"
  "ds_read_b64_tr_b16 a[52:53], v9 offset:40960\n"
  "v_mfma_f32_16x16x32_bf16 a[136:139], a[32:35], a[100:103], a[136:139]\n"
  "ds_read_b64_tr_b16 a[54:55], v9 offset:41984\n"
  "v_mfma_f32_16x16x32_bf16 a[140:143], a[36:39], a[100:103], a[140:143]\n"
  "v_mfma_f32_16x16x32_bf16 a[200:203], a[40:43], a[100:103], a[200:203]\n"
  "ds_read_b128 a[84:87], v3 offset:2048\n"
  "v_mfma_f32_16x16x32_bf16 a[204:207], a[44:47], a[100:103], a[204:207]\n"
  "ds_read_b64_tr_b16 a[56:57], v10 offset:40960\n"
  "v_mfma_f32_16x16x32_bf16 a[144:147], a[32:35], a[104:107], a[144:147]\n"
  "ds_read_b64_tr_b16 a[58:59], v10 offset:41984\n"
  "v_mfma_f32_16x16x32_bf16 a[148:151], a[36:39], a[104:107], a[148:151]\n"
  "ds_read_b128 a[88:91], v3 offset:4096\n"
  "v_mfma_f32_16x16x32_bf16 a[208:211], a[40:43], a[104:107], a[208:211]\n"
  "ds_read_b64_tr_b16 a[60:61], v11 offset:40960\n"
  "v_mfma_f32_16x16x32_bf16 a[212:215], a[44:47], a[104:107], a[212:215]\n"
  "v_mfma_f32_16x16x32_bf16 a[152:155], a[32:35], a[108:111], a[152:155]\n"
  "ds_read_b64_tr_b16 a[62:63], v11 offset:41984\n"
  "buffer_load_dword a0, v70, s[40:43], 0 offen offset:0\n"
  "v_mfma_f32_16x16x32_bf16 a[156:159], a[36:39], a[108:111], a[156:159]\n"
  "ds_read_b128 a[92:95], v3 offset:6144\n"
  "v_mfma_f32_16x16x32_bf16 a[216:219], a[40:43], a[108:111], a[216:219]\n"
  "ds_read_b64_tr_b16 a[64:65], v12 offset:40960\n"
  "v_mfma_f32_16x16x32_bf16 a[220:223], a[44:47], a[108:111], a[220:223]\n"
  "ds_read_b64_tr_b16 a[66:67], v12 offset:41984\n"
  "v_mfma_f32_16x16x32_bf16 a[160:163], a[32:35], a[112:115], a[160:163]\n"
  "ds_read_b64_tr_b16 a[68:69], v13 offset:40960\n"
  "v_mfma_f32_16x16x32_bf16 a[164:167], a[36:39], a[112:115], a[164:167]\n"
  "v_mfma_f32_16x16x32_bf16 a[224:227], a[40:43], a[112:115], a[224:227]\n"
  "ds_read_b64_tr_b16 a[70:71], v13 offset:41984\n"
  "v_mfma_f32_16x16x32_bf16 a[228:231], a[44:47], a[112:115], a[228:231]\n"
  "ds_read_b64_tr_b16 a[72:73], v14 offset:40960\n"
  "v_mfma_f32_16x16x32_bf16 a[168:171], a[32:35], a[116:119], a[168:171]\n"
  "ds_read_b64_tr_b16 a[74:75], v14 offset:41984\n"
  "v_mfma_f32_16x16x32_bf16 a[172:175], a[36:39], a[116:119], a[172:175]\n"
  "ds_read_b64_tr_b16 a[76:77], v15 offset:40960\n"
  "v_mfma_f32_16x16x32_bf16 a[232:235], a[40:43], a[116:119], a[232:235]\n"
  "ds_read_b64_tr_b16 a[78:79], v15 offset:41984\n"
  "v_mfma_f32_16x16x32_bf16 a[236:239], a[44:47], a[116:119], a[236:239]\n"
  "v_mfma_f32_16x16x32_bf16 a[176:179], a[32:35], a[120:123], a[176:179]\n"
  "v_mfma_f32_16x16x32_bf16 a[180:183], a[36:39], a[120:123], a[180:183]\n"
  "v_mfma_f32_16x16x32_bf16 a[240:243], a[40:43], a[120:123], a[240:243]\n"
  "v_mfma_f32_16x16x32_bf16 a[244:247], a[44:47], a[120:123], a[244:247]\n"
  "v_mfma_f32_16x16x32_bf16 a[184:187], a[32:35], a[124:127], a[184:187]\n"
  "v_mfma_f32_16x16x32_bf16 a[188:191], a[36:39], a[124:127], a[188:191]\n"
  "v_mfma_f32_16x16x32_bf16 a[248:251], a[40:43], a[124:127], a[248:251]\n"
  "v_mfma_f32_16x16x32_bf16 a[252:255], a[44:47], a[124:127], a[252:255]\n"
  "s_waitcnt lgkmcnt(0)\n"
  "s_barrier\n"
  "v_mfma_f32_16x16x32_bf16 v[128:131], a[48:51], a[80:83], v[128:131]\n"
  "ds_read_b64_tr_b16 a[96:97], v8 offset:57344\n"
  "s_add_u32 m0, s60, 65536\n"
  "s_mov_b32 s64, 512\n"
  "v_mfma_f32_16x16x32_bf16 v[160:163], a[48:51], a[84:87], v[160:163]\n"
  "ds_read_b64_tr_b16 a[98:99], v8 offset:58368\n"
  "buffer_load_dwordx4 v16, s[24:27], s64 offen offset:0 lds\n"
  "v_mfma_f32_16x16x32_bf16 v[192:195], a[48:51], a[88:91], v[192:195]\n"
  "v_mfma_f32_16x16x32_bf16 v[224:227], a[48:51], a[92:95], v[224:227]\n"
  "ds_read_b64_tr_b16 a[100:101], v9 offset:57344\n"
  "v_mfma_f32_16x16x32_bf16 v[132:135], a[52:55], a[80:83], v[132:135]\n"
  "ds_read_b64_tr_b16 a[102:103], v9 offset:58368\n"
  "v_mfma_f32_16x16x32_bf16 v[164:167], a[52:55], a[84:87], v[164:167]\n"
  "v_mfma_f32_16x16x32_bf16 v[196:199], a[52:55], a[88:91], v[196:199]\n"
  "ds_read_b64_tr_b16 a[104:105], v10 offset:57344\n"
  "v_mfma_f32_16x16x32_bf16 v[228:231], a[52:55], a[92:95], v[228:231]\n"
  "ds_read_b64_tr_b16 a[106:107], v10 offset:58368\n"
  "buffer_load_dwordx4 v17, s[24:27], s64 offen offset:1024 lds\n"
  "v_mfma_f32_16x16x32_bf16 v[136:139], a[56:59], a[80:83], v[136:139]\n"
  "v_mfma_f32_16x16x32_bf16 v[168:171], a[56:59], a[84:87], v[168:171]\n"
  "ds_read_b64_tr_b16 a[108:109], v11 offset:57344\n"
  "v_mfma_f32_16x16x32_bf16 v[200:203], a[56:59], a[88:91], v[200:203]\n"
  "ds_read_b64_tr_b16 a[110:111], v11 offset:58368\n"
  "v_mfma_f32_16x16x32_bf16 v[232:235], a[56:59], a[92:95], v[232:235]\n"
  "v_mfma_f32_16x16x32_bf16 v[140:143], a[60:63], a[80:83], v[140:143]\n"
  "ds_read_b64_tr_b16 a[112:113], v12 offset:57344\n"
  "v_mfma_f32_16x16x32_bf16 v[172:175], a[60:63], a[84:87], v[172:175]\n"
  "ds_read_b64_tr_b16 a[114:115], v12 offset:58368\n"
  "buffer_load_dwordx4 v18, s[24:27], s64 offen offset:2048 lds\n"
  "v_mfma_f32_16x16x32_bf16 v[204:207], a[60:63], a[88:91], v[204:207]\n"
  "v_mfma_f32_16x16x32_bf16 v[236:239], a[60:63], a[92:95], v[236:239]\n"
  "ds_read_b64_tr_b16 a[116:117], v13 offset:57344\n"
  "v_mfma_f32_16x16x32_bf16 v[144:147], a[64:67], a[80:83], v[144:147]\n"
  "ds_read_b64_tr_b16 a[118:119], v13 offset:58368\n"
  "v_mfma_f32_16x16x32_bf16 v[176:179], a[64:67], a[84:87], v[176:179]\n"
  "v_mfma_f32_16x16x32_bf16 v[208:211], a[64:67], a[88:91], v[208:211]\n"
  "ds_read_b64_tr_b16 a[120:121], v14 offset:57344\n"
  "v_mfma_f32_16x16x32_bf16 v[240:243], a[64:67], a[92:95], v[240:243]\n"
  "ds_read_b64_tr_b16 a[122:123], v14 offset:58368\n"
  "buffer_load_dwordx4 v19, s[24:27], s64 offen offset:3072 lds\n"
  "v_mfma_f32_16x16x32_bf16 v[148:151], a[68:71], a[80:83], v[148:151]\n"
  "s_add_u32 m0, s62, 98304\n"
  "s_mov_b32 s64, 2097152\n"
  "v_mfma_f32_16x16x32_bf16 v[180:183], a[68:71], a[84:87], v[180:183]\n"
  "ds_read_b64_tr_b16 a[124:125], v15 offset:57344\n"
  "v_mfma_f32_16x16x32_bf16 v[212:215], a[68:71], a[88:91], v[212:215]\n"
  "ds_read_b64_tr_b16 a[126:127], v15 offset:58368\n"
  "v_mfma_f32_16x16x32_bf16 v[244:247], a[68:71], a[92:95], v[244:247]\n"
  "v_mfma_f32_16x16x32_bf16 v[152:155], a[72:75], a[80:83], v[152:155]\n"
  "v_add_u32 v2, 0xffff0000, v2\n"
  "v_mfma_f32_16x16x32_bf16 v[184:187], a[72:75], a[84:87], v[184:187]\n"
  "buffer_load_dwordx4 v24, s[28:31], s64 offen offset:0 lds\n"
  "v_add_u32 v3, 0xffff0000, v3\n"
  "v_mfma_f32_16x16x32_bf16 v[216:219], a[72:75], a[88:91], v[216:219]\n"
  "v_add_u32 v8, 0xffff0000, v8\n"
  "v_mfma_f32_16x16x32_bf16 v[248:251], a[72:75], a[92:95], v[248:251]\n"
  "v_add_u32 v9, 0xffff0000, v9\n"
  "v_add_u32 v10, 0xffff0000, v10\n"
  "v_mfma_f32_16x16x32_bf16 v[156:159], a[76:79], a[80:83], v[156:159]\n"
  "v_add_u32 v11, 0xffff0000, v11\n"
  "v_mfma_f32_16x16x32_bf16 v[188:191], a[76:79], a[84:87], v[188:191]\n"
  "v_add_u32 v12, 0xffff0000, v12\n"
  "v_mfma_f32_16x16x32_bf16 v[220:223], a[76:79], a[88:91], v[220:223]\n"
  "v_add_u32 v13, 0xffff0000, v13\n"
  "v_mfma_f32_16x16x32_bf16 v[252:255], a[76:79], a[92:95], v[252:255]\n"
  "buffer_load_dwordx4 v25, s[28:31], s64 offen offset:1024 lds\n"
  "v_add_u32 v14, 0xffff0000, v14\n"
  "v_add_u32 v15, 0xffff0000, v15\n"
  "s_waitcnt vmcnt(7) lgkmcnt(0)\n"
  "s_barrier\n"
  "buffer_atomic_add v7, v6, s[92:95], s69 offen sc1\n"
  "v_mfma_f32_16x16x32_bf16 a[128:131], a[80:83], a[96:99], a[128:131]\n"
  "ds_read_b64_tr_b16 a[48:49], v8 offset:32768\n"
  "v_mfma_f32_16x16x32_bf16 a[132:135], a[84:87], a[96:99], a[132:135]\n"
  "ds_read_b64_tr_b16 a[50:51], v8 offset:33792\n"
  "v_mfma_f32_16x16x32_bf16 a[192:195], a[88:91], a[96:99], a[192:195]\n"
  "ds_read_b128 a[32:35], v2 offset:0\n"
  "v_mfma_f32_16x16x32_bf16 a[196:199], a[92:95], a[96:99], a[196:199]\n"
  "ds_read_b64_tr_b16 a[52:53], v9 offset:32768\n"
  "v_mfma_f32_16x16x32_bf16 a[136:139], a[80:83], a[100:103], a[136:139]\n"
  "ds_read_b64_tr_b16 a[54:55], v9 offset:33792\n"
  "v_mfma_f32_16x16x32_bf16 a[140:143], a[84:87], a[100:103], a[140:143]\n"
  "buffer_load_dwordx4 v26, s[28:31], s64 offen offset:2048 lds\n"
  "v_mfma_f32_16x16x32_bf16 a[200:203], a[88:91], a[100:103], a[200:203]\n"
  "ds_read_b128 a[36:39], v2 offset:2048\n"
  "v_mfma_f32_16x16x32_bf16 a[204:207], a[92:95], a[100:103], a[204:207]\n"
  "ds_read_b64_tr_b16 a[56:57], v10 offset:32768\n"
  "v_mfma_f32_16x16x32_bf16 a[144:147], a[80:83], a[104:107], a[144:147]\n"
  "ds_read_b64_tr_b16 a[58:59], v10 offset:33792\n"
  "v_mfma_f32_16x16x32_bf16 a[148:151], a[84:87], a[104:107], a[148:151]\n"
  "ds_read_b128 a[40:43], v2 offset:4096\n"
  "v_mfma_f32_16x16x32_bf16 a[208:211], a[88:91], a[104:107], a[208:211]\n"
  "ds_read_b64_tr_b16 a[60:61], v11 offset:32768\n"
  "v_mfma_f32_16x16x32_bf16 a[212:215], a[92:95], a[104:107], a[212:215]\n"
  "buffer_load_dwordx4 v27, s[28:31], s64 offen offset:3072 lds\n"
  "v_mfma_f32_16x16x32_bf16 a[152:155], a[80:83], a[108:111], a[152:155]\n"
  "ds_read_b64_tr_b16 a[62:63], v11 offset:33792\n"
  "s_add_u32 m0, s60, 69632\n"
  "s_mov_b32 s64, 512\n"
  "v_mfma_f32_16x16x32_bf16 a[156:159], a[84:87], a[108:111], a[156:159]\n"
  "ds_read_b128 a[44:47], v2 offset:6144\n"
  "v_mfma_f32_16x16x32_bf16 a[216:219], a[88:91], a[108:111], a[216:219]\n"
  "ds_read_b64_tr_b16 a[64:65], v12 offset:32768\n"
  "v_mfma_f32_16x16x32_bf16 a[220:223], a[92:95], a[108:111], a[220:223]\n"
  "ds_read_b64_tr_b16 a[66:67], v12 offset:33792\n"
  "v_mfma_f32_16x16x32_bf16 a[160:163], a[80:83], a[112:115], a[160:163]\n"
  "ds_read_b64_tr_b16 a[68:69], v13 offset:32768\n"
  "v_mfma_f32_16x16x32_bf16 a[164:167], a[84:87], a[112:115], a[164:167]\n"
  "buffer_load_dwordx4 v20, s[24:27], s64 offen offset:0 lds\n"
  "v_mfma_f32_16x16x32_bf16 a[224:227], a[88:91], a[112:115], a[224:227]\n"
  "ds_read_b64_tr_b16 a[70:71], v13 offset:33792\n"
  "v_mfma_f32_16x16x32_bf16 a[228:231], a[92:95], a[112:115], a[228:231]\n"
  "ds_read_b64_tr_b16 a[72:73], v14 offset:32768\n"
  "v_mfma_f32_16x16x32_bf16 a[168:171], a[80:83], a[116:119], a[168:171]\n"
  "ds_read_b64_tr_b16 a[74:75], v14 offset:33792\n"
  "v_mfma_f32_16x16x32_bf16 a[172:175], a[84:87], a[116:119], a[172:175]\n"
  "ds_read_b64_tr_b16 a[76:77], v15 offset:32768\n"
  "v_mfma_f32_16x16x32_bf16 a[232:235], a[88:91], a[116:119], a[232:235]\n"
  "ds_read_b64_tr_b16 a[78:79], v15 offset:33792\n"
  "v_mfma_f32_16x16x32_bf16 a[236:239], a[92:95], a[116:119], a[236:239]\n"
  "buffer_load_dwordx4 v21, s[24:27], s64 offen offset:1024 lds\n"
  "v_mfma_f32_16x16x32_bf16 a[176:179], a[80:83], a[120:123], a[176:179]\n"
  "v_mfma_f32_16x16x32_bf16 a[180:183], a[84:87], a[120:123], a[180:183]\n"
  "v_mfma_f32_16x16x32_bf16 a[240:243], a[88:91], a[120:123], a[240:243]\n"
  "v_mfma_f32_16x16x32_bf16 a[244:247], a[92:95], a[120:123], a[244:247]\n"
  "v_mfma_f32_16x16x32_bf16 a[184:187], a[80:83], a[124:127], a[184:187]\n"
  "v_mfma_f32_16x16x32_bf16 a[188:191], a[84:87], a[124:127], a[188:191]\n"
  "buffer_load_dwordx4 v22, s[24:27], s64 offen offset:2048 lds\n"
  "v_mfma_f32_16x16x32_bf16 a[248:251], a[88:91], a[124:127], a[248:251]\n"
  "v_mfma_f32_16x16x32_bf16 a[252:255], a[92:95], a[124:127], a[252:255]\n"
  "s_waitcnt lgkmcnt(0)\n"
  "v_mfma_f32_16x16x32_bf16 v[128:131], a[48:51], a[32:35], v[128:131]\n"
  "ds_read_b64_tr_b16 a[96:97], v8 offset:49152\n"
  "v_mfma_f32_16x16x32_bf16 v[160:163], a[48:51], a[36:39], v[160:163]\n"
  "ds_read_b64_tr_b16 a[98:99], v8 offset:50176\n"
  "v_mfma_f32_16x16x32_bf16 v[192:195], a[48:51], a[40:43], v[192:195]\n"
  "v_mfma_f32_16x16x32_bf16 v[224:227], a[48:51], a[44:47], v[224:227]\n"
  "ds_read_b64_tr_b16 a[100:101], v9 offset:49152\n"
  "buffer_load_dwordx4 v23, s[24:27], s64 offen offset:3072 lds\n"
  "v_mfma_f32_16x16x32_bf16 v[132:135], a[52:55], a[32:35], v[132:135]\n"
  "ds_read_b64_tr_b16 a[102:103], v9 offset:50176\n"
  "s_add_u32 m0, s62, 114688\n"
  "s_mov_b32 s64, 2097152\n"
  "v_mfma_f32_16x16x32_bf16 v[164:167], a[52:55], a[36:39], v[164:167]\n"
  "v_mfma_f32_16x16x32_bf16 v[196:199], a[52:55], a[40:43], v[196:199]\n"
  "ds_read_b64_tr_b16 a[104:105], v10 offset:49152\n"
  "v_mfma_f32_16x16x32_bf16 v[228:231], a[52:55], a[44:47], v[228:231]\n"
  "ds_read_b64_tr_b16 a[106:107], v10 offset:50176\n"
  "v_mfma_f32_16x16x32_bf16 v[136:139], a[56:59], a[32:35], v[136:139]\n"
  "v_mfma_f32_16x16x32_bf16 v[168:171], a[56:59], a[36:39], v[168:171]\n"
  "ds_read_b64_tr_b16 a[108:109], v11 offset:49152\n"
  "buffer_load_dwordx4 v24, s[36:39], s64 offen offset:0 lds\n"
  "v_mfma_f32_16x16x32_bf16 v[200:203], a[56:59], a[40:43], v[200:203]\n"
  "ds_read_b64_tr_b16 a[110:111], v11 offset:50176\n"
  "v_mfma_f32_16x16x32_bf16 v[232:235], a[56:59], a[44:47], v[232:235]\n"
  "v_mfma_f32_16x16x32_bf16 v[140:143], a[60:63], a[32:35], v[140:143]\n"
  "ds_read_b64_tr_b16 a[112:113], v12 offset:49152\n"
  "v_mfma_f32_16x16x32_bf16 v[172:175], a[60:63], a[36:39], v[172:175]\n"
  "ds_read_b64_tr_b16 a[114:115], v12 offset:50176\n"
  "v_mfma_f32_16x16x32_bf16 v[204:207], a[60:63], a[40:43], v[204:207]\n"
  "v_mfma_f32_16x16x32_bf16 v[236:239], a[60:63], a[44:47], v[236:239]\n"
  "ds_read_b64_tr_b16 a[116:117], v13 offset:49152\n"
  "buffer_load_dwordx4 v25, s[36:39], s64 offen offset:1024 lds\n"
  "v_mfma_f32_16x16x32_bf16 v[144:147], a[64:67], a[32:35], v[144:147]\n"
  "ds_read_b64_tr_b16 a[118:119], v13 offset:50176\n"
  "v_mfma_f32_16x16x32_bf16 v[176:179], a[64:67], a[36:39], v[176:179]\n"
  "v_mfma_f32_16x16x32_bf16 v[208:211], a[64:67], a[40:43], v[208:211]\n"
  "ds_read_b64_tr_b16 a[120:121], v14 offset:49152\n"
  "v_mfma_f32_16x16x32_bf16 v[240:243], a[64:67], a[44:47], v[240:243]\n"
  "ds_read_b64_tr_b16 a[122:123], v14 offset:50176\n"
  "v_mfma_f32_16x16x32_bf16 v[148:151], a[68:71], a[32:35], v[148:151]\n"
  "v_mfma_f32_16x16x32_bf16 v[180:183], a[68:71], a[36:39], v[180:183]\n"
  "ds_read_b64_tr_b16 a[124:125], v15 offset:49152\n"
  "buffer_load_dwordx4 v26, s[36:39], s64 offen offset:2048 lds\n"
  "v_mfma_f32_16x16x32_bf16 v[212:215], a[68:71], a[40:43], v[212:215]\n"
  "ds_read_b64_tr_b16 a[126:127], v15 offset:50176\n"
  "v_mfma_f32_16x16x32_bf16 v[244:247], a[68:71], a[44:47], v[244:247]\n"
  "v_mfma_f32_16x16x32_bf16 v[152:155], a[72:75], a[32:35], v[152:155]\n"
  "v_mfma_f32_16x16x32_bf16 v[184:187], a[72:75], a[36:39], v[184:187]\n"
  "v_mfma_f32_16x16x32_bf16 v[216:219], a[72:75], a[40:43], v[216:219]\n"
  "v_mfma_f32_16x16x32_bf16 v[248:251], a[72:75], a[44:47], v[248:251]\n"
  "buffer_load_dwordx4 v27, s[36:39], s64 offen offset:3072 lds\n"
  "v_mfma_f32_16x16x32_bf16 v[156:159], a[76:79], a[32:35], v[156:159]\n"
  "v_mfma_f32_16x16x32_bf16 v[188:191], a[76:79], a[36:39], v[188:191]\n"
  "v_mfma_f32_16x16x32_bf16 v[220:223], a[76:79], a[40:43], v[220:223]\n"
  "v_mfma_f32_16x16x32_bf16 v[252:255], a[76:79], a[44:47], v[252:255]\n"
  "s_waitcnt lgkmcnt(0)\n"
  "v_mfma_f32_16x16x32_bf16 a[128:131], a[32:35], a[96:99], a[128:131]\n"
  "ds_read_b64_tr_b16 a[48:49], v8 offset:40960\n"
  "v_mfma_f32_16x16x32_bf16 a[132:135], a[36:39], a[96:99], a[132:135]\n"
  "ds_read_b64_tr_b16 a[50:51], v8 offset:41984\n"
  "v_mfma_f32_16x16x32_bf16 a[192:195], a[40:43], a[96:99], a[192:195]\n"
  "ds_read_b128 a[80:83], v3 offset:0\n"
  "v_mfma_f32_16x16x32_bf16 a[196:199], a[44:47], a[96:99], a[196:199]\n"
  "ds_read_b64_tr_b16 a[52:53], v9 offset:40960\n"
  "v_mfma_f32_16x16x32_bf16 a[136:139], a[32:35], a[100:103], a[136:139]\n"
  "ds_read_b64_tr_b16 a[54:55], v9 offset:41984\n"
  "v_mfma_f32_16x16x32_bf16 a[140:143], a[36:39], a[100:103], a[140:143]\n"
  "v_mfma_f32_16x16x32_bf16 a[200:203], a[40:43], a[100:103], a[200:203]\n"
  "ds_read_b128 a[84:87], v3 offset:2048\n"
  "v_mfma_f32_16x16x32_bf16 a[204:207], a[44:47], a[100:103], a[204:207]\n"
  "ds_read_b64_tr_b16 a[56:57], v10 offset:40960\n"
  "v_mfma_f32_16x16x32_bf16 a[144:147], a[32:35], a[104:107], a[144:147]\n"
  "ds_read_b64_tr_b16 a[58:59], v10 offset:41984\n"
  "v_mfma_f32_16x16x32_bf16 a[148:151], a[36:39], a[104:107], a[148:151]\n"
  "ds_read_b128 a[88:91], v3 offset:4096\n"
  "v_mfma_f32_16x16x32_bf16 a[208:211], a[40:43], a[104:107], a[208:211]\n"
  "ds_read_b64_tr_b16 a[60:61], v11 offset:40960\n"
  "v_mfma_f32_16x16x32_bf16 a[212:215], a[44:47], a[104:107], a[212:215]\n"
  "v_mfma_f32_16x16x32_bf16 a[152:155], a[32:35], a[108:111], a[152:155]\n"
  "ds_read_b64_tr_b16 a[62:63], v11 offset:41984\n"
  "v_mfma_f32_16x16x32_bf16 a[156:159], a[36:39], a[108:111], a[156:159]\n"
  "ds_read_b128 a[92:95], v3 offset:6144\n"
  "v_mfma_f32_16x16x32_bf16 a[216:219], a[40:43], a[108:111], a[216:219]\n"
  "ds_read_b64_tr_b16 a[64:65], v12 offset:40960\n"
  "v_mfma_f32_16x16x32_bf16 a[220:223], a[44:47], a[108:111], a[220:223]\n"
  "ds_read_b64_tr_b16 a[66:67], v12 offset:41984\n"
  "v_mfma_f32_16x16x32_bf16 a[160:163], a[32:35], a[112:115], a[160:163]\n"
  "ds_read_b64_tr_b16 a[68:69], v13 offset:40960\n"
  "v_mfma_f32_16x16x32_bf16 a[164:167], a[36:39], a[112:115], a[164:167]\n"
  "v_mfma_f32_16x16x32_bf16 a[224:227], a[40:43], a[112:115], a[224:227]\n"
  "ds_read_b64_tr_b16 a[70:71], v13 offset:41984\n"
  "v_mfma_f32_16x16x32_bf16 a[228:231], a[44:47], a[112:115], a[228:231]\n"
  "ds_read_b64_tr_b16 a[72:73], v14 offset:40960\n"
  "v_mfma_f32_16x16x32_bf16 a[168:171], a[32:35], a[116:119], a[168:171]\n"
  "ds_read_b64_tr_b16 a[74:75], v14 offset:41984\n"
  "v_mfma_f32_16x16x32_bf16 a[172:175], a[36:39], a[116:119], a[172:175]\n"
  "ds_read_b64_tr_b16 a[76:77], v15 offset:40960\n"
  "v_mfma_f32_16x16x32_bf16 a[232:235], a[40:43], a[116:119], a[232:235]\n"
  "ds_read_b64_tr_b16 a[78:79], v15 offset:41984\n"
  "v_mfma_f32_16x16x32_bf16 a[236:239], a[44:47], a[116:119], a[236:239]\n"
  "v_mfma_f32_16x16x32_bf16 a[176:179], a[32:35], a[120:123], a[176:179]\n"
  "v_mfma_f32_16x16x32_bf16 a[180:183], a[36:39], a[120:123], a[180:183]\n"
  "v_mfma_f32_16x16x32_bf16 a[240:243], a[40:43], a[120:123], a[240:243]\n"
  "v_mfma_f32_16x16x32_bf16 a[244:247], a[44:47], a[120:123], a[244:247]\n"
  "v_mfma_f32_16x16x32_bf16 a[184:187], a[32:35], a[124:127], a[184:187]\n"
  "v_mfma_f32_16x16x32_bf16 a[188:191], a[36:39], a[124:127], a[188:191]\n"
  "v_mfma_f32_16x16x32_bf16 a[248:251], a[40:43], a[124:127], a[248:251]\n"
  "v_mfma_f32_16x16x32_bf16 a[252:255], a[44:47], a[124:127], a[252:255]\n"
  "s_waitcnt lgkmcnt(0)\n"
  "s_barrier\n"
  "v_mfma_f32_16x16x32_bf16 v[128:131], a[48:51], a[80:83], v[128:131]\n"
  "ds_read_b64_tr_b16 a[96:97], v8 offset:57344\n"
  "s_add_u32 m0, s60, 0\n"
  "s_mov_b32 s64, 640\n"
  "v_mfma_f32_16x16x32_bf16 v[160:163], a[48:51], a[84:87], v[160:163]\n"
  "ds_read_b64_tr_b16 a[98:99], v8 offset:58368\n"
  "buffer_load_dwordx4 v16, s[24:27], s64 offen offset:0 lds\n"
  "v_mfma_f32_16x16x32_bf16 v[192:195], a[48:51], a[88:91], v[192:195]\n"
  "buffer_load_dword a0, v69, s[44:47], 0 offen offset:0\n"
  "v_mfma_f32_16x16x32_bf16 v[224:227], a[48:51], a[92:95], v[224:227]\n"
  "ds_read_b64_tr_b16 a[100:101], v9 offset:57344\n"
  "v_mfma_f32_16x16x32_bf16 v[132:135], a[52:55], a[80:83], v[132:135]\n"
  "ds_read_b64_tr_b16 a[102:103], v9 offset:58368\n"
  "v_mfma_f32_16x16x32_bf16 v[164:167], a[52:55], a[84:87], v[164:167]\n"
  "v_mfma_f32_16x16x32_bf16 v[196:199], a[52:55], a[88:91], v[196:199]\n"
  "ds_read_b64_tr_b16 a[104:105], v10 offset:57344\n"
  "v_mfma_f32_16x16x32_bf16 v[228:231], a[52:55], a[92:95], v[228:231]\n"
  "ds_read_b64_tr_b16 a[106:107], v10 offset:58368\n"
  "buffer_load_dwordx4 v17, s[24:27], s64 offen offset:1024 lds\n"
  "v_mfma_f32_16x16x32_bf16 v[136:139], a[56:59], a[80:83], v[136:139]\n"
  "v_mfma_f32_16x16x32_bf16 v[168:171], a[56:59], a[84:87], v[168:171]\n"
  "ds_read_b64_tr_b16 a[108:109], v11 offset:57344\n"
  "v_mfma_f32_16x16x32_bf16 v[200:203], a[56:59], a[88:91], v[200:203]\n"
  "ds_read_b64_tr_b16 a[110:111], v11 offset:58368\n"
  "v_mfma_f32_16x16x32_bf16 v[232:235], a[56:59], a[92:95], v[232:235]\n"
  "v_mfma_f32_16x16x32_bf16 v[140:143], a[60:63], a[80:83], v[140:143]\n"
  "ds_read_b64_tr_b16 a[112:113], v12 offset:57344\n"
  "v_mfma_f32_16x16x32_bf16 v[172:175], a[60:63], a[84:87], v[172:175]\n"
  "ds_read_b64_tr_b16 a[114:115], v12 offset:58368\n"
  "buffer_load_dwordx4 v18, s[24:27], s64 offen offset:2048 lds\n"
  "v_mfma_f32_16x16x32_bf16 v[204:207], a[60:63], a[88:91], v[204:207]\n"
  "v_mfma_f32_16x16x32_bf16 v[236:239], a[60:63], a[92:95], v[236:239]\n"
  "ds_read_b64_tr_b16 a[116:117], v13 offset:57344\n"
  "v_mfma_f32_16x16x32_bf16 v[144:147], a[64:67], a[80:83], v[144:147]\n"
  "ds_read_b64_tr_b16 a[118:119], v13 offset:58368\n"
  "v_mfma_f32_16x16x32_bf16 v[176:179], a[64:67], a[84:87], v[176:179]\n"
  "v_mfma_f32_16x16x32_bf16 v[208:211], a[64:67], a[88:91], v[208:211]\n"
  "ds_read_b64_tr_b16 a[120:121], v14 offset:57344\n"
  "v_mfma_f32_16x16x32_bf16 v[240:243], a[64:67], a[92:95], v[240:243]\n"
  "ds_read_b64_tr_b16 a[122:123], v14 offset:58368\n"
  "buffer_load_dwordx4 v19, s[24:27], s64 offen offset:3072 lds\n"
  "v_mfma_f32_16x16x32_bf16 v[148:151], a[68:71], a[80:83], v[148:151]\n"
  "s_add_u32 m0, s62, 32768\n"
  "s_mov_b32 s64, 2621440\n"
  "v_mfma_f32_16x16x32_bf16 v[180:183], a[68:71], a[84:87], v[180:183]\n"
  "ds_read_b64_tr_b16 a[124:125], v15 offset:57344\n"
  "v_mfma_f32_16x16x32_bf16 v[212:215], a[68:71], a[88:91], v[212:215]\n"
  "ds_read_b64_tr_b16 a[126:127], v15 offset:58368\n"
  "v_mfma_f32_16x16x32_bf16 v[244:247], a[68:71], a[92:95], v[244:247]\n"
  "v_mfma_f32_16x16x32_bf16 v[152:155], a[72:75], a[80:83], v[152:155]\n"
  "v_add_u32 v2, 0x10000, v2\n"
  "v_mfma_f32_16x16x32_bf16 v[184:187], a[72:75], a[84:87], v[184:187]\n"
  "buffer_load_dwordx4 v24, s[28:31], s64 offen offset:0 lds\n"
  "v_add_u32 v3, 0x10000, v3\n"
  "v_mfma_f32_16x16x32_bf16 v[216:219], a[72:75], a[88:91], v[216:219]\n"
  "v_add_u32 v8, 0x10000, v8\n"
  "v_mfma_f32_16x16x32_bf16 v[248:251], a[72:75], a[92:95], v[248:251]\n"
  "v_add_u32 v9, 0x10000, v9\n"
  "v_add_u32 v10, 0x10000, v10\n"
  "v_mfma_f32_16x16x32_bf16 v[156:159], a[76:79], a[80:83], v[156:159]\n"
  "v_add_u32 v11, 0x10000, v11\n"
  "v_mfma_f32_16x16x32_bf16 v[188:191], a[76:79], a[84:87], v[188:191]\n"
  "v_add_u32 v12, 0x10000, v12\n"
  "v_mfma_f32_16x16x32_bf16 v[220:223], a[76:79], a[88:91], v[220:223]\n"
  "v_add_u32 v13, 0x10000, v13\n"
  "v_mfma_f32_16x16x32_bf16 v[252:255], a[76:79], a[92:95], v[252:255]\n"
  "buffer_load_dwordx4 v25, s[28:31], s64 offen offset:1024 lds\n"
  "v_add_u32 v14, 0x10000, v14\n"
  "v_add_u32 v15, 0x10000, v15\n"
  "s_waitcnt vmcnt(7) lgkmcnt(0)\n"
  "s_barrier\n"
  "v_mfma_f32_16x16x32_bf16 a[128:131], a[80:83], a[96:99], a[128:131]\n"
  "ds_read_b64_tr_b16 a[48:49], v8 offset:32768\n"
  "v_mfma_f32_16x16x32_bf16 a[132:135], a[84:87], a[96:99], a[132:135]\n"
  "ds_read_b64_tr_b16 a[50:51], v8 offset:33792\n"
  "v_mfma_f32_16x16x32_bf16 a[192:195], a[88:91], a[96:99], a[192:195]\n"
  "ds_read_b128 a[32:35], v2 offset:0\n"
  "v_mfma_f32_16x16x32_bf16 a[196:199], a[92:95], a[96:99], a[196:199]\n"
  "ds_read_b64_tr_b16 a[52:53], v9 offset:32768\n"
  "v_mfma_f32_16x16x32_bf16 a[136:139], a[80:83], a[100:103], a[136:139]\n"
  "ds_read_b64_tr_b16 a[54:55], v9 offset:33792\n"
  "v_mfma_f32_16x16x32_bf16 a[140:143], a[84:87], a[100:103], a[140:143]\n"
  "buffer_load_dwordx4 v26, s[28:31], s64 offen offset:2048 lds\n"
  "v_mfma_f32_16x16x32_bf16 a[200:203], a[88:91], a[100:103], a[200:203]\n"
  "ds_read_b128 a[36:39], v2 offset:2048\n"
  "v_mfma_f32_16x16x32_bf16 a[204:207], a[92:95], a[100:103], a[204:207]\n"
  "ds_read_b64_tr_b16 a[56:57], v10 offset:32768\n"
  "v_mfma_f32_16x16x32_bf16 a[144:147], a[80:83], a[104:107], a[144:147]\n"
  "ds_read_b64_tr_b16 a[58:59], v10 offset:33792\n"
  "v_mfma_f32_16x16x32_bf16 a[148:151], a[84:87], a[104:107], a[148:151]\n"
  "ds_read_b128 a[40:43], v2 offset:4096\n"
  "v_mfma_f32_16x16x32_bf16 a[208:211], a[88:91], a[104:107], a[208:211]\n"
  "ds_read_b64_tr_b16 a[60:61], v11 offset:32768\n"
  "v_mfma_f32_16x16x32_bf16 a[212:215], a[92:95], a[104:107], a[212:215]\n"
  "buffer_load_dwordx4 v27, s[28:31], s64 offen offset:3072 lds\n"
  "v_mfma_f32_16x16x32_bf16 a[152:155], a[80:83], a[108:111], a[152:155]\n"
  "ds_read_b64_tr_b16 a[62:63], v11 offset:33792\n"
  "s_add_u32 m0, s60, 4096\n"
  "s_mov_b32 s64, 640\n"
  "v_mfma_f32_16x16x32_bf16 a[156:159], a[84:87], a[108:111], a[156:159]\n"
  "ds_read_b128 a[44:47], v2 offset:6144\n"
  "v_mfma_f32_16x16x32_bf16 a[216:219], a[88:91], a[108:111], a[216:219]\n"
  "ds_read_b64_tr_b16 a[64:65], v12 offset:32768\n"
  "v_mfma_f32_16x16x32_bf16 a[220:223], a[92:95], a[108:111], a[220:223]\n"
  "ds_read_b64_tr_b16 a[66:67], v12 offset:33792\n"
  "v_mfma_f32_16x16x32_bf16 a[160:163], a[80:83], a[112:115], a[160:163]\n"
  "ds_read_b64_tr_b16 a[68:69], v13 offset:32768\n"
  "v_mfma_f32_16x16x32_bf16 a[164:167], a[84:87], a[112:115], a[164:167]\n"
  "buffer_load_dwordx4 v20, s[24:27], s64 offen offset:0 lds\n"
  "v_mfma_f32_16x16x32_bf16 a[224:227], a[88:91], a[112:115], a[224:227]\n"
  "ds_read_b64_tr_b16 a[70:71], v13 offset:33792\n"
  "v_mfma_f32_16x16x32_bf16 a[228:231], a[92:95], a[112:115], a[228:231]\n"
  "ds_read_b64_tr_b16 a[72:73], v14 offset:32768\n"
  "v_mfma_f32_16x16x32_bf16 a[168:171], a[80:83], a[116:119], a[168:171]\n"
  "ds_read_b64_tr_b16 a[74:75], v14 offset:33792\n"
  "v_mfma_f32_16x16x32_bf16 a[172:175], a[84:87], a[116:119], a[172:175]\n"
  "ds_read_b64_tr_b16 a[76:77], v15 offset:32768\n"
  "v_mfma_f32_16x16x32_bf16 a[232:235], a[88:91], a[116:119], a[232:235]\n"
  "ds_read_b64_tr_b16 a[78:79], v15 offset:33792\n"
  "v_mfma_f32_16x16x32_bf16 a[236:239], a[92:95], a[116:119], a[236:239]\n"
  "buffer_load_dwordx4 v21, s[24:27], s64 offen offset:1024 lds\n"
  "v_mfma_f32_16x16x32_bf16 a[176:179], a[80:83], a[120:123], a[176:179]\n"
  "v_mfma_f32_16x16x32_bf16 a[180:183], a[84:87], a[120:123], a[180:183]\n"
  "v_mfma_f32_16x16x32_bf16 a[240:243], a[88:91], a[120:123], a[240:243]\n"
  "v_mfma_f32_16x16x32_bf16 a[244:247], a[92:95], a[120:123], a[244:247]\n"
  "v_mfma_f32_16x16x32_bf16 a[184:187], a[80:83], a[124:127], a[184:187]\n"
  "v_mfma_f32_16x16x32_bf16 a[188:191], a[84:87], a[124:127], a[188:191]\n"
  "buffer_load_dwordx4 v22, s[24:27], s64 offen offset:2048 lds\n"
  "v_mfma_f32_16x16x32_bf16 a[248:251], a[88:91], a[124:127], a[248:251]\n"
  "v_mfma_f32_16x16x32_bf16 a[252:255], a[92:95], a[124:127], a[252:255]\n"
  "s_waitcnt lgkmcnt(0)\n"
  "v_mfma_f32_16x16x32_bf16 v[128:131], a[48:51], a[32:35], v[128:131]\n"
  "ds_read_b64_tr_b16 a[96:97], v8 offset:49152\n"
  "v_mfma_f32_16x16x32_bf16 v[160:163], a[48:51], a[36:39], v[160:163]\n"
  "ds_read_b64_tr_b16 a[98:99], v8 offset:50176\n"
  "v_mfma_f32_16x16x32_bf16 v[192:195], a[48:51], a[40:43], v[192:195]\n"
  "v_mfma_f32_16x16x32_bf16 v[224:227], a[48:51], a[44:47], v[224:227]\n"
  "ds_read_b64_tr_b16 a[100:101], v9 offset:49152\n"
  "buffer_load_dwordx4 v23, s[24:27], s64 offen offset:3072 lds\n"
  "v_mfma_f32_16x16x32_bf16 v[132:135], a[52:55], a[32:35], v[132:135]\n"
  "ds_read_b64_tr_b16 a[102:103], v9 offset:50176\n"
  "s_add_u32 m0, s62, 49152\n"
  "s_mov_b32 s64, 2621440\n"
  "v_mfma_f32_16x16x32_bf16 v[164:167], a[52:55], a[36:39], v[164:167]\n"
  "v_mfma_f32_16x16x32_bf16 v[196:199], a[52:55], a[40:43], v[196:199]\n"
  "ds_read_b64_tr_b16 a[104:105], v10 offset:49152\n"
  "v_mfma_f32_16x16x32_bf16 v[228:231], a[52:55], a[44:47], v[228:231]\n"
  "ds_read_b64_tr_b16 a[106:107], v10 offset:50176\n"
  "v_mfma_f32_16x16x32_bf16 v[136:139], a[56:59], a[32:35], v[136:139]\n"
  "v_mfma_f32_16x16x32_bf16 v[168:171], a[56:59], a[36:39], v[168:171]\n"
  "ds_read_b64_tr_b16 a[108:109], v11 offset:49152\n"
  "buffer_load_dwordx4 v24, s[36:39], s64 offen offset:0 lds\n"
  "v_mfma_f32_16x16x32_bf16 v[200:203], a[56:59], a[40:43], v[200:203]\n"
  "ds_read_b64_tr_b16 a[110:111], v11 offset:50176\n"
  "v_mfma_f32_16x16x32_bf16 v[232:235], a[56:59], a[44:47], v[232:235]\n"
  "v_mfma_f32_16x16x32_bf16 v[140:143], a[60:63], a[32:35], v[140:143]\n"
  "ds_read_b64_tr_b16 a[112:113], v12 offset:49152\n"
  "v_mfma_f32_16x16x32_bf16 v[172:175], a[60:63], a[36:39], v[172:175]\n"
  "ds_read_b64_tr_b16 a[114:115], v12 offset:50176\n"
  "v_mfma_f32_16x16x32_bf16 v[204:207], a[60:63], a[40:43], v[204:207]\n"
  "v_mfma_f32_16x16x32_bf16 v[236:239], a[60:63], a[44:47], v[236:239]\n"
  "ds_read_b64_tr_b16 a[116:117], v13 offset:49152\n"
  "buffer_load_dwordx4 v25, s[36:39], s64 offen offset:1024 lds\n"
  "v_mfma_f32_16x16x32_bf16 v[144:147], a[64:67], a[32:35], v[144:147]\n"
  "ds_read_b64_tr_b16 a[118:119], v13 offset:50176\n"
  "v_mfma_f32_16x16x32_bf16 v[176:179], a[64:67], a[36:39], v[176:179]\n"
  "v_mfma_f32_16x16x32_bf16 v[208:211], a[64:67], a[40:43], v[208:211]\n"
  "ds_read_b64_tr_b16 a[120:121], v14 offset:49152\n"
  "v_mfma_f32_16x16x32_bf16 v[240:243], a[64:67], a[44:47], v[240:243]\n"
  "ds_read_b64_tr_b16 a[122:123], v14 offset:50176\n"
  "v_mfma_f32_16x16x32_bf16 v[148:151], a[68:71], a[32:35], v[148:151]\n"
  "v_mfma_f32_16x16x32_bf16 v[180:183], a[68:71], a[36:39], v[180:183]\n"
  "ds_read_b64_tr_b16 a[124:125], v15 offset:49152\n"
  "buffer_load_dwordx4 v26, s[36:39], s64 offen offset:2048 lds\n"
  "v_mfma_f32_16x16x32_bf16 v[212:215], a[68:71], a[40:43], v[212:215]\n"
  "ds_read_b64_tr_b16 a[126:127], v15 offset:50176\n"
  "v_mfma_f32_16x16x32_bf16 v[244:247], a[68:71], a[44:47], v[244:247]\n"
  "v_mfma_f32_16x16x32_bf16 v[152:155], a[72:75], a[32:35], v[152:155]\n"
  "v_mfma_f32_16x16x32_bf16 v[184:187], a[72:75], a[36:39], v[184:187]\n"
  "v_mfma_f32_16x16x32_bf16 v[216:219], a[72:75], a[40:43], v[216:219]\n"
  "v_mfma_f32_16x16x32_bf16 v[248:251], a[72:75], a[44:47], v[248:251]\n"
  "buffer_load_dwordx4 v27, s[36:39], s64 offen offset:3072 lds\n"
  "v_mfma_f32_16x16x32_bf16 v[156:159], a[76:79], a[32:35], v[156:159]\n"
  "v_mfma_f32_16x16x32_bf16 v[188:191], a[76:79], a[36:39], v[188:191]\n"
  "v_mfma_f32_16x16x32_bf16 v[220:223], a[76:79], a[40:43], v[220:223]\n"
  "v_mfma_f32_16x16x32_bf16 v[252:255], a[76:79], a[44:47], v[252:255]\n"
  "s_waitcnt lgkmcnt(0)\n"
  "v_mfma_f32_16x16x32_bf16 a[128:131], a[32:35], a[96:99], a[128:131]\n"
  "ds_read_b64_tr_b16 a[48:49], v8 offset:40960\n"
  "v_mfma_f32_16x16x32_bf16 a[132:135], a[36:39], a[96:99], a[132:135]\n"
  "ds_read_b64_tr_b16 a[50:51], v8 offset:41984\n"
  "v_mfma_f32_16x16x32_bf16 a[192:195], a[40:43], a[96:99], a[192:195]\n"
  "ds_read_b128 a[80:83], v3 offset:0\n"
  "v_mfma_f32_16x16x32_bf16 a[196:199], a[44:47], a[96:99], a[196:199]\n"
  "ds_read_b64_tr_b16 a[52:53], v9 offset:40960\n"
  "v_mfma_f32_16x16x32_bf16 a[136:139], a[32:35], a[100:103], a[136:139]\n"
  "ds_read_b64_tr_b16 a[54:55], v9 offset:41984\n"
  "v_mfma_f32_16x16x32_bf16 a[140:143], a[36:39], a[100:103], a[140:143]\n"
  "v_mfma_f32_16x16x32_bf16 a[200:203], a[40:43], a[100:103], a[200:203]\n"
  "ds_read_b128 a[84:87], v3 offset:2048\n"
  "v_mfma_f32_16x16x32_bf16 a[204:207], a[44:47], a[100:103], a[204:207]\n"
  "ds_read_b64_tr_b16 a[56:57], v10 offset:40960\n"
  "v_mfma_f32_16x16x32_bf16 a[144:147], a[32:35], a[104:107], a[144:147]\n"
  "ds_read_b64_tr_b16 a[58:59], v10 offset:41984\n"
  "v_mfma_f32_16x16x32_bf16 a[148:151], a[36:39], a[104:107], a[148:151]\n"
  "ds_read_b128 a[88:91], v3 offset:4096\n"
  "v_mfma_f32_16x16x32_bf16 a[208:211], a[40:43], a[104:107], a[208:211]\n"
  "ds_read_b64_tr_b16 a[60:61], v11 offset:40960\n"
  "v_mfma_f32_16x16x32_bf16 a[212:215], a[44:47], a[104:107], a[212:215]\n"
  "v_mfma_f32_16x16x32_bf16 a[152:155], a[32:35], a[108:111], a[152:155]\n"
  "ds_read_b64_tr_b16 a[62:63], v11 offset:41984\n"
  "v_mfma_f32_16x16x32_bf16 a[156:159], a[36:39], a[108:111], a[156:159]\n"
  "ds_read_b128 a[92:95], v3 offset:6144\n"
  "v_mfma_f32_16x16x32_bf16 a[216:219], a[40:43], a[108:111], a[216:219]\n"
  "ds_read_b64_tr_b16 a[64:65], v12 offset:40960\n"
  "v_mfma_f32_16x16x32_bf16 a[220:223], a[44:47], a[108:111], a[220:223]\n"
  "ds_read_b64_tr_b16 a[66:67], v12 offset:41984\n"
  "v_mfma_f32_16x16x32_bf16 a[160:163], a[32:35], a[112:115], a[160:163]\n"
  "ds_read_b64_tr_b16 a[68:69], v13 offset:40960\n"
  "v_mfma_f32_16x16x32_bf16 a[164:167], a[36:39], a[112:115], a[164:167]\n"
  "v_mfma_f32_16x16x32_bf16 a[224:227], a[40:43], a[112:115], a[224:227]\n"
  "ds_read_b64_tr_b16 a[70:71], v13 offset:41984\n"
  "v_mfma_f32_16x16x32_bf16 a[228:231], a[44:47], a[112:115], a[228:231]\n"
  "ds_read_b64_tr_b16 a[72:73], v14 offset:40960\n"
  "v_mfma_f32_16x16x32_bf16 a[168:171], a[32:35], a[116:119], a[168:171]\n"
  "ds_read_b64_tr_b16 a[74:75], v14 offset:41984\n"
  "v_mfma_f32_16x16x32_bf16 a[172:175], a[36:39], a[116:119], a[172:175]\n"
  "ds_read_b64_tr_b16 a[76:77], v15 offset:40960\n"
  "v_mfma_f32_16x16x32_bf16 a[232:235], a[40:43], a[116:119], a[232:235]\n"
  "ds_read_b64_tr_b16 a[78:79], v15 offset:41984\n"
  "v_mfma_f32_16x16x32_bf16 a[236:239], a[44:47], a[116:119], a[236:239]\n"
  "v_mfma_f32_16x16x32_bf16 a[176:179], a[32:35], a[120:123], a[176:179]\n"
  "v_mfma_f32_16x16x32_bf16 a[180:183], a[36:39], a[120:123], a[180:183]\n"
  "v_mfma_f32_16x16x32_bf16 a[240:243], a[40:43], a[120:123], a[240:243]\n"
  "v_mfma_f32_16x16x32_bf16 a[244:247], a[44:47], a[120:123], a[244:247]\n"
  "v_mfma_f32_16x16x32_bf16 a[184:187], a[32:35], a[124:127], a[184:187]\n"
  "v_mfma_f32_16x16x32_bf16 a[188:191], a[36:39], a[124:127], a[188:191]\n"
  "v_mfma_f32_16x16x32_bf16 a[248:251], a[40:43], a[124:127], a[248:251]\n"
  "v_mfma_f32_16x16x32_bf16 a[252:255], a[44:47], a[124:127], a[252:255]\n"
  "s_waitcnt lgkmcnt(0)\n"
  "s_barrier\n"
  "v_mfma_f32_16x16x32_bf16 v[128:131], a[48:51], a[80:83], v[128:131]\n"
  "ds_read_b64_tr_b16 a[96:97], v8 offset:57344\n"
  "s_add_u32 m0, s60, 65536\n"
  "s_mov_b32 s64, 768\n"
  "v_mfma_f32_16x16x32_bf16 v[160:163], a[48:51], a[84:87], v[160:163]\n"
  "ds_read_b64_tr_b16 a[98:99], v8 offset:58368\n"
  "buffer_load_dwordx4 v16, s[24:27], s64 offen offset:0 lds\n"
  "v_mfma_f32_16x16x32_bf16 v[192:195], a[48:51], a[88:91], v[192:195]\n"
  "v_mfma_f32_16x16x32_bf16 v[224:227], a[48:51], a[92:95], v[224:227]\n"
  "ds_read_b64_tr_b16 a[100:101], v9 offset:57344\n"
  "v_mfma_f32_16x16x32_bf16 v[132:135], a[52:55], a[80:83], v[132:135]\n"
  "ds_read_b64_tr_b16 a[102:103], v9 offset:58368\n"
  "v_mfma_f32_16x16x32_bf16 v[164:167], a[52:55], a[84:87], v[164:167]\n"
  "v_mfma_f32_16x16x32_bf16 v[196:199], a[52:55], a[88:91], v[196:199]\n"
  "ds_read_b64_tr_b16 a[104:105], v10 offset:57344\n"
  "v_mfma_f32_16x16x32_bf16 v[228:231], a[52:55], a[92:95], v[228:231]\n"
  "ds_read_b64_tr_b16 a[106:107], v10 offset:58368\n"
  "buffer_load_dwordx4 v17, s[24:27], s64 offen offset:1024 lds\n"
  "v_mfma_f32_16x16x32_bf16 v[136:139], a[56:59], a[80:83], v[136:139]\n"
  "v_mfma_f32_16x16x32_bf16 v[168:171], a[56:59], a[84:87], v[168:171]\n"
  "ds_read_b64_tr_b16 a[108:109], v11 offset:57344\n"
  "v_mfma_f32_16x16x32_bf16 v[200:203], a[56:59], a[88:91], v[200:203]\n"
  "ds_read_b64_tr_b16 a[110:111], v11 offset:58368\n"
  "v_mfma_f32_16x16x32_bf16 v[232:235], a[56:59], a[92:95], v[232:235]\n"
  "v_mfma_f32_16x16x32_bf16 v[140:143], a[60:63], a[80:83], v[140:143]\n"
  "ds_read_b64_tr_b16 a[112:113], v12 offset:57344\n"
  "v_mfma_f32_16x16x32_bf16 v[172:175], a[60:63], a[84:87], v[172:175]\n"
  "ds_read_b64_tr_b16 a[114:115], v12 offset:58368\n"
  "buffer_load_dwordx4 v18, s[24:27], s64 offen offset:2048 lds\n"
  "v_mfma_f32_16x16x32_bf16 v[204:207], a[60:63], a[88:91], v[204:207]\n"
  "v_mfma_f32_16x16x32_bf16 v[236:239], a[60:63], a[92:95], v[236:239]\n"
  "ds_read_b64_tr_b16 a[116:117], v13 offset:57344\n"
  "v_mfma_f32_16x16x32_bf16 v[144:147], a[64:67], a[80:83], v[144:147]\n"
  "ds_read_b64_tr_b16 a[118:119], v13 offset:58368\n"
  "v_mfma_f32_16x16x32_bf16 v[176:179], a[64:67], a[84:87], v[176:179]\n"
  "v_mfma_f32_16x16x32_bf16 v[208:211], a[64:67], a[88:91], v[208:211]\n"
  "ds_read_b64_tr_b16 a[120:121], v14 offset:57344\n"
  "v_mfma_f32_16x16x32_bf16 v[240:243], a[64:67], a[92:95], v[240:243]\n"
  "ds_read_b64_tr_b16 a[122:123], v14 offset:58368\n"
  "buffer_load_dwordx4 v19, s[24:27], s64 offen offset:3072 lds\n"
  "v_mfma_f32_16x16x32_bf16 v[148:151], a[68:71], a[80:83], v[148:151]\n"
  "s_add_u32 m0, s62, 98304\n"
  "s_mov_b32 s64, 3145728\n"
  "v_mfma_f32_16x16x32_bf16 v[180:183], a[68:71], a[84:87], v[180:183]\n"
  "ds_read_b64_tr_b16 a[124:125], v15 offset:57344\n"
  "v_mfma_f32_16x16x32_bf16 v[212:215], a[68:71], a[88:91], v[212:215]\n"
  "ds_read_b64_tr_b16 a[126:127], v15 offset:58368\n"
  "v_mfma_f32_16x16x32_bf16 v[244:247], a[68:71], a[92:95], v[244:247]\n"
  "v_mfma_f32_16x16x32_bf16 v[152:155], a[72:75], a[80:83], v[152:155]\n"
  "v_add_u32 v2, 0xffff0000, v2\n"
  "v_mfma_f32_16x16x32_bf16 v[184:187], a[72:75], a[84:87], v[184:187]\n"
  "buffer_load_dwordx4 v24, s[28:31], s64 offen offset:0 lds\n"
  "v_add_u32 v3, 0xffff0000, v3\n"
  "v_mfma_f32_16x16x32_bf16 v[216:219], a[72:75], a[88:91], v[216:219]\n"
  "v_add_u32 v8, 0xffff0000, v8\n"
  "v_mfma_f32_16x16x32_bf16 v[248:251], a[72:75], a[92:95], v[248:251]\n"
  "v_add_u32 v9, 0xffff0000, v9\n"
  "v_add_u32 v10, 0xffff0000, v10\n"
  "v_mfma_f32_16x16x32_bf16 v[156:159], a[76:79], a[80:83], v[156:159]\n"
  "v_add_u32 v11, 0xffff0000, v11\n"
  "v_mfma_f32_16x16x32_bf16 v[188:191], a[76:79], a[84:87], v[188:191]\n"
  "v_add_u32 v12, 0xffff0000, v12\n"
  "v_mfma_f32_16x16x32_bf16 v[220:223], a[76:79], a[88:91], v[220:223]\n"
  "v_add_u32 v13, 0xffff0000, v13\n"
  "v_mfma_f32_16x16x32_bf16 v[252:255], a[76:79], a[92:95], v[252:255]\n"
  "buffer_load_dwordx4 v25, s[28:31], s64 offen offset:1024 lds\n"
  "v_add_u32 v14, 0xffff0000, v14\n"
  "v_add_u32 v15, 0xffff0000, v15\n"
  "s_waitcnt vmcnt(6) lgkmcnt(0)\n"
  "s_barrier\n"
  "buffer_atomic_add v7, v6, s[92:95], s69 offen sc1\n"
  "v_mfma_f32_16x16x32_bf16 a[128:131], a[80:83], a[96:99], a[128:131]\n"
  "ds_read_b64_tr_b16 a[48:49], v8 offset:32768\n"
  "buffer_load_dword a0, v70, s[48:51], 0 offen offset:0\n"
  "v_mfma_f32_16x16x32_bf16 a[132:135], a[84:87], a[96:99], a[132:135]\n"
  "ds_read_b64_tr_b16 a[50:51], v8 offset:33792\n"
  "v_mfma_f32_16x16x32_bf16 a[192:195], a[88:91], a[96:99], a[192:195]\n"
  "ds_read_b128 a[32:35], v2 offset:0\n"
  "v_mfma_f32_16x16x32_bf16 a[196:199], a[92:95], a[96:99], a[196:199]\n"
  "ds_read_b64_tr_b16 a[52:53], v9 offset:32768\n"
  "v_mfma_f32_16x16x32_bf16 a[136:139], a[80:83], a[100:103], a[136:139]\n"
  "ds_read_b64_tr_b16 a[54:55], v9 offset:33792\n"
  "v_mfma_f32_16x16x32_bf16 a[140:143], a[84:87], a[100:103], a[140:143]\n"
  "buffer_load_dwordx4 v26, s[28:31], s64 offen offset:2048 lds\n"
  "v_mfma_f32_16x16x32_bf16 a[200:203], a[88:91], a[100:103], a[200:203]\n"
  "ds_read_b128 a[36:39], v2 offset:2048\n"
  "v_mfma_f32_16x16x32_bf16 a[204:207], a[92:95], a[100:103], a[204:207]\n"
  "ds_read_b64_tr_b16 a[56:57], v10 offset:32768\n"
  "v_mfma_f32_16x16x32_bf16 a[144:147], a[80:83], a[104:107], a[144:147]\n"
  "ds_read_b64_tr_b16 a[58:59], v10 offset:33792\n"
  "v_mfma_f32_16x16x32_bf16 a[148:151], a[84:87], a[104:107], a[148:151]\n"
  "ds_read_b128 a[40:43], v2 offset:4096\n"
  "v_mfma_f32_16x16x32_bf16 a[208:211], a[88:91], a[104:107], a[208:211]\n"
  "ds_read_b64_tr_b16 a[60:61], v11 offset:32768\n"
  "v_mfma_f32_16x16x32_bf16 a[212:215], a[92:95], a[104:107], a[212:215]\n"
  "buffer_load_dwordx4 v27, s[28:31], s64 offen offset:3072 lds\n"
  "v_mfma_f32_16x16x32_bf16 a[152:155], a[80:83], a[108:111], a[152:155]\n"
  "ds_read_b64_tr_b16 a[62:63], v11 offset:33792\n"
  "s_add_u32 m0, s60, 69632\n"
  "s_mov_b32 s64, 768\n"
  "v_mfma_f32_16x16x32_bf16 a[156:159], a[84:87], a[108:111], a[156:159]\n"
  "ds_read_b128 a[44:47], v2 offset:6144\n"
  "v_mfma_f32_16x16x32_bf16 a[216:219], a[88:91], a[108:111], a[216:219]\n"
  "ds_read_b64_tr_b16 a[64:65], v12 offset:32768\n"
  "v_mfma_f32_16x16x32_bf16 a[220:223], a[92:95], a[108:111], a[220:223]\n"
  "ds_read_b64_tr_b16 a[66:67], v12 offset:33792\n"
  "v_mfma_f32_16x16x32_bf16 a[160:163], a[80:83], a[112:115], a[160:163]\n"
  "ds_read_b64_tr_b16 a[68:69], v13 offset:32768\n"
  "v_mfma_f32_16x16x32_bf16 a[164:167], a[84:87], a[112:115], a[164:167]\n"
  "buffer_load_dwordx4 v20, s[24:27], s64 offen offset:0 lds\n"
  "v_mfma_f32_16x16x32_bf16 a[224:227], a[88:91], a[112:115], a[224:227]\n"
  "ds_read_b64_tr_b16 a[70:71], v13 offset:33792\n"
  "v_mfma_f32_16x16x32_bf16 a[228:231], a[92:95], a[112:115], a[228:231]\n"
  "ds_read_b64_tr_b16 a[72:73], v14 offset:32768\n"
  "v_mfma_f32_16x16x32_bf16 a[168:171], a[80:83], a[116:119], a[168:171]\n"
  "ds_read_b64_tr_b16 a[74:75], v14 offset:33792\n"
  "v_mfma_f32_16x16x32_bf16 a[172:175], a[84:87], a[116:119], a[172:175]\n"
  "ds_read_b64_tr_b16 a[76:77], v15 offset:32768\n"
  "v_mfma_f32_16x16x32_bf16 a[232:235], a[88:91], a[116:119], a[232:235]\n"
  "ds_read_b64_tr_b16 a[78:79], v15 offset:33792\n"
  "v_mfma_f32_16x16x32_bf16 a[236:239], a[92:95], a[116:119], a[236:239]\n"
  "buffer_load_dwordx4 v21, s[24:27], s64 offen offset:1024 lds\n"
  "v_mfma_f32_16x16x32_bf16 a[176:179], a[80:83], a[120:123], a[176:179]\n"
  "v_mfma_f32_16x16x32_bf16 a[180:183], a[84:87], a[120:123], a[180:183]\n"
  "v_mfma_f32_16x16x32_bf16 a[240:243], a[88:91], a[120:123], a[240:243]\n"
  "v_mfma_f32_16x16x32_bf16 a[244:247], a[92:95], a[120:123], a[244:247]\n"
  "v_mfma_f32_16x16x32_bf16 a[184:187], a[80:83], a[124:127], a[184:187]\n"
  "v_mfma_f32_16x16x32_bf16 a[188:191], a[84:87], a[124:127], a[188:191]\n"
  "buffer_load_dwordx4 v22, s[24:27], s64 offen offset:2048 lds\n"
  "v_mfma_f32_16x16x32_bf16 a[248:251], a[88:91], a[124:127], a[248:251]\n"
  "v_mfma_f32_16x16x32_bf16 a[252:255], a[92:95], a[124:127], a[252:255]\n"
  "s_waitcnt lgkmcnt(0)\n"
  "v_mfma_f32_16x16x32_bf16 v[128:131], a[48:51], a[32:35], v[128:131]\n"
  "ds_read_b64_tr_b16 a[96:97], v8 offset:49152\n"
  "v_mfma_f32_16x16x32_bf16 v[160:163], a[48:51], a[36:39], v[160:163]\n"
  "ds_read_b64_tr_b16 a[98:99], v8 offset:50176\n"
  "v_mfma_f32_16x16x32_bf16 v[192:195], a[48:51], a[40:43], v[192:195]\n"
  "v_mfma_f32_16x16x32_bf16 v[224:227], a[48:51], a[44:47], v[224:227]\n"
  "ds_read_b64_tr_b16 a[100:101], v9 offset:49152\n"
  "buffer_load_dwordx4 v23, s[24:27], s64 offen offset:3072 lds\n"
  "v_mfma_f32_16x16x32_bf16 v[132:135], a[52:55], a[32:35], v[132:135]\n"
  "ds_read_b64_tr_b16 a[102:103], v9 offset:50176\n"
  "s_add_u32 m0, s62, 114688\n"
  "s_mov_b32 s64, 3145728\n"
  "v_mfma_f32_16x16x32_bf16 v[164:167], a[52:55], a[36:39], v[164:167]\n"
  "v_mfma_f32_16x16x32_bf16 v[196:199], a[52:55], a[40:43], v[196:199]\n"
  "ds_read_b64_tr_b16 a[104:105], v10 offset:49152\n"
  "v_mfma_f32_16x16x32_bf16 v[228:231], a[52:55], a[44:47], v[228:231]\n"
  "ds_read_b64_tr_b16 a[106:107], v10 offset:50176\n"
  "v_mfma_f32_16x16x32_bf16 v[136:139], a[56:59], a[32:35], v[136:139]\n"
  "v_mfma_f32_16x16x32_bf16 v[168:171], a[56:59], a[36:39], v[168:171]\n"
  "ds_read_b64_tr_b16 a[108:109], v11 offset:49152\n"
  "buffer_load_dwordx4 v24, s[36:39], s64 offen offset:0 lds\n"
  "v_mfma_f32_16x16x32_bf16 v[200:203], a[56:59], a[40:43], v[200:203]\n"
  "ds_read_b64_tr_b16 a[110:111], v11 offset:50176\n"
  "v_mfma_f32_16x16x32_bf16 v[232:235], a[56:59], a[44:47], v[232:235]\n"
  "v_mfma_f32_16x16x32_bf16 v[140:143], a[60:63], a[32:35], v[140:143]\n"
  "ds_read_b64_tr_b16 a[112:113], v12 offset:49152\n"
  "v_mfma_f32_16x16x32_bf16 v[172:175], a[60:63], a[36:39], v[172:175]\n"
  "ds_read_b64_tr_b16 a[114:115], v12 offset:50176\n"
  "v_mfma_f32_16x16x32_bf16 v[204:207], a[60:63], a[40:43], v[204:207]\n"
  "v_mfma_f32_16x16x32_bf16 v[236:239], a[60:63], a[44:47], v[236:239]\n"
  "ds_read_b64_tr_b16 a[116:117], v13 offset:49152\n"
  "buffer_load_dwordx4 v25, s[36:39], s64 offen offset:1024 lds\n"
  "v_mfma_f32_16x16x32_bf16 v[144:147], a[64:67], a[32:35], v[144:147]\n"
  "ds_read_b64_tr_b16 a[118:119], v13 offset:50176\n"
  "v_mfma_f32_16x16x32_bf16 v[176:179], a[64:67], a[36:39], v[176:179]\n"
  "v_mfma_f32_16x16x32_bf16 v[208:211], a[64:67], a[40:43], v[208:211]\n"
  "ds_read_b64_tr_b16 a[120:121], v14 offset:49152\n"
  "v_mfma_f32_16x16x32_bf16 v[240:243], a[64:67], a[44:47], v[240:243]\n"
  "ds_read_b64_tr_b16 a[122:123], v14 offset:50176\n"
  "v_mfma_f32_16x16x32_bf16 v[148:151], a[68:71], a[32:35], v[148:151]\n"
  "v_mfma_f32_16x16x32_bf16 v[180:183], a[68:71], a[36:39], v[180:183]\n"
  "ds_read_b64_tr_b16 a[124:125], v15 offset:49152\n"
  "buffer_load_dwordx4 v26, s[36:39], s64 offen offset:2048 lds\n"
  "v_mfma_f32_16x16x32_bf16 v[212:215], a[68:71], a[40:43], v[212:215]\n"
  "ds_read_b64_tr_b16 a[126:127], v15 offset:50176\n"
  "v_mfma_f32_16x16x32_bf16 v[244:247], a[68:71], a[44:47], v[244:247]\n"
  "v_mfma_f32_16x16x32_bf16 v[152:155], a[72:75], a[32:35], v[152:155]\n"
  "v_mfma_f32_16x16x32_bf16 v[184:187], a[72:75], a[36:39], v[184:187]\n"
  "v_mfma_f32_16x16x32_bf16 v[216:219], a[72:75], a[40:43], v[216:219]\n"
  "v_mfma_f32_16x16x32_bf16 v[248:251], a[72:75], a[44:47], v[248:251]\n"
  "buffer_load_dwordx4 v27, s[36:39], s64 offen offset:3072 lds\n"
  "v_mfma_f32_16x16x32_bf16 v[156:159], a[76:79], a[32:35], v[156:159]\n"
  "v_mfma_f32_16x16x32_bf16 v[188:191], a[76:79], a[36:39], v[188:191]\n"
  "v_mfma_f32_16x16x32_bf16 v[220:223], a[76:79], a[40:43], v[220:223]\n"
  "v_mfma_f32_16x16x32_bf16 v[252:255], a[76:79], a[44:47], v[252:255]\n"
  "s_waitcnt lgkmcnt(0)\n"
  "v_mfma_f32_16x16x32_bf16 a[128:131], a[32:35], a[96:99], a[128:131]\n"
  "ds_read_b64_tr_b16 a[48:49], v8 offset:40960\n"
  "v_mfma_f32_16x16x32_bf16 a[132:135], a[36:39], a[96:99], a[132:135]\n"
  "ds_read_b64_tr_b16 a[50:51], v8 offset:41984\n"
  "v_mfma_f32_16x16x32_bf16 a[192:195], a[40:43], a[96:99], a[192:195]\n"
  "ds_read_b128 a[80:83], v3 offset:0\n"
  "v_mfma_f32_16x16x32_bf16 a[196:199], a[44:47], a[96:99], a[196:199]\n"
  "ds_read_b64_tr_b16 a[52:53], v9 offset:40960\n"
  "v_mfma_f32_16x16x32_bf16 a[136:139], a[32:35], a[100:103], a[136:139]\n"
  "ds_read_b64_tr_b16 a[54:55], v9 offset:41984\n"
  "v_mfma_f32_16x16x32_bf16 a[140:143], a[36:39], a[100:103], a[140:143]\n"
  "v_mfma_f32_16x16x32_bf16 a[200:203], a[40:43], a[100:103], a[200:203]\n"
  "ds_read_b128 a[84:87], v3 offset:2048\n"
  "v_mfma_f32_16x16x32_bf16 a[204:207], a[44:47], a[100:103], a[204:207]\n"
  "ds_read_b64_tr_b16 a[56:57], v10 offset:40960\n"
  "v_mfma_f32_16x16x32_bf16 a[144:147], a[32:35], a[104:107], a[144:147]\n"
  "ds_read_b64_tr_b16 a[58:59], v10 offset:41984\n"
  "v_mfma_f32_16x16x32_bf16 a[148:151], a[36:39], a[104:107], a[148:151]\n"
  "ds_read_b128 a[88:91], v3 offset:4096\n"
  "v_mfma_f32_16x16x32_bf16 a[208:211], a[40:43], a[104:107], a[208:211]\n"
  "ds_read_b64_tr_b16 a[60:61], v11 offset:40960\n"
  "v_mfma_f32_16x16x32_bf16 a[212:215], a[44:47], a[104:107], a[212:215]\n"
  "v_mfma_f32_16x16x32_bf16 a[152:155], a[32:35], a[108:111], a[152:155]\n"
  "ds_read_b64_tr_b16 a[62:63], v11 offset:41984\n"
  "v_mfma_f32_16x16x32_bf16 a[156:159], a[36:39], a[108:111], a[156:159]\n"
  "ds_read_b128 a[92:95], v3 offset:6144\n"
  "v_mfma_f32_16x16x32_bf16 a[216:219], a[40:43], a[108:111], a[216:219]\n"
  "ds_read_b64_tr_b16 a[64:65], v12 offset:40960\n"
  "v_mfma_f32_16x16x32_bf16 a[220:223], a[44:47], a[108:111], a[220:223]\n"
  "ds_read_b64_tr_b16 a[66:67], v12 offset:41984\n"
  "v_mfma_f32_16x16x32_bf16 a[160:163], a[32:35], a[112:115], a[160:163]\n"
  "ds_read_b64_tr_b16 a[68:69], v13 offset:40960\n"
  "v_mfma_f32_16x16x32_bf16 a[164:167], a[36:39], a[112:115], a[164:167]\n"
  "v_mfma_f32_16x16x32_bf16 a[224:227], a[40:43], a[112:115], a[224:227]\n"
  "ds_read_b64_tr_b16 a[70:71], v13 offset:41984\n"
  "v_mfma_f32_16x16x32_bf16 a[228:231], a[44:47], a[112:115], a[228:231]\n"
  "ds_read_b64_tr_b16 a[72:73], v14 offset:40960\n"
  "v_mfma_f32_16x16x32_bf16 a[168:171], a[32:35], a[116:119], a[168:171]\n"
  "ds_read_b64_tr_b16 a[74:75], v14 offset:41984\n"
  "v_mfma_f32_16x16x32_bf16 a[172:175], a[36:39], a[116:119], a[172:175]\n"
  "ds_read_b64_tr_b16 a[76:77], v15 offset:40960\n"
  "v_mfma_f32_16x16x32_bf16 a[232:235], a[40:43], a[116:119], a[232:235]\n"
  "ds_read_b64_tr_b16 a[78:79], v15 offset:41984\n"
  "v_mfma_f32_16x16x32_bf16 a[236:239], a[44:47], a[116:119], a[236:239]\n"
  "v_mfma_f32_16x16x32_bf16 a[176:179], a[32:35], a[120:123], a[176:179]\n"
  "v_mfma_f32_16x16x32_bf16 a[180:183], a[36:39], a[120:123], a[180:183]\n"
  "v_mfma_f32_16x16x32_bf16 a[240:243], a[40:43], a[120:123], a[240:243]\n"
  "v_mfma_f32_16x16x32_bf16 a[244:247], a[44:47], a[120:123], a[244:247]\n"
  "v_mfma_f32_16x16x32_bf16 a[184:187], a[32:35], a[124:127], a[184:187]\n"
  "v_mfma_f32_16x16x32_bf16 a[188:191], a[36:39], a[124:127], a[188:191]\n"
  "v_mfma_f32_16x16x32_bf16 a[248:251], a[40:43], a[124:127], a[248:251]\n"
  "v_mfma_f32_16x16x32_bf16 a[252:255], a[44:47], a[124:127], a[252:255]\n"
  "s_waitcnt lgkmcnt(0)\n"
  "s_barrier\n"
  "v_mfma_f32_16x16x32_bf16 v[128:131], a[48:51], a[80:83], v[128:131]\n"
  "ds_read_b64_tr_b16 a[96:97], v8 offset:57344\n"
  "s_add_u32 m0, s60, 0\n"
  "s_mov_b32 s64, 896\n"
  "v_mfma_f32_16x16x32_bf16 v[160:163], a[48:51], a[84:87], v[160:163]\n"
  "ds_read_b64_tr_b16 a[98:99], v8 offset:58368\n"
  "buffer_load_dwordx4 v16, s[24:27], s64 offen offset:0 lds\n"
  "v_mfma_f32_16x16x32_bf16 v[192:195], a[48:51], a[88:91], v[192:195]\n"
  "v_mfma_f32_16x16x32_bf16 v[224:227], a[48:51], a[92:95], v[224:227]\n"
  "ds_read_b64_tr_b16 a[100:101], v9 offset:57344\n"
  "v_mfma_f32_16x16x32_bf16 v[132:135], a[52:55], a[80:83], v[132:135]\n"
  "ds_read_b64_tr_b16 a[102:103], v9 offset:58368\n"
  "v_mfma_f32_16x16x32_bf16 v[164:167], a[52:55], a[84:87], v[164:167]\n"
  "v_mfma_f32_16x16x32_bf16 v[196:199], a[52:55], a[88:91], v[196:199]\n"
  "ds_read_b64_tr_b16 a[104:105], v10 offset:57344\n"
  "v_mfma_f32_16x16x32_bf16 v[228:231], a[52:55], a[92:95], v[228:231]\n"
  "ds_read_b64_tr_b16 a[106:107], v10 offset:58368\n"
  "buffer_load_dwordx4 v17, s[24:27], s64 offen offset:1024 lds\n"
  "v_mfma_f32_16x16x32_bf16 v[136:139], a[56:59], a[80:83], v[136:139]\n"
  "v_mfma_f32_16x16x32_bf16 v[168:171], a[56:59], a[84:87], v[168:171]\n"
  "ds_read_b64_tr_b16 a[108:109], v11 offset:57344\n"
  "v_mfma_f32_16x16x32_bf16 v[200:203], a[56:59], a[88:91], v[200:203]\n"
  "ds_read_b64_tr_b16 a[110:111], v11 offset:58368\n"
  "v_mfma_f32_16x16x32_bf16 v[232:235], a[56:59], a[92:95], v[232:235]\n"
  "v_mfma_f32_16x16x32_bf16 v[140:143], a[60:63], a[80:83], v[140:143]\n"
  "ds_read_b64_tr_b16 a[112:113], v12 offset:57344\n"
  "v_mfma_f32_16x16x32_bf16 v[172:175], a[60:63], a[84:87], v[172:175]\n"
  "ds_read_b64_tr_b16 a[114:115], v12 offset:58368\n"
  "buffer_load_dwordx4 v18, s[24:27], s64 offen offset:2048 lds\n"
  "v_mfma_f32_16x16x32_bf16 v[204:207], a[60:63], a[88:91], v[204:207]\n"
  "v_mfma_f32_16x16x32_bf16 v[236:239], a[60:63], a[92:95], v[236:239]\n"
  "ds_read_b64_tr_b16 a[116:117], v13 offset:57344\n"
  "v_mfma_f32_16x16x32_bf16 v[144:147], a[64:67], a[80:83], v[144:147]\n"
  "ds_read_b64_tr_b16 a[118:119], v13 offset:58368\n"
  "v_mfma_f32_16x16x32_bf16 v[176:179], a[64:67], a[84:87], v[176:179]\n"
  "v_mfma_f32_16x16x32_bf16 v[208:211], a[64:67], a[88:91], v[208:211]\n"
  "ds_read_b64_tr_b16 a[120:121], v14 offset:57344\n"
  "v_mfma_f32_16x16x32_bf16 v[240:243], a[64:67], a[92:95], v[240:243]\n"
  "ds_read_b64_tr_b16 a[122:123], v14 offset:58368\n"
  "buffer_load_dwordx4 v19, s[24:27], s64 offen offset:3072 lds\n"
  "v_mfma_f32_16x16x32_bf16 v[148:151], a[68:71], a[80:83], v[148:151]\n"
  "s_add_u32 m0, s62, 32768\n"
  "s_mov_b32 s64, 3670016\n"
  "v_mfma_f32_16x16x32_bf16 v[180:183], a[68:71], a[84:87], v[180:183]\n"
  "ds_read_b64_tr_b16 a[124:125], v15 offset:57344\n"
  "v_mfma_f32_16x16x32_bf16 v[212:215], a[68:71], a[88:91], v[212:215]\n"
  "ds_read_b64_tr_b16 a[126:127], v15 offset:58368\n"
  "v_mfma_f32_16x16x32_bf16 v[244:247], a[68:71], a[92:95], v[244:247]\n"
  "v_mfma_f32_16x16x32_bf16 v[152:155], a[72:75], a[80:83], v[152:155]\n"
  "v_add_u32 v2, 0x10000, v2\n"
  "v_mfma_f32_16x16x32_bf16 v[184:187], a[72:75], a[84:87], v[184:187]\n"
  "buffer_load_dwordx4 v24, s[28:31], s64 offen offset:0 lds\n"
  "v_add_u32 v3, 0x10000, v3\n"
  "v_mfma_f32_16x16x32_bf16 v[216:219], a[72:75], a[88:91], v[216:219]\n"
  "v_add_u32 v8, 0x10000, v8\n"
  "v_mfma_f32_16x16x32_bf16 v[248:251], a[72:75], a[92:95], v[248:251]\n"
  "v_add_u32 v9, 0x10000, v9\n"
  "v_add_u32 v10, 0x10000, v10\n"
  "v_mfma_f32_16x16x32_bf16 v[156:159], a[76:79], a[80:83], v[156:159]\n"
  "v_add_u32 v11, 0x10000, v11\n"
  "v_mfma_f32_16x16x32_bf16 v[188:191], a[76:79], a[84:87], v[188:191]\n"
  "v_add_u32 v12, 0x10000, v12\n"
  "v_mfma_f32_16x16x32_bf16 v[220:223], a[76:79], a[88:91], v[220:223]\n"
  "v_add_u32 v13, 0x10000, v13\n"
  "v_mfma_f32_16x16x32_bf16 v[252:255], a[76:79], a[92:95], v[252:255]\n"
  "buffer_load_dwordx4 v25, s[28:31], s64 offen offset:1024 lds\n"
  "v_add_u32 v14, 0x10000, v14\n"
  "v_add_u32 v15, 0x10000, v15\n"
  "s_waitcnt vmcnt(6) lgkmcnt(0)\n"
  "s_barrier\n"
  "v_mfma_f32_16x16x32_bf16 a[128:131], a[80:83], a[96:99], a[128:131]\n"
  "ds_read_b64_tr_b16 a[48:49], v8 offset:32768\n"
  "v_mfma_f32_16x16x32_bf16 a[132:135], a[84:87], a[96:99], a[132:135]\n"
  "ds_read_b64_tr_b16 a[50:51], v8 offset:33792\n"
  "v_mfma_f32_16x16x32_bf16 a[192:195], a[88:91], a[96:99], a[192:195]\n"
  "ds_read_b128 a[32:35], v2 offset:0\n"
  "v_mfma_f32_16x16x32_bf16 a[196:199], a[92:95], a[96:99], a[196:199]\n"
  "ds_read_b64_tr_b16 a[52:53], v9 offset:32768\n"
  "v_mfma_f32_16x16x32_bf16 a[136:139], a[80:83], a[100:103], a[136:139]\n"
  "ds_read_b64_tr_b16 a[54:55], v9 offset:33792\n"
  "v_mfma_f32_16x16x32_bf16 a[140:143], a[84:87], a[100:103], a[140:143]\n"
  "buffer_load_dwordx4 v26, s[28:31], s64 offen offset:2048 lds\n"
  "v_mfma_f32_16x16x32_bf16 a[200:203], a[88:91], a[100:103], a[200:203]\n"
  "ds_read_b128 a[36:39], v2 offset:2048\n"
  "v_mfma_f32_16x16x32_bf16 a[204:207], a[92:95], a[100:103], a[204:207]\n"
  "ds_read_b64_tr_b16 a[56:57], v10 offset:32768\n"
  "v_mfma_f32_16x16x32_bf16 a[144:147], a[80:83], a[104:107], a[144:147]\n"
  "ds_read_b64_tr_b16 a[58:59], v10 offset:33792\n"
  "v_mfma_f32_16x16x32_bf16 a[148:151], a[84:87], a[104:107], a[148:151]\n"
  "ds_read_b128 a[40:43], v2 offset:4096\n"
  "v_mfma_f32_16x16x32_bf16 a[208:211], a[88:91], a[104:107], a[208:211]\n"
  "ds_read_b64_tr_b16 a[60:61], v11 offset:32768\n"
  "v_mfma_f32_16x16x32_bf16 a[212:215], a[92:95], a[104:107], a[212:215]\n"
  "buffer_load_dwordx4 v27, s[28:31], s64 offen offset:3072 lds\n"
  "v_mfma_f32_16x16x32_bf16 a[152:155], a[80:83], a[108:111], a[152:155]\n"
  "ds_read_b64_tr_b16 a[62:63], v11 offset:33792\n"
  "s_add_u32 m0, s60, 4096\n"
  "s_mov_b32 s64, 896\n"
  "v_mfma_f32_16x16x32_bf16 a[156:159], a[84:87], a[108:111], a[156:159]\n"
  "ds_read_b128 a[44:47], v2 offset:6144\n"
  "v_mfma_f32_16x16x32_bf16 a[216:219], a[88:91], a[108:111], a[216:219]\n"
  "ds_read_b64_tr_b16 a[64:65], v12 offset:32768\n"
  "buffer_load_dword a0, v70, s[84:87], 0 offen offset:0\n"
  "v_mfma_f32_16x16x32_bf16 a[220:223], a[92:95], a[108:111], a[220:223]\n"
  "ds_read_b64_tr_b16 a[66:67], v12 offset:33792\n"
  "v_mfma_f32_16x16x32_bf16 a[160:163], a[80:83], a[112:115], a[160:163]\n"
  "ds_read_b64_tr_b16 a[68:69], v13 offset:32768\n"
  "v_mfma_f32_16x16x32_bf16 a[164:167], a[84:87], a[112:115], a[164:167]\n"
  "buffer_load_dwordx4 v20, s[24:27], s64 offen offset:0 lds\n"
  "v_mfma_f32_16x16x32_bf16 a[224:227], a[88:91], a[112:115], a[224:227]\n"
  "ds_read_b64_tr_b16 a[70:71], v13 offset:33792\n"
  "v_mfma_f32_16x16x32_bf16 a[228:231], a[92:95], a[112:115], a[228:231]\n"
  "ds_read_b64_tr_b16 a[72:73], v14 offset:32768\n"
  "v_mfma_f32_16x16x32_bf16 a[168:171], a[80:83], a[116:119], a[168:171]\n"
  "ds_read_b64_tr_b16 a[74:75], v14 offset:33792\n"
  "v_mfma_f32_16x16x32_bf16 a[172:175], a[84:87], a[116:119], a[172:175]\n"
  "ds_read_b64_tr_b16 a[76:77], v15 offset:32768\n"
  "v_mfma_f32_16x16x32_bf16 a[232:235], a[88:91], a[116:119], a[232:235]\n"
  "ds_read_b64_tr_b16 a[78:79], v15 offset:33792\n"
  "v_mfma_f32_16x16x32_bf16 a[236:239], a[92:95], a[116:119], a[236:239]\n"
  "buffer_load_dwordx4 v21, s[24:27], s64 offen offset:1024 lds\n"
  "v_mfma_f32_16x16x32_bf16 a[176:179], a[80:83], a[120:123], a[176:179]\n"
  "v_mfma_f32_16x16x32_bf16 a[180:183], a[84:87], a[120:123], a[180:183]\n"
  "v_mfma_f32_16x16x32_bf16 a[240:243], a[88:91], a[120:123], a[240:243]\n"
  "v_mfma_f32_16x16x32_bf16 a[244:247], a[92:95], a[120:123], a[244:247]\n"
  "v_mfma_f32_16x16x32_bf16 a[184:187], a[80:83], a[124:127], a[184:187]\n"
  "v_mfma_f32_16x16x32_bf16 a[188:191], a[84:87], a[124:127], a[188:191]\n"
  "buffer_load_dwordx4 v22, s[24:27], s64 offen offset:2048 lds\n"
  "v_mfma_f32_16x16x32_bf16 a[248:251], a[88:91], a[124:127], a[248:251]\n"
  "v_mfma_f32_16x16x32_bf16 a[252:255], a[92:95], a[124:127], a[252:255]\n"
  "s_waitcnt lgkmcnt(0)\n"
  "v_mfma_f32_16x16x32_bf16 v[128:131], a[48:51], a[32:35], v[128:131]\n"
  "ds_read_b64_tr_b16 a[96:97], v8 offset:49152\n"
  "v_mfma_f32_16x16x32_bf16 v[160:163], a[48:51], a[36:39], v[160:163]\n"
  "ds_read_b64_tr_b16 a[98:99], v8 offset:50176\n"
  "v_mfma_f32_16x16x32_bf16 v[192:195], a[48:51], a[40:43], v[192:195]\n"
  "v_mfma_f32_16x16x32_bf16 v[224:227], a[48:51], a[44:47], v[224:227]\n"
  "ds_read_b64_tr_b16 a[100:101], v9 offset:49152\n"
  "buffer_load_dwordx4 v23, s[24:27], s64 offen offset:3072 lds\n"
  "v_mfma_f32_16x16x32_bf16 v[132:135], a[52:55], a[32:35], v[132:135]\n"
  "ds_read_b64_tr_b16 a[102:103], v9 offset:50176\n"
  "s_add_u32 m0, s62, 49152\n"
  "s_mov_b32 s64, 3670016\n"
  "v_mfma_f32_16x16x32_bf16 v[164:167], a[52:55], a[36:39], v[164:167]\n"
  "v_mfma_f32_16x16x32_bf16 v[196:199], a[52:55], a[40:43], v[196:199]\n"
  "ds_read_b64_tr_b16 a[104:105], v10 offset:49152\n"
  "v_mfma_f32_16x16x32_bf16 v[228:231], a[52:55], a[44:47], v[228:231]\n"
  "ds_read_b64_tr_b16 a[106:107], v10 offset:50176\n"
  "v_mfma_f32_16x16x32_bf16 v[136:139], a[56:59], a[32:35], v[136:139]\n"
  "v_mfma_f32_16x16x32_bf16 v[168:171], a[56:59], a[36:39], v[168:171]\n"
  "ds_read_b64_tr_b16 a[108:109], v11 offset:49152\n"
  "buffer_load_dwordx4 v24, s[36:39], s64 offen offset:0 lds\n"
  "v_mfma_f32_16x16x32_bf16 v[200:203], a[56:59], a[40:43], v[200:203]\n"
  "ds_read_b64_tr_b16 a[110:111], v11 offset:50176\n"
  "v_mfma_f32_16x16x32_bf16 v[232:235], a[56:59], a[44:47], v[232:235]\n"
  "v_mfma_f32_16x16x32_bf16 v[140:143], a[60:63], a[32:35], v[140:143]\n"
  "ds_read_b64_tr_b16 a[112:113], v12 offset:49152\n"
  "v_mfma_f32_16x16x32_bf16 v[172:175], a[60:63], a[36:39], v[172:175]\n"
  "ds_read_b64_tr_b16 a[114:115], v12 offset:50176\n"
  "v_mfma_f32_16x16x32_bf16 v[204:207], a[60:63], a[40:43], v[204:207]\n"
  "v_mfma_f32_16x16x32_bf16 v[236:239], a[60:63], a[44:47], v[236:239]\n"
  "ds_read_b64_tr_b16 a[116:117], v13 offset:49152\n"
  "buffer_load_dwordx4 v25, s[36:39], s64 offen offset:1024 lds\n"
  "v_mfma_f32_16x16x32_bf16 v[144:147], a[64:67], a[32:35], v[144:147]\n"
  "ds_read_b64_tr_b16 a[118:119], v13 offset:50176\n"
  "v_mfma_f32_16x16x32_bf16 v[176:179], a[64:67], a[36:39], v[176:179]\n"
  "v_mfma_f32_16x16x32_bf16 v[208:211], a[64:67], a[40:43], v[208:211]\n"
  "ds_read_b64_tr_b16 a[120:121], v14 offset:49152\n"
  "v_mfma_f32_16x16x32_bf16 v[240:243], a[64:67], a[44:47], v[240:243]\n"
  "ds_read_b64_tr_b16 a[122:123], v14 offset:50176\n"
  "v_mfma_f32_16x16x32_bf16 v[148:151], a[68:71], a[32:35], v[148:151]\n"
  "v_mfma_f32_16x16x32_bf16 v[180:183], a[68:71], a[36:39], v[180:183]\n"
  "ds_read_b64_tr_b16 a[124:125], v15 offset:49152\n"
  "buffer_load_dwordx4 v26, s[36:39], s64 offen offset:2048 lds\n"
  "v_mfma_f32_16x16x32_bf16 v[212:215], a[68:71], a[40:43], v[212:215]\n"
  "ds_read_b64_tr_b16 a[126:127], v15 offset:50176\n"
  "v_mfma_f32_16x16x32_bf16 v[244:247], a[68:71], a[44:47], v[244:247]\n"
  "v_mfma_f32_16x16x32_bf16 v[152:155], a[72:75], a[32:35], v[152:155]\n"
  "v_mfma_f32_16x16x32_bf16 v[184:187], a[72:75], a[36:39], v[184:187]\n"
  "v_mfma_f32_16x16x32_bf16 v[216:219], a[72:75], a[40:43], v[216:219]\n"
  "v_mfma_f32_16x16x32_bf16 v[248:251], a[72:75], a[44:47], v[248:251]\n"
  "buffer_load_dwordx4 v27, s[36:39], s64 offen offset:3072 lds\n"
  "v_mfma_f32_16x16x32_bf16 v[156:159], a[76:79], a[32:35], v[156:159]\n"
  "v_mfma_f32_16x16x32_bf16 v[188:191], a[76:79], a[36:39], v[188:191]\n"
  "v_mfma_f32_16x16x32_bf16 v[220:223], a[76:79], a[40:43], v[220:223]\n"
  "v_mfma_f32_16x16x32_bf16 v[252:255], a[76:79], a[44:47], v[252:255]\n"
  "s_waitcnt lgkmcnt(0)\n"
  "v_mfma_f32_16x16x32_bf16 a[128:131], a[32:35], a[96:99], a[128:131]\n"
  "ds_read_b64_tr_b16 a[48:49], v8 offset:40960\n"
  "v_mfma_f32_16x16x32_bf16 a[132:135], a[36:39], a[96:99], a[132:135]\n"
  "ds_read_b64_tr_b16 a[50:51], v8 offset:41984\n"
  "v_mfma_f32_16x16x32_bf16 a[192:195], a[40:43], a[96:99], a[192:195]\n"
  "ds_read_b128 a[80:83], v3 offset:0\n"
  "v_mfma_f32_16x16x32_bf16 a[196:199], a[44:47], a[96:99], a[196:199]\n"
  "ds_read_b64_tr_b16 a[52:53], v9 offset:40960\n"
  "v_mfma_f32_16x16x32_bf16 a[136:139], a[32:35], a[100:103], a[136:139]\n"
  "ds_read_b64_tr_b16 a[54:55], v9 offset:41984\n"
  "v_mfma_f32_16x16x32_bf16 a[140:143], a[36:39], a[100:103], a[140:143]\n"
  "v_mfma_f32_16x16x32_bf16 a[200:203], a[40:43], a[100:103], a[200:203]\n"
  "ds_read_b128 a[84:87], v3 offset:2048\n"
  "v_mfma_f32_16x16x32_bf16 a[204:207], a[44:47], a[100:103], a[204:207]\n"
  "ds_read_b64_tr_b16 a[56:57], v10 offset:40960\n"
  "v_mfma_f32_16x16x32_bf16 a[144:147], a[32:35], a[104:107], a[144:147]\n"
  "ds_read_b64_tr_b16 a[58:59], v10 offset:41984\n"
  "v_mfma_f32_16x16x32_bf16 a[148:151], a[36:39], a[104:107], a[148:151]\n"
  "ds_read_b128 a[88:91], v3 offset:4096\n"
  "v_mfma_f32_16x16x32_bf16 a[208:211], a[40:43], a[104:107], a[208:211]\n"
  "ds_read_b64_tr_b16 a[60:61], v11 offset:40960\n"
  "v_mfma_f32_16x16x32_bf16 a[212:215], a[44:47], a[104:107], a[212:215]\n"
  "v_mfma_f32_16x16x32_bf16 a[152:155], a[32:35], a[108:111], a[152:155]\n"
  "ds_read_b64_tr_b16 a[62:63], v11 offset:41984\n"
  "v_mfma_f32_16x16x32_bf16 a[156:159], a[36:39], a[108:111], a[156:159]\n"
  "ds_read_b128 a[92:95], v3 offset:6144\n"
  "v_mfma_f32_16x16x32_bf16 a[216:219], a[40:43], a[108:111], a[216:219]\n"
  "ds_read_b64_tr_b16 a[64:65], v12 offset:40960\n"
  "v_mfma_f32_16x16x32_bf16 a[220:223], a[44:47], a[108:111], a[220:223]\n"
  "ds_read_b64_tr_b16 a[66:67], v12 offset:41984\n"
  "v_mfma_f32_16x16x32_bf16 a[160:163], a[32:35], a[112:115], a[160:163]\n"
  "ds_read_b64_tr_b16 a[68:69], v13 offset:40960\n"
  "v_mfma_f32_16x16x32_bf16 a[164:167], a[36:39], a[112:115], a[164:167]\n"
  "v_mfma_f32_16x16x32_bf16 a[224:227], a[40:43], a[112:115], a[224:227]\n"
  "ds_read_b64_tr_b16 a[70:71], v13 offset:41984\n"
  "v_mfma_f32_16x16x32_bf16 a[228:231], a[44:47], a[112:115], a[228:231]\n"
  "ds_read_b64_tr_b16 a[72:73], v14 offset:40960\n"
  "v_mfma_f32_16x16x32_bf16 a[168:171], a[32:35], a[116:119], a[168:171]\n"
  "ds_read_b64_tr_b16 a[74:75], v14 offset:41984\n"
  "v_mfma_f32_16x16x32_bf16 a[172:175], a[36:39], a[116:119], a[172:175]\n"
  "ds_read_b64_tr_b16 a[76:77], v15 offset:40960\n"
  "v_mfma_f32_16x16x32_bf16 a[232:235], a[40:43], a[116:119], a[232:235]\n"
  "ds_read_b64_tr_b16 a[78:79], v15 offset:41984\n"
  "v_mfma_f32_16x16x32_bf16 a[236:239], a[44:47], a[116:119], a[236:239]\n"
  "v_mfma_f32_16x16x32_bf16 a[176:179], a[32:35], a[120:123], a[176:179]\n"
  "v_mfma_f32_16x16x32_bf16 a[180:183], a[36:39], a[120:123], a[180:183]\n"
  "v_mfma_f32_16x16x32_bf16 a[240:243], a[40:43], a[120:123], a[240:243]\n"
  "v_mfma_f32_16x16x32_bf16 a[244:247], a[44:47], a[120:123], a[244:247]\n"
  "v_mfma_f32_16x16x32_bf16 a[184:187], a[32:35], a[124:127], a[184:187]\n"
  "v_mfma_f32_16x16x32_bf16 a[188:191], a[36:39], a[124:127], a[188:191]\n"
  "v_mfma_f32_16x16x32_bf16 a[248:251], a[40:43], a[124:127], a[248:251]\n"
  "v_mfma_f32_16x16x32_bf16 a[252:255], a[44:47], a[124:127], a[252:255]\n"
  "s_waitcnt lgkmcnt(0)\n"
  "s_barrier\n"
  "v_mfma_f32_16x16x32_bf16 v[128:131], a[48:51], a[80:83], v[128:131]\n"
  "ds_read_b64_tr_b16 a[96:97], v8 offset:57344\n"
  "v_mfma_f32_16x16x32_bf16 v[160:163], a[48:51], a[84:87], v[160:163]\n"
  "ds_read_b64_tr_b16 a[98:99], v8 offset:58368\n"
  "v_mfma_f32_16x16x32_bf16 v[192:195], a[48:51], a[88:91], v[192:195]\n"
  "v_mfma_f32_16x16x32_bf16 v[224:227], a[48:51], a[92:95], v[224:227]\n"
  "ds_read_b64_tr_b16 a[100:101], v9 offset:57344\n"
  "v_mfma_f32_16x16x32_bf16 v[132:135], a[52:55], a[80:83], v[132:135]\n"
  "ds_read_b64_tr_b16 a[102:103], v9 offset:58368\n"
  "v_mfma_f32_16x16x32_bf16 v[164:167], a[52:55], a[84:87], v[164:167]\n"
  "v_mfma_f32_16x16x32_bf16 v[196:199], a[52:55], a[88:91], v[196:199]\n"
  "ds_read_b64_tr_b16 a[104:105], v10 offset:57344\n"
  "v_mfma_f32_16x16x32_bf16 v[228:231], a[52:55], a[92:95], v[228:231]\n"
  "ds_read_b64_tr_b16 a[106:107], v10 offset:58368\n"
  "v_mfma_f32_16x16x32_bf16 v[136:139], a[56:59], a[80:83], v[136:139]\n"
  "v_mfma_f32_16x16x32_bf16 v[168:171], a[56:59], a[84:87], v[168:171]\n"
  "ds_read_b64_tr_b16 a[108:109], v11 offset:57344\n"
  "v_mfma_f32_16x16x32_bf16 v[200:203], a[56:59], a[88:91], v[200:203]\n"
  "ds_read_b64_tr_b16 a[110:111], v11 offset:58368\n"
  "v_mfma_f32_16x16x32_bf16 v[232:235], a[56:59], a[92:95], v[232:235]\n"
  "v_mfma_f32_16x16x32_bf16 v[140:143], a[60:63], a[80:83], v[140:143]\n"
  "ds_read_b64_tr_b16 a[112:113], v12 offset:57344\n"
  "v_mfma_f32_16x16x32_bf16 v[172:175], a[60:63], a[84:87], v[172:175]\n"
  "ds_read_b64_tr_b16 a[114:115], v12 offset:58368\n"
  "v_mfma_f32_16x16x32_bf16 v[204:207], a[60:63], a[88:91], v[204:207]\n"
  "v_mfma_f32_16x16x32_bf16 v[236:239], a[60:63], a[92:95], v[236:239]\n"
  "ds_read_b64_tr_b16 a[116:117], v13 offset:57344\n"
  "v_mfma_f32_16x16x32_bf16 v[144:147], a[64:67], a[80:83], v[144:147]\n"
  "ds_read_b64_tr_b16 a[118:119], v13 offset:58368\n"
  "v_mfma_f32_16x16x32_bf16 v[176:179], a[64:67], a[84:87], v[176:179]\n"
  "v_mfma_f32_16x16x32_bf16 v[208:211], a[64:67], a[88:91], v[208:211]\n"
  "ds_read_b64_tr_b16 a[120:121], v14 offset:57344\n"
  "v_mfma_f32_16x16x32_bf16 v[240:243], a[64:67], a[92:95], v[240:243]\n"
  "ds_read_b64_tr_b16 a[122:123], v14 offset:58368\n"
  "v_mfma_f32_16x16x32_bf16 v[148:151], a[68:71], a[80:83], v[148:151]\n"
  "v_mfma_f32_16x16x32_bf16 v[180:183], a[68:71], a[84:87], v[180:183]\n"
  "ds_read_b64_tr_b16 a[124:125], v15 offset:57344\n"
  "v_mfma_f32_16x16x32_bf16 v[212:215], a[68:71], a[88:91], v[212:215]\n"
  "ds_read_b64_tr_b16 a[126:127], v15 offset:58368\n"
  "v_mfma_f32_16x16x32_bf16 v[244:247], a[68:71], a[92:95], v[244:247]\n"
  "v_mfma_f32_16x16x32_bf16 v[152:155], a[72:75], a[80:83], v[152:155]\n"
  "v_add_u32 v2, 0xffff0000, v2\n"
  "v_mfma_f32_16x16x32_bf16 v[184:187], a[72:75], a[84:87], v[184:187]\n"
  "v_add_u32 v3, 0xffff0000, v3\n"
  "v_mfma_f32_16x16x32_bf16 v[216:219], a[72:75], a[88:91], v[216:219]\n"
  "v_add_u32 v8, 0xffff0000, v8\n"
  "v_mfma_f32_16x16x32_bf16 v[248:251], a[72:75], a[92:95], v[248:251]\n"
  "v_add_u32 v9, 0xffff0000, v9\n"
  "v_add_u32 v10, 0xffff0000, v10\n"
  "v_mfma_f32_16x16x32_bf16 v[156:159], a[76:79], a[80:83], v[156:159]\n"
  "v_add_u32 v11, 0xffff0000, v11\n"
  "v_mfma_f32_16x16x32_bf16 v[188:191], a[76:79], a[84:87], v[188:191]\n"
  "v_add_u32 v12, 0xffff0000, v12\n"
  "v_mfma_f32_16x16x32_bf16 v[220:223], a[76:79], a[88:91], v[220:223]\n"
  "v_add_u32 v13, 0xffff0000, v13\n"
  "v_mfma_f32_16x16x32_bf16 v[252:255], a[76:79], a[92:95], v[252:255]\n"
  "v_add_u32 v14, 0xffff0000, v14\n"
  "v_add_u32 v15, 0xffff0000, v15\n"
  "s_waitcnt vmcnt(0) lgkmcnt(0)\n"
  "s_barrier\n"
  "buffer_atomic_add v7, v6, s[92:95], s69 offen sc1\n"
  "v_mfma_f32_16x16x32_bf16 a[128:131], a[80:83], a[96:99], a[128:131]\n"
  "ds_read_b64_tr_b16 a[48:49], v8 offset:32768\n"
  "s_add_u32 m0, s60, 114688\n"
  "s_mov_b32 s64, 0\n"
  "v_mfma_f32_16x16x32_bf16 a[132:135], a[84:87], a[96:99], a[132:135]\n"
  "ds_read_b64_tr_b16 a[50:51], v8 offset:33792\n"
  "buffer_load_dwordx4 v28, s[40:43], s64 offen offset:0 lds\n"
  "v_mfma_f32_16x16x32_bf16 a[192:195], a[88:91], a[96:99], a[192:195]\n"
  "ds_read_b128 a[32:35], v2 offset:0\n"
  "v_mfma_f32_16x16x32_bf16 a[196:199], a[92:95], a[96:99], a[196:199]\n"
  "ds_read_b64_tr_b16 a[52:53], v9 offset:32768\n"
  "v_mfma_f32_16x16x32_bf16 a[136:139], a[80:83], a[100:103], a[136:139]\n"
  "ds_read_b64_tr_b16 a[54:55], v9 offset:33792\n"
  "v_mfma_f32_16x16x32_bf16 a[140:143], a[84:87], a[100:103], a[140:143]\n"
  "v_mfma_f32_16x16x32_bf16 a[200:203], a[88:91], a[100:103], a[200:203]\n"
  "ds_read_b128 a[36:39], v2 offset:2048\n"
  "v_mfma_f32_16x16x32_bf16 a[204:207], a[92:95], a[100:103], a[204:207]\n"
  "ds_read_b64_tr_b16 a[56:57], v10 offset:32768\n"
  "buffer_load_dwordx4 v29, s[40:43], s64 offen offset:1024 lds\n"
  "v_mfma_f32_16x16x32_bf16 a[144:147], a[80:83], a[104:107], a[144:147]\n"
  "ds_read_b64_tr_b16 a[58:59], v10 offset:33792\n"
  "v_mfma_f32_16x16x32_bf16 a[148:151], a[84:87], a[104:107], a[148:151]\n"
  "ds_read_b128 a[40:43], v2 offset:4096\n"
  "v_mfma_f32_16x16x32_bf16 a[208:211], a[88:91], a[104:107], a[208:211]\n"
  "ds_read_b64_tr_b16 a[60:61], v11 offset:32768\n"
  "v_mfma_f32_16x16x32_bf16 a[212:215], a[92:95], a[104:107], a[212:215]\n"
  "v_mfma_f32_16x16x32_bf16 a[152:155], a[80:83], a[108:111], a[152:155]\n"
  "ds_read_b64_tr_b16 a[62:63], v11 offset:33792\n"
  "v_mfma_f32_16x16x32_bf16 a[156:159], a[84:87], a[108:111], a[156:159]\n"
  "ds_read_b128 a[44:47], v2 offset:6144\n"
  "buffer_load_dwordx4 v30, s[40:43], s64 offen offset:2048 lds\n"
  "v_mfma_f32_16x16x32_bf16 a[216:219], a[88:91], a[108:111], a[216:219]\n"
  "ds_read_b64_tr_b16 a[64:65], v12 offset:32768\n"
  "v_mfma_f32_16x16x32_bf16 a[220:223], a[92:95], a[108:111], a[220:223]\n"
  "ds_read_b64_tr_b16 a[66:67], v12 offset:33792\n"
  "v_mfma_f32_16x16x32_bf16 a[160:163], a[80:83], a[112:115], a[160:163]\n"
  "ds_read_b64_tr_b16 a[68:69], v13 offset:32768\n"
  "v_mfma_f32_16x16x32_bf16 a[164:167], a[84:87], a[112:115], a[164:167]\n"
  "v_mfma_f32_16x16x32_bf16 a[224:227], a[88:91], a[112:115], a[224:227]\n"
  "ds_read_b64_tr_b16 a[70:71], v13 offset:33792\n"
  "v_mfma_f32_16x16x32_bf16 a[228:231], a[92:95], a[112:115], a[228:231]\n"
  "ds_read_b64_tr_b16 a[72:73], v14 offset:32768\n"
  "buffer_load_dwordx4 v31, s[40:43], s64 offen offset:3072 lds\n"
  "v_mfma_f32_16x16x32_bf16 a[168:171], a[80:83], a[116:119], a[168:171]\n"
  "ds_read_b64_tr_b16 a[74:75], v14 offset:33792\n"
  "s_add_u32 m0, s62, 147456\n"
  "s_mov_b32 s64, 0\n"
  "v_mfma_f32_16x16x32_bf16 a[172:175], a[84:87], a[116:119], a[172:175]\n"
  "ds_read_b64_tr_b16 a[76:77], v15 offset:32768\n"
  "v_mfma_f32_16x16x32_bf16 a[232:235], a[88:91], a[116:119], a[232:235]\n"
  "ds_read_b64_tr_b16 a[78:79], v15 offset:33792\n"
  "v_mfma_f32_16x16x32_bf16 a[236:239], a[92:95], a[116:119], a[236:239]\n"
  "v_mfma_f32_16x16x32_bf16 a[176:179], a[80:83], a[120:123], a[176:179]\n"
  "v_mfma_f32_16x16x32_bf16 a[180:183], a[84:87], a[120:123], a[180:183]\n"
  "buffer_load_dwordx4 v24, s[44:47], s64 offen offset:0 lds\n"
  "v_mfma_f32_16x16x32_bf16 a[240:243], a[88:91], a[120:123], a[240:243]\n"
  "v_mfma_f32_16x16x32_bf16 a[244:247], a[92:95], a[120:123], a[244:247]\n"
  "v_mfma_f32_16x16x32_bf16 a[184:187], a[80:83], a[124:127], a[184:187]\n"
  "v_mfma_f32_16x16x32_bf16 a[188:191], a[84:87], a[124:127], a[188:191]\n"
  "v_mfma_f32_16x16x32_bf16 a[248:251], a[88:91], a[124:127], a[248:251]\n"
  "v_mfma_f32_16x16x32_bf16 a[252:255], a[92:95], a[124:127], a[252:255]\n"
  "buffer_load_dwordx4 v25, s[44:47], s64 offen offset:1024 lds\n"
  "s_waitcnt lgkmcnt(0)\n"
  "v_mfma_f32_16x16x32_bf16 v[128:131], a[48:51], a[32:35], v[128:131]\n"
  "ds_read_b64_tr_b16 a[96:97], v8 offset:49152\n"
  "v_mfma_f32_16x16x32_bf16 v[160:163], a[48:51], a[36:39], v[160:163]\n"
  "ds_read_b64_tr_b16 a[98:99], v8 offset:50176\n"
  "v_mfma_f32_16x16x32_bf16 v[192:195], a[48:51], a[40:43], v[192:195]\n"
  "v_mfma_f32_16x16x32_bf16 v[224:227], a[48:51], a[44:47], v[224:227]\n"
  "ds_read_b64_tr_b16 a[100:101], v9 offset:49152\n"
  "v_mfma_f32_16x16x32_bf16 v[132:135], a[52:55], a[32:35], v[132:135]\n"
  "ds_read_b64_tr_b16 a[102:103], v9 offset:50176\n"
  "v_mfma_f32_16x16x32_bf16 v[164:167], a[52:55], a[36:39], v[164:167]\n"
  "buffer_load_dwordx4 v26, s[44:47], s64 offen offset:2048 lds\n"
  "v_mfma_f32_16x16x32_bf16 v[196:199], a[52:55], a[40:43], v[196:199]\n"
  "ds_read_b64_tr_b16 a[104:105], v10 offset:49152\n"
  "v_mfma_f32_16x16x32_bf16 v[228:231], a[52:55], a[44:47], v[228:231]\n"
  "ds_read_b64_tr_b16 a[106:107], v10 offset:50176\n"
  "v_mfma_f32_16x16x32_bf16 v[136:139], a[56:59], a[32:35], v[136:139]\n"
  "v_mfma_f32_16x16x32_bf16 v[168:171], a[56:59], a[36:39], v[168:171]\n"
  "ds_read_b64_tr_b16 a[108:109], v11 offset:49152\n"
  "v_mfma_f32_16x16x32_bf16 v[200:203], a[56:59], a[40:43], v[200:203]\n"
  "ds_read_b64_tr_b16 a[110:111], v11 offset:50176\n"
  "v_mfma_f32_16x16x32_bf16 v[232:235], a[56:59], a[44:47], v[232:235]\n"
  "buffer_load_dwordx4 v27, s[44:47], s64 offen offset:3072 lds\n"
  "v_mfma_f32_16x16x32_bf16 v[140:143], a[60:63], a[32:35], v[140:143]\n"
  "ds_read_b64_tr_b16 a[112:113], v12 offset:49152\n"
  "s_add_u32 m0, s60, 118784\n"
  "s_mov_b32 s64, 0\n"
  "v_mfma_f32_16x16x32_bf16 v[172:175], a[60:63], a[36:39], v[172:175]\n"
  "ds_read_b64_tr_b16 a[114:115], v12 offset:50176\n"
  "v_mfma_f32_16x16x32_bf16 v[204:207], a[60:63], a[40:43], v[204:207]\n"
  "v_mfma_f32_16x16x32_bf16 v[236:239], a[60:63], a[44:47], v[236:239]\n"
  "ds_read_b64_tr_b16 a[116:117], v13 offset:49152\n"
  "v_mfma_f32_16x16x32_bf16 v[144:147], a[64:67], a[32:35], v[144:147]\n"
  "ds_read_b64_tr_b16 a[118:119], v13 offset:50176\n"
  "v_mfma_f32_16x16x32_bf16 v[176:179], a[64:67], a[36:39], v[176:179]\n"
  "buffer_load_dwordx4 v32, s[40:43], s64 offen offset:0 lds\n"
  "v_mfma_f32_16x16x32_bf16 v[208:211], a[64:67], a[40:43], v[208:211]\n"
  "ds_read_b64_tr_b16 a[120:121], v14 offset:49152\n"
  "v_mfma_f32_16x16x32_bf16 v[240:243], a[64:67], a[44:47], v[240:243]\n"
  "ds_read_b64_tr_b16 a[122:123], v14 offset:50176\n"
  "v_mfma_f32_16x16x32_bf16 v[148:151], a[68:71], a[32:35], v[148:151]\n"
  "v_mfma_f32_16x16x32_bf16 v[180:183], a[68:71], a[36:39], v[180:183]\n"
  "ds_read_b64_tr_b16 a[124:125], v15 offset:49152\n"
  "v_mfma_f32_16x16x32_bf16 v[212:215], a[68:71], a[40:43], v[212:215]\n"
  "ds_read_b64_tr_b16 a[126:127], v15 offset:50176\n"
  "v_mfma_f32_16x16x32_bf16 v[244:247], a[68:71], a[44:47], v[244:247]\n"
  "buffer_load_dwordx4 v33, s[40:43], s64 offen offset:1024 lds\n"
  "v_mfma_f32_16x16x32_bf16 v[152:155], a[72:75], a[32:35], v[152:155]\n"
  "v_mfma_f32_16x16x32_bf16 v[184:187], a[72:75], a[36:39], v[184:187]\n"
  "v_mfma_f32_16x16x32_bf16 v[216:219], a[72:75], a[40:43], v[216:219]\n"
  "v_mfma_f32_16x16x32_bf16 v[248:251], a[72:75], a[44:47], v[248:251]\n"
  "v_mfma_f32_16x16x32_bf16 v[156:159], a[76:79], a[32:35], v[156:159]\n"
  "v_mfma_f32_16x16x32_bf16 v[188:191], a[76:79], a[36:39], v[188:191]\n"
  "buffer_load_dwordx4 v34, s[40:43], s64 offen offset:2048 lds\n"
  "v_mfma_f32_16x16x32_bf16 v[220:223], a[76:79], a[40:43], v[220:223]\n"
  "v_mfma_f32_16x16x32_bf16 v[252:255], a[76:79], a[44:47], v[252:255]\n"
  "s_waitcnt lgkmcnt(0)\n"
  "v_mfma_f32_16x16x32_bf16 a[128:131], a[32:35], a[96:99], a[128:131]\n"
  "ds_read_b64_tr_b16 a[48:49], v8 offset:40960\n"
  "v_mfma_f32_16x16x32_bf16 a[132:135], a[36:39], a[96:99], a[132:135]\n"
  "ds_read_b64_tr_b16 a[50:51], v8 offset:41984\n"
  "v_mfma_f32_16x16x32_bf16 a[192:195], a[40:43], a[96:99], a[192:195]\n"
  "ds_read_b128 a[80:83], v3 offset:0\n"
  "v_mfma_f32_16x16x32_bf16 a[196:199], a[44:47], a[96:99], a[196:199]\n"
  "ds_read_b64_tr_b16 a[52:53], v9 offset:40960\n"
  "buffer_load_dwordx4 v35, s[40:43], s64 offen offset:3072 lds\n"
  "v_mfma_f32_16x16x32_bf16 a[136:139], a[32:35], a[100:103], a[136:139]\n"
  "ds_read_b64_tr_b16 a[54:55], v9 offset:41984\n"
  "v_mfma_f32_16x16x32_bf16 a[140:143], a[36:39], a[100:103], a[140:143]\n"
  "v_mfma_f32_16x16x32_bf16 a[200:203], a[40:43], a[100:103], a[200:203]\n"
  "ds_read_b128 a[84:87], v3 offset:2048\n"
  "v_mfma_f32_16x16x32_bf16 a[204:207], a[44:47], a[100:103], a[204:207]\n"
  "ds_read_b64_tr_b16 a[56:57], v10 offset:40960\n"
  "v_mfma_f32_16x16x32_bf16 a[144:147], a[32:35], a[104:107], a[144:147]\n"
  "ds_read_b64_tr_b16 a[58:59], v10 offset:41984\n"
  "v_mfma_f32_16x16x32_bf16 a[148:151], a[36:39], a[104:107], a[148:151]\n"
  "ds_read_b128 a[88:91], v3 offset:4096\n"
  "v_mfma_f32_16x16x32_bf16 a[208:211], a[40:43], a[104:107], a[208:211]\n"
  "ds_read_b64_tr_b16 a[60:61], v11 offset:40960\n"
  "v_mfma_f32_16x16x32_bf16 a[212:215], a[44:47], a[104:107], a[212:215]\n"
  "v_mfma_f32_16x16x32_bf16 a[152:155], a[32:35], a[108:111], a[152:155]\n"
  "ds_read_b64_tr_b16 a[62:63], v11 offset:41984\n"
  "v_mfma_f32_16x16x32_bf16 a[156:159], a[36:39], a[108:111], a[156:159]\n"
  "ds_read_b128 a[92:95], v3 offset:6144\n"
  "v_mfma_f32_16x16x32_bf16 a[216:219], a[40:43], a[108:111], a[216:219]\n"
  "ds_read_b64_tr_b16 a[64:65], v12 offset:40960\n"
  "v_mfma_f32_16x16x32_bf16 a[220:223], a[44:47], a[108:111], a[220:223]\n"
  "ds_read_b64_tr_b16 a[66:67], v12 offset:41984\n"
  "v_mfma_f32_16x16x32_bf16 a[160:163], a[32:35], a[112:115], a[160:163]\n"
  "ds_read_b64_tr_b16 a[68:69], v13 offset:40960\n"
  "v_mfma_f32_16x16x32_bf16 a[164:167], a[36:39], a[112:115], a[164:167]\n"
  "v_mfma_f32_16x16x32_bf16 a[224:227], a[40:43], a[112:115], a[224:227]\n"
  "ds_read_b64_tr_b16 a[70:71], v13 offset:41984\n"
  "v_mfma_f32_16x16x32_bf16 a[228:231], a[44:47], a[112:115], a[228:231]\n"
  "ds_read_b64_tr_b16 a[72:73], v14 offset:40960\n"
  "v_mfma_f32_16x16x32_bf16 a[168:171], a[32:35], a[116:119], a[168:171]\n"
  "ds_read_b64_tr_b16 a[74:75], v14 offset:41984\n"
  "v_mfma_f32_16x16x32_bf16 a[172:175], a[36:39], a[116:119], a[172:175]\n"
  "ds_read_b64_tr_b16 a[76:77], v15 offset:40960\n"
  "v_mfma_f32_16x16x32_bf16 a[232:235], a[40:43], a[116:119], a[232:235]\n"
  "ds_read_b64_tr_b16 a[78:79], v15 offset:41984\n"
  "v_mfma_f32_16x16x32_bf16 a[236:239], a[44:47], a[116:119], a[236:239]\n"
  "v_mfma_f32_16x16x32_bf16 a[176:179], a[32:35], a[120:123], a[176:179]\n"
  "v_mfma_f32_16x16x32_bf16 a[180:183], a[36:39], a[120:123], a[180:183]\n"
  "v_mfma_f32_16x16x32_bf16 a[240:243], a[40:43], a[120:123], a[240:243]\n"
  "v_mfma_f32_16x16x32_bf16 a[244:247], a[44:47], a[120:123], a[244:247]\n"
  "v_mfma_f32_16x16x32_bf16 a[184:187], a[32:35], a[124:127], a[184:187]\n"
  "v_mfma_f32_16x16x32_bf16 a[188:191], a[36:39], a[124:127], a[188:191]\n"
  "v_mfma_f32_16x16x32_bf16 a[248:251], a[40:43], a[124:127], a[248:251]\n"
  "v_mfma_f32_16x16x32_bf16 a[252:255], a[44:47], a[124:127], a[252:255]\n"
  "s_waitcnt lgkmcnt(0)\n"
  "s_barrier\n"
  "v_mfma_f32_16x16x32_bf16 v[128:131], a[48:51], a[80:83], v[128:131]\n"
  "ds_read_b64_tr_b16 a[96:97], v8 offset:57344\n"
  "v_mfma_f32_16x16x32_bf16 v[160:163], a[48:51], a[84:87], v[160:163]\n"
  "ds_read_b64_tr_b16 a[98:99], v8 offset:58368\n"
  "v_mfma_f32_16x16x32_bf16 v[192:195], a[48:51], a[88:91], v[192:195]\n"
  "v_mfma_f32_16x16x32_bf16 v[224:227], a[48:51], a[92:95], v[224:227]\n"
  "ds_read_b64_tr_b16 a[100:101], v9 offset:57344\n"
  "v_mfma_f32_16x16x32_bf16 v[132:135], a[52:55], a[80:83], v[132:135]\n"
  "ds_read_b64_tr_b16 a[102:103], v9 offset:58368\n"
  "v_mfma_f32_16x16x32_bf16 v[164:167], a[52:55], a[84:87], v[164:167]\n"
  "v_mfma_f32_16x16x32_bf16 v[196:199], a[52:55], a[88:91], v[196:199]\n"
  "ds_read_b64_tr_b16 a[104:105], v10 offset:57344\n"
  "v_mfma_f32_16x16x32_bf16 v[228:231], a[52:55], a[92:95], v[228:231]\n"
  "ds_read_b64_tr_b16 a[106:107], v10 offset:58368\n"
  "v_mfma_f32_16x16x32_bf16 v[136:139], a[56:59], a[80:83], v[136:139]\n"
  "v_mfma_f32_16x16x32_bf16 v[168:171], a[56:59], a[84:87], v[168:171]\n"
  "ds_read_b64_tr_b16 a[108:109], v11 offset:57344\n"
  "v_mfma_f32_16x16x32_bf16 v[200:203], a[56:59], a[88:91], v[200:203]\n"
  "ds_read_b64_tr_b16 a[110:111], v11 offset:58368\n"
  "v_mfma_f32_16x16x32_bf16 v[232:235], a[56:59], a[92:95], v[232:235]\n"
  "v_mfma_f32_16x16x32_bf16 v[140:143], a[60:63], a[80:83], v[140:143]\n"
  "ds_read_b64_tr_b16 a[112:113], v12 offset:57344\n"
  "v_mfma_f32_16x16x32_bf16 v[172:175], a[60:63], a[84:87], v[172:175]\n"
  "ds_read_b64_tr_b16 a[114:115], v12 offset:58368\n"
  "v_mfma_f32_16x16x32_bf16 v[204:207], a[60:63], a[88:91], v[204:207]\n"
  "v_mfma_f32_16x16x32_bf16 v[236:239], a[60:63], a[92:95], v[236:239]\n"
  "ds_read_b64_tr_b16 a[116:117], v13 offset:57344\n"
  "v_mfma_f32_16x16x32_bf16 v[144:147], a[64:67], a[80:83], v[144:147]\n"
  "ds_read_b64_tr_b16 a[118:119], v13 offset:58368\n"
  "v_mfma_f32_16x16x32_bf16 v[176:179], a[64:67], a[84:87], v[176:179]\n"
  "v_mfma_f32_16x16x32_bf16 v[208:211], a[64:67], a[88:91], v[208:211]\n"
  "ds_read_b64_tr_b16 a[120:121], v14 offset:57344\n"
  "v_mfma_f32_16x16x32_bf16 v[240:243], a[64:67], a[92:95], v[240:243]\n"
  "ds_read_b64_tr_b16 a[122:123], v14 offset:58368\n"
  "v_mfma_f32_16x16x32_bf16 v[148:151], a[68:71], a[80:83], v[148:151]\n"
  "v_mfma_f32_16x16x32_bf16 v[180:183], a[68:71], a[84:87], v[180:183]\n"
  "ds_read_b64_tr_b16 a[124:125], v15 offset:57344\n"
  "v_mfma_f32_16x16x32_bf16 v[212:215], a[68:71], a[88:91], v[212:215]\n"
  "ds_read_b64_tr_b16 a[126:127], v15 offset:58368\n"
  "v_mfma_f32_16x16x32_bf16 v[244:247], a[68:71], a[92:95], v[244:247]\n"
  "v_mfma_f32_16x16x32_bf16 v[152:155], a[72:75], a[80:83], v[152:155]\n"
  "v_add_u32 v2, 0x1c000, v2\n"
  "v_mfma_f32_16x16x32_bf16 v[184:187], a[72:75], a[84:87], v[184:187]\n"
  "v_add_u32 v3, 0x1c000, v3\n"
  "v_mfma_f32_16x16x32_bf16 v[216:219], a[72:75], a[88:91], v[216:219]\n"
  "v_add_u32 v8, 0x1c000, v8\n"
  "v_mfma_f32_16x16x32_bf16 v[248:251], a[72:75], a[92:95], v[248:251]\n"
  "v_add_u32 v9, 0x1c000, v9\n"
  "v_add_u32 v10, 0x1c000, v10\n"
  "v_mfma_f32_16x16x32_bf16 v[156:159], a[76:79], a[80:83], v[156:159]\n"
  "v_add_u32 v11, 0x1c000, v11\n"
  "v_mfma_f32_16x16x32_bf16 v[188:191], a[76:79], a[84:87], v[188:191]\n"
  "v_add_u32 v12, 0x1c000, v12\n"
  "v_mfma_f32_16x16x32_bf16 v[220:223], a[76:79], a[88:91], v[220:223]\n"
  "v_add_u32 v13, 0x1c000, v13\n"
  "v_mfma_f32_16x16x32_bf16 v[252:255], a[76:79], a[92:95], v[252:255]\n"
  "v_add_u32 v14, 0x1c000, v14\n"
  "v_add_u32 v15, 0x1c000, v15\n"
  "s_waitcnt vmcnt(0) lgkmcnt(0)\n"
  "s_barrier\n"
  "buffer_atomic_add v7, v6, s[92:95], s69 offen sc1\n"
  "v_mfma_f32_16x16x32_bf16 a[128:131], a[80:83], a[96:99], a[128:131]\n"
  "ds_read_b64_tr_b16 a[48:49], v8 offset:32768\n"
  "s_add_u32 m0, s60, 16384\n"
  "s_mov_b32 s64, 128\n"
  "v_mfma_f32_16x16x32_bf16 a[132:135], a[84:87], a[96:99], a[132:135]\n"
  "ds_read_b64_tr_b16 a[50:51], v8 offset:33792\n"
  "buffer_load_dwordx4 v28, s[40:43], s64 offen offset:0 lds\n"
  "v_mfma_f32_16x16x32_bf16 a[192:195], a[88:91], a[96:99], a[192:195]\n"
  "ds_read_b128 a[32:35], v2 offset:0\n"
  "v_cvt_pk_bf16_f32 v64, v128, v129\n"
  "v_mfma_f32_16x16x32_bf16 a[196:199], a[92:95], a[96:99], a[196:199]\n"
  "ds_read_b64_tr_b16 a[52:53], v9 offset:32768\n"
  "v_cvt_pk_bf16_f32 v65, v130, v131\n"
  "v_cvt_pk_bf16_f32 v66, v132, v133\n"
  "v_mfma_f32_16x16x32_bf16 a[136:139], a[80:83], a[100:103], a[136:139]\n"
  "ds_read_b64_tr_b16 a[54:55], v9 offset:33792\n"
  "v_cvt_pk_bf16_f32 v67, v134, v135\n"
  "v_cvt_pk_bf16_f32 v68, v160, v161\n"
  "v_mfma_f32_16x16x32_bf16 a[140:143], a[84:87], a[100:103], a[140:143]\n"
  "v_cvt_pk_bf16_f32 v69, v162, v163\n"
  "v_cvt_pk_bf16_f32 v70, v164, v165\n"
  "v_mfma_f32_16x16x32_bf16 a[200:203], a[88:91], a[100:103], a[200:203]\n"
  "ds_read_b128 a[36:39], v2 offset:2048\n"
  "v_cvt_pk_bf16_f32 v71, v166, v167\n"
  "v_cvt_pk_bf16_f32 v72, v136, v137\n"
  "v_mfma_f32_16x16x32_bf16 a[204:207], a[92:95], a[100:103], a[204:207]\n"
  "ds_read_b64_tr_b16 a[56:57], v10 offset:32768\n"
  "buffer_load_dwordx4 v29, s[40:43], s64 offen offset:1024 lds\n"
  "v_cvt_pk_bf16_f32 v73, v138, v139\n"
  "v_cvt_pk_bf16_f32 v74, v140, v141\n"
  "v_mfma_f32_16x16x32_bf16 a[144:147], a[80:83], a[104:107], a[144:147]\n"
  "ds_read_b64_tr_b16 a[58:59], v10 offset:33792\n"
  "v_cvt_pk_bf16_f32 v75, v142, v143\n"
  "v_cvt_pk_bf16_f32 v76, v168, v169\n"
  "v_mfma_f32_16x16x32_bf16 a[148:151], a[84:87], a[104:107], a[148:151]\n"
  "ds_read_b128 a[40:43], v2 offset:4096\n"
  "v_cvt_pk_bf16_f32 v77, v170, v171\n"
  "v_cvt_pk_bf16_f32 v78, v172, v173\n"
  "v_mfma_f32_16x16x32_bf16 a[208:211], a[88:91], a[104:107], a[208:211]\n"
  "ds_read_b64_tr_b16 a[60:61], v11 offset:32768\n"
  "v_cvt_pk_bf16_f32 v79, v174, v175\n"
  "v_cvt_pk_bf16_f32 v80, v144, v145\n"
  "v_mfma_f32_16x16x32_bf16 a[212:215], a[92:95], a[104:107], a[212:215]\n"
  "v_cvt_pk_bf16_f32 v81, v146, v147\n"
  "v_cvt_pk_bf16_f32 v82, v148, v149\n"
  "v_mfma_f32_16x16x32_bf16 a[152:155], a[80:83], a[108:111], a[152:155]\n"
  "ds_read_b64_tr_b16 a[62:63], v11 offset:33792\n"
  "v_cvt_pk_bf16_f32 v83, v150, v151\n"
  "v_cvt_pk_bf16_f32 v84, v176, v177\n"
  "v_mfma_f32_16x16x32_bf16 a[156:159], a[84:87], a[108:111], a[156:159]\n"
  "ds_read_b128 a[44:47], v2 offset:6144\n"
  "buffer_load_dwordx4 v30, s[40:43], s64 offen offset:2048 lds\n"
  "v_cvt_pk_bf16_f32 v85, v178, v179\n"
  "v_cvt_pk_bf16_f32 v86, v180, v181\n"
  "v_mfma_f32_16x16x32_bf16 a[216:219], a[88:91], a[108:111], a[216:219]\n"
  "ds_read_b64_tr_b16 a[64:65], v12 offset:32768\n"
  "v_cvt_pk_bf16_f32 v87, v182, v183\n"
  "v_cvt_pk_bf16_f32 v88, v152, v153\n"
  "v_mfma_f32_16x16x32_bf16 a[220:223], a[92:95], a[108:111], a[220:223]\n"
  "ds_read_b64_tr_b16 a[66:67], v12 offset:33792\n"
  "v_cvt_pk_bf16_f32 v89, v154, v155\n"
  "v_cvt_pk_bf16_f32 v90, v156, v157\n"
  "v_mfma_f32_16x16x32_bf16 a[160:163], a[80:83], a[112:115], a[160:163]\n"
  "ds_read_b64_tr_b16 a[68:69], v13 offset:32768\n"
  "v_cvt_pk_bf16_f32 v91, v158, v159\n"
  "v_cvt_pk_bf16_f32 v92, v184, v185\n"
  "v_mfma_f32_16x16x32_bf16 a[164:167], a[84:87], a[112:115], a[164:167]\n"
  "v_cvt_pk_bf16_f32 v93, v186, v187\n"
  "v_cvt_pk_bf16_f32 v94, v188, v189\n"
  "v_mfma_f32_16x16x32_bf16 a[224:227], a[88:91], a[112:115], a[224:227]\n"
  "ds_read_b64_tr_b16 a[70:71], v13 offset:33792\n"
  "v_cvt_pk_bf16_f32 v95, v190, v191\n"
  "v_cvt_pk_bf16_f32 v96, v192, v193\n"
  "v_mfma_f32_16x16x32_bf16 a[228:231], a[92:95], a[112:115], a[228:231]\n"
  "ds_read_b64_tr_b16 a[72:73], v14 offset:32768\n"
  "buffer_load_dwordx4 v31, s[40:43], s64 offen offset:3072 lds\n"
  "v_cvt_pk_bf16_f32 v97, v194, v195\n"
  "v_cvt_pk_bf16_f32 v98, v196, v197\n"
  "v_mfma_f32_16x16x32_bf16 a[168:171], a[80:83], a[116:119], a[168:171]\n"
  "ds_read_b64_tr_b16 a[74:75], v14 offset:33792\n"
  "s_add_u32 m0, s62, 49152\n"
  "s_mov_b32 s64, 524288\n"
  "v_cvt_pk_bf16_f32 v99, v198, v199\n"
  "v_cvt_pk_bf16_f32 v100, v224, v225\n"
  "v_mfma_f32_16x16x32_bf16 a[172:175], a[84:87], a[116:119], a[172:175]\n"
  "ds_read_b64_tr_b16 a[76:77], v15 offset:32768\n"
  "v_cvt_pk_bf16_f32 v101, v226, v227\n"
  "v_cvt_pk_bf16_f32 v102, v228, v229\n"
  "v_mfma_f32_16x16x32_bf16 a[232:235], a[88:91], a[116:119], a[232:235]\n"
  "ds_read_b64_tr_b16 a[78:79], v15 offset:33792\n"
  "v_cvt_pk_bf16_f32 v103, v230, v231\n"
  "v_cvt_pk_bf16_f32 v104, v200, v201\n"
  "v_mfma_f32_16x16x32_bf16 a[236:239], a[92:95], a[116:119], a[236:239]\n"
  "v_cvt_pk_bf16_f32 v105, v202, v203\n"
  "v_cvt_pk_bf16_f32 v106, v204, v205\n"
  "v_mfma_f32_16x16x32_bf16 a[176:179], a[80:83], a[120:123], a[176:179]\n"
  "v_cvt_pk_bf16_f32 v107, v206, v207\n"
  "v_cvt_pk_bf16_f32 v108, v232, v233\n"
  "v_mfma_f32_16x16x32_bf16 a[180:183], a[84:87], a[120:123], a[180:183]\n"
  "buffer_load_dwordx4 v24, s[44:47], s64 offen offset:0 lds\n"
  "v_cvt_pk_bf16_f32 v109, v234, v235\n"
  "v_cvt_pk_bf16_f32 v110, v236, v237\n"
  "v_mfma_f32_16x16x32_bf16 a[240:243], a[88:91], a[120:123], a[240:243]\n"
  "v_cvt_pk_bf16_f32 v111, v238, v239\n"
  "v_cvt_pk_bf16_f32 v112, v208, v209\n"
  "v_mfma_f32_16x16x32_bf16 a[244:247], a[92:95], a[120:123], a[244:247]\n"
  "v_cvt_pk_bf16_f32 v113, v210, v211\n"
  "v_cvt_pk_bf16_f32 v114, v212, v213\n"
  "v_mfma_f32_16x16x32_bf16 a[184:187], a[80:83], a[124:127], a[184:187]\n"
  "v_cvt_pk_bf16_f32 v115, v214, v215\n"
  "v_cvt_pk_bf16_f32 v116, v240, v241\n"
  "v_mfma_f32_16x16x32_bf16 a[188:191], a[84:87], a[124:127], a[188:191]\n"
  "v_cvt_pk_bf16_f32 v117, v242, v243\n"
  "v_cvt_pk_bf16_f32 v118, v244, v245\n"
  "v_mfma_f32_16x16x32_bf16 a[248:251], a[88:91], a[124:127], a[248:251]\n"
  "v_cvt_pk_bf16_f32 v119, v246, v247\n"
  "v_cvt_pk_bf16_f32 v120, v216, v217\n"
  "v_mfma_f32_16x16x32_bf16 a[252:255], a[92:95], a[124:127], a[252:255]\n"
  "buffer_load_dwordx4 v25, s[44:47], s64 offen offset:1024 lds\n"
  "v_cvt_pk_bf16_f32 v121, v218, v219\n"
  "v_cvt_pk_bf16_f32 v122, v220, v221\n"
  "v_cvt_pk_bf16_f32 v123, v222, v223\n"
  "v_cvt_pk_bf16_f32 v124, v248, v249\n"
  "v_cvt_pk_bf16_f32 v125, v250, v251\n"
  "v_cvt_pk_bf16_f32 v126, v252, v253\n"
  "v_cvt_pk_bf16_f32 v127, v254, v255\n"
  "s_waitcnt lgkmcnt(0)\n"
  "v_mfma_f32_16x16x32_bf16 v[128:131], a[48:51], a[32:35], 0\n"
  "ds_read_b64_tr_b16 a[96:97], v8 offset:40960\n"
  "v_mfma_f32_16x16x32_bf16 v[160:163], a[48:51], a[36:39], 0\n"
  "ds_read_b64_tr_b16 a[98:99], v8 offset:41984\n"
  "v_mfma_f32_16x16x32_bf16 v[192:195], a[48:51], a[40:43], 0\n"
  "ds_read_b128 a[80:83], v3 offset:0\n"
  "v_mfma_f32_16x16x32_bf16 v[224:227], a[48:51], a[44:47], 0\n"
  "ds_read_b64_tr_b16 a[100:101], v9 offset:40960\n"
  "buffer_load_dwordx4 v26, s[44:47], s64 offen offset:2048 lds\n"
  "v_mfma_f32_16x16x32_bf16 v[132:135], a[52:55], a[32:35], 0\n"
  "ds_read_b64_tr_b16 a[102:103], v9 offset:41984\n"
  "v_mfma_f32_16x16x32_bf16 v[164:167], a[52:55], a[36:39], 0\n"
  "v_mfma_f32_16x16x32_bf16 v[196:199], a[52:55], a[40:43], 0\n"
  "ds_read_b128 a[84:87], v3 offset:2048\n"
  "v_mfma_f32_16x16x32_bf16 v[228:231], a[52:55], a[44:47], 0\n"
  "ds_read_b64_tr_b16 a[104:105], v10 offset:40960\n"
  "buffer_load_dwordx4 v27, s[44:47], s64 offen offset:3072 lds\n"
  "v_mfma_f32_16x16x32_bf16 v[136:139], a[56:59], a[32:35], 0\n"
  "ds_read_b64_tr_b16 a[106:107], v10 offset:41984\n"
  "s_add_u32 m0, s60, 20480\n"
  "s_mov_b32 s64, 128\n"
  "s_add_u32 s65, s74, 0\n"
  "v_mfma_f32_16x16x32_bf16 v[168:171], a[56:59], a[36:39], 0\n"
  "ds_read_b128 a[88:91], v3 offset:4096\n"
  "v_mfma_f32_16x16x32_bf16 v[200:203], a[56:59], a[40:43], 0\n"
  "ds_read_b64_tr_b16 a[108:109], v11 offset:40960\n"
  "v_mfma_f32_16x16x32_bf16 v[232:235], a[56:59], a[44:47], 0\n"
  "buffer_load_dwordx4 v32, s[40:43], s64 offen offset:0 lds\n"
  "v_mfma_f32_16x16x32_bf16 v[140:143], a[60:63], a[32:35], 0\n"
  "ds_read_b64_tr_b16 a[110:111], v11 offset:41984\n"
  "v_mfma_f32_16x16x32_bf16 v[172:175], a[60:63], a[36:39], 0\n"
  "ds_read_b128 a[92:95], v3 offset:6144\n"
  "v_mfma_f32_16x16x32_bf16 v[204:207], a[60:63], a[40:43], 0\n"
  "ds_read_b64_tr_b16 a[112:113], v12 offset:40960\n"
  "v_mfma_f32_16x16x32_bf16 v[236:239], a[60:63], a[44:47], 0\n"
  "ds_read_b64_tr_b16 a[114:115], v12 offset:41984\n"
  "buffer_load_dwordx4 v33, s[40:43], s64 offen offset:1024 lds\n"
  "v_mfma_f32_16x16x32_bf16 v[144:147], a[64:67], a[32:35], 0\n"
  "ds_read_b64_tr_b16 a[116:117], v13 offset:40960\n"
  "v_mfma_f32_16x16x32_bf16 v[176:179], a[64:67], a[36:39], 0\n"
  "v_mfma_f32_16x16x32_bf16 v[208:211], a[64:67], a[40:43], 0\n"
  "ds_read_b64_tr_b16 a[118:119], v13 offset:41984\n"
  "v_mfma_f32_16x16x32_bf16 v[240:243], a[64:67], a[44:47], 0\n"
  "ds_read_b64_tr_b16 a[120:121], v14 offset:40960\n"
  "buffer_load_dwordx4 v34, s[40:43], s64 offen offset:2048 lds\n"
  "v_mfma_f32_16x16x32_bf16 v[148:151], a[68:71], a[32:35], 0\n"
  "ds_read_b64_tr_b16 a[122:123], v14 offset:41984\n"
  "v_mfma_f32_16x16x32_bf16 v[180:183], a[68:71], a[36:39], 0\n"
  "ds_read_b64_tr_b16 a[124:125], v15 offset:40960\n"
  "v_mfma_f32_16x16x32_bf16 v[212:215], a[68:71], a[40:43], 0\n"
  "ds_read_b64_tr_b16 a[126:127], v15 offset:41984\n"
  "buffer_store_dwordx4 v[64:67], v52, s[88:91], s65 offen offset:0 sc1\n"
  "v_mfma_f32_16x16x32_bf16 v[244:247], a[68:71], a[44:47], 0\n"
  "buffer_load_dwordx4 v35, s[40:43], s64 offen offset:3072 lds\n"
  "v_mfma_f32_16x16x32_bf16 v[152:155], a[72:75], a[32:35], 0\n"
  "s_add_u32 m0, s60, 65536\n"
  "s_mov_b32 s64, 256\n"
  "v_add_u32 v2, 0xfffe8000, v2\n"
  "v_mfma_f32_16x16x32_bf16 v[184:187], a[72:75], a[36:39], 0\n"
  "v_add_u32 v3, 0xfffe8000, v3\n"
  "v_mfma_f32_16x16x32_bf16 v[216:219], a[72:75], a[40:43], 0\n"
  "v_add_u32 v8, 0xfffe8000, v8\n"
  "v_mfma_f32_16x16x32_bf16 v[248:251], a[72:75], a[44:47], 0\n"
  "buffer_load_dwordx4 v28, s[40:43], s64 offen offset:0 lds\n"
  "v_add_u32 v9, 0xfffe8000, v9\n"
  "v_add_u32 v10, 0xfffe8000, v10\n"
  "v_mfma_f32_16x16x32_bf16 v[156:159], a[76:79], a[32:35], 0\n"
  "v_add_u32 v11, 0xfffe8000, v11\n"
  "v_mfma_f32_16x16x32_bf16 v[188:191], a[76:79], a[36:39], 0\n"
  "v_add_u32 v12, 0xfffe8000, v12\n"
  "v_mfma_f32_16x16x32_bf16 v[220:223], a[76:79], a[40:43], 0\n"
  "v_add_u32 v13, 0xfffe8000, v13\n"
  "v_mfma_f32_16x16x32_bf16 v[252:255], a[76:79], a[44:47], 0\n"
  "buffer_load_dwordx4 v29, s[40:43], s64 offen offset:1024 lds\n"
  "v_add_u32 v14, 0xfffe8000, v14\n"
  "v_add_u32 v15, 0xfffe8000, v15\n"
  "s_waitcnt vmcnt(2) lgkmcnt(0)\n"
  "s_barrier\n"
  "v_mfma_f32_16x16x32_bf16 v[128:131], a[96:99], a[80:83], v[128:131]\n"
  "ds_read_b64_tr_b16 a[48:49], v8 offset:32768\n"
  "v_mfma_f32_16x16x32_bf16 v[160:163], a[96:99], a[84:87], v[160:163]\n"
  "ds_read_b64_tr_b16 a[50:51], v8 offset:33792\n"
  "v_mfma_f32_16x16x32_bf16 v[192:195], a[96:99], a[88:91], v[192:195]\n"
  "ds_read_b128 a[32:35], v2 offset:0\n"
  "v_mfma_f32_16x16x32_bf16 v[224:227], a[96:99], a[92:95], v[224:227]\n"
  "ds_read_b64_tr_b16 a[52:53], v9 offset:32768\n"
  "buffer_load_dwordx4 v30, s[40:43], s64 offen offset:2048 lds\n"
  "v_mfma_f32_16x16x32_bf16 v[132:135], a[100:103], a[80:83], v[132:135]\n"
  "ds_read_b64_tr_b16 a[54:55], v9 offset:33792\n"
  "buffer_store_dwordx4 v[68:71], v52, s[88:91], s65 offen offset:1024 sc1\n"
  "v_mfma_f32_16x16x32_bf16 v[164:167], a[100:103], a[84:87], v[164:167]\n"
  "v_mfma_f32_16x16x32_bf16 v[196:199], a[100:103], a[88:91], v[196:199]\n"
  "ds_read_b128 a[36:39], v2 offset:2048\n"
  "v_mfma_f32_16x16x32_bf16 v[228:231], a[100:103], a[92:95], v[228:231]\n"
  "ds_read_b64_tr_b16 a[56:57], v10 offset:32768\n"
  "buffer_load_dwordx4 v31, s[40:43], s64 offen offset:3072 lds\n"
  "v_mfma_f32_16x16x32_bf16 v[136:139], a[104:107], a[80:83], v[136:139]\n"
  "ds_read_b64_tr_b16 a[58:59], v10 offset:33792\n"
  "s_add_u32 m0, s62, 98304\n"
  "s_mov_b32 s64, 1048576\n"
  "v_mfma_f32_16x16x32_bf16 v[168:171], a[104:107], a[84:87], v[168:171]\n"
  "ds_read_b128 a[40:43], v2 offset:4096\n"
  "v_mfma_f32_16x16x32_bf16 v[200:203], a[104:107], a[88:91], v[200:203]\n"
  "ds_read_b64_tr_b16 a[60:61], v11 offset:32768\n"
  "v_mfma_f32_16x16x32_bf16 v[232:235], a[104:107], a[92:95], v[232:235]\n"
  "buffer_load_dwordx4 v24, s[44:47], s64 offen offset:0 lds\n"
  "v_mfma_f32_16x16x32_bf16 v[140:143], a[108:111], a[80:83], v[140:143]\n"
  "ds_read_b64_tr_b16 a[62:63], v11 offset:33792\n"
  "v_mfma_f32_16x16x32_bf16 v[172:175], a[108:111], a[84:87], v[172:175]\n"
  "ds_read_b128 a[44:47], v2 offset:6144\n"
  "v_mfma_f32_16x16x32_bf16 v[204:207], a[108:111], a[88:91], v[204:207]\n"
  "ds_read_b64_tr_b16 a[64:65], v12 offset:32768\n"
  "v_mfma_f32_16x16x32_bf16 v[236:239], a[108:111], a[92:95], v[236:239]\n"
  "ds_read_b64_tr_b16 a[66:67], v12 offset:33792\n"
  "buffer_load_dwordx4 v25, s[44:47], s64 offen offset:1024 lds\n"
  "v_mfma_f32_16x16x32_bf16 v[144:147], a[112:115], a[80:83], v[144:147]\n"
  "ds_read_b64_tr_b16 a[68:69], v13 offset:32768\n"
  "v_mfma_f32_16x16x32_bf16 v[176:179], a[112:115], a[84:87], v[176:179]\n"
  "v_mfma_f32_16x16x32_bf16 v[208:211], a[112:115], a[88:91], v[208:211]\n"
  "ds_read_b64_tr_b16 a[70:71], v13 offset:33792\n"
  "buffer_store_dwordx4 v[72:75], v52, s[88:91], s65 offen offset:2048 sc1\n"
  "v_mfma_f32_16x16x32_bf16 v[240:243], a[112:115], a[92:95], v[240:243]\n"
  "ds_read_b64_tr_b16 a[72:73], v14 offset:32768\n"
  "buffer_load_dwordx4 v26, s[44:47], s64 offen offset:2048 lds\n"
  "v_mfma_f32_16x16x32_bf16 v[148:151], a[116:119], a[80:83], v[148:151]\n"
  "ds_read_b64_tr_b16 a[74:75], v14 offset:33792\n"
  "v_mfma_f32_16x16x32_bf16 v[180:183], a[116:119], a[84:87], v[180:183]\n"
  "ds_read_b64_tr_b16 a[76:77], v15 offset:32768\n"
  "v_mfma_f32_16x16x32_bf16 v[212:215], a[116:119], a[88:91], v[212:215]\n"
  "ds_read_b64_tr_b16 a[78:79], v15 offset:33792\n"
  "v_mfma_f32_16x16x32_bf16 v[244:247], a[116:119], a[92:95], v[244:247]\n"
  "buffer_load_dwordx4 v27, s[44:47], s64 offen offset:3072 lds\n"
  "v_mfma_f32_16x16x32_bf16 v[152:155], a[120:123], a[80:83], v[152:155]\n"
  "s_add_u32 m0, s60, 69632\n"
  "s_mov_b32 s64, 256\n"
  "v_mfma_f32_16x16x32_bf16 v[184:187], a[120:123], a[84:87], v[184:187]\n"
  "v_mfma_f32_16x16x32_bf16 v[216:219], a[120:123], a[88:91], v[216:219]\n"
  "v_mfma_f32_16x16x32_bf16 v[248:251], a[120:123], a[92:95], v[248:251]\n"
  "buffer_load_dwordx4 v32, s[40:43], s64 offen offset:0 lds\n"
  "v_mfma_f32_16x16x32_bf16 v[156:159], a[124:127], a[80:83], v[156:159]\n"
  "v_mfma_f32_16x16x32_bf16 v[188:191], a[124:127], a[84:87], v[188:191]\n"
  "v_mfma_f32_16x16x32_bf16 v[220:223], a[124:127], a[88:91], v[220:223]\n"
  "v_mfma_f32_16x16x32_bf16 v[252:255], a[124:127], a[92:95], v[252:255]\n"
  "buffer_load_dwordx4 v33, s[40:43], s64 offen offset:1024 lds\n"
  "s_waitcnt lgkmcnt(0)\n"
  "v_mfma_f32_16x16x32_bf16 v[128:131], a[48:51], a[32:35], v[128:131]\n"
  "ds_read_b64_tr_b16 a[96:97], v8 offset:40960\n"
  "buffer_store_dwordx4 v[76:79], v52, s[88:91], s65 offen offset:3072 sc1\n"
  "v_mfma_f32_16x16x32_bf16 v[160:163], a[48:51], a[36:39], v[160:163]\n"
  "ds_read_b64_tr_b16 a[98:99], v8 offset:41984\n"
  "v_mfma_f32_16x16x32_bf16 v[192:195], a[48:51], a[40:43], v[192:195]\n"
  "ds_read_b128 a[80:83], v3 offset:0\n"
  "v_mfma_f32_16x16x32_bf16 v[224:227], a[48:51], a[44:47], v[224:227]\n"
  "ds_read_b64_tr_b16 a[100:101], v9 offset:40960\n"
  "buffer_load_dwordx4 v34, s[40:43], s64 offen offset:2048 lds\n"
  "v_mfma_f32_16x16x32_bf16 v[132:135], a[52:55], a[32:35], v[132:135]\n"
  "ds_read_b64_tr_b16 a[102:103], v9 offset:41984\n"
  "v_mfma_f32_16x16x32_bf16 v[164:167], a[52:55], a[36:39], v[164:167]\n"
  "v_mfma_f32_16x16x32_bf16 v[196:199], a[52:55], a[40:43], v[196:199]\n"
  "ds_read_b128 a[84:87], v3 offset:2048\n"
  "v_mfma_f32_16x16x32_bf16 v[228:231], a[52:55], a[44:47], v[228:231]\n"
  "ds_read_b64_tr_b16 a[104:105], v10 offset:40960\n"
  "buffer_load_dwordx4 v35, s[40:43], s64 offen offset:3072 lds\n"
  "v_mfma_f32_16x16x32_bf16 v[136:139], a[56:59], a[32:35], v[136:139]\n"
  "ds_read_b64_tr_b16 a[106:107], v10 offset:41984\n"
  "s_add_u32 m0, s60, 114688\n"
  "s_mov_b32 s64, 384\n"
  "v_mfma_f32_16x16x32_bf16 v[168:171], a[56:59], a[36:39], v[168:171]\n"
  "ds_read_b128 a[88:91], v3 offset:4096\n"
  "v_mfma_f32_16x16x32_bf16 v[200:203], a[56:59], a[40:43], v[200:203]\n"
  "ds_read_b64_tr_b16 a[108:109], v11 offset:40960\n"
  "v_mfma_f32_16x16x32_bf16 v[232:235], a[56:59], a[44:47], v[232:235]\n"
  "buffer_load_dwordx4 v28, s[40:43], s64 offen offset:0 lds\n"
  "v_mfma_f32_16x16x32_bf16 v[140:143], a[60:63], a[32:35], v[140:143]\n"
  "ds_read_b64_tr_b16 a[110:111], v11 offset:41984\n"
  "v_mfma_f32_16x16x32_bf16 v[172:175], a[60:63], a[36:39], v[172:175]\n"
  "ds_read_b128 a[92:95], v3 offset:6144\n"
  "v_mfma_f32_16x16x32_bf16 v[204:207], a[60:63], a[40:43], v[204:207]\n"
  "ds_read_b64_tr_b16 a[112:113], v12 offset:40960\n"
  "buffer_store_dwordx4 v[80:83], v53, s[88:91], s65 offen offset:0 sc1\n"
  "v_mfma_f32_16x16x32_bf16 v[236:239], a[60:63], a[44:47], v[236:239]\n"
  "ds_read_b64_tr_b16 a[114:115], v12 offset:41984\n"
  "buffer_load_dwordx4 v29, s[40:43], s64 offen offset:1024 lds\n"
  "v_mfma_f32_16x16x32_bf16 v[144:147], a[64:67], a[32:35], v[144:147]\n"
  "ds_read_b64_tr_b16 a[116:117], v13 offset:40960\n"
  "v_mfma_f32_16x16x32_bf16 v[176:179], a[64:67], a[36:39], v[176:179]\n"
  "v_mfma_f32_16x16x32_bf16 v[208:211], a[64:67], a[40:43], v[208:211]\n"
  "ds_read_b64_tr_b16 a[118:119], v13 offset:41984\n"
  "v_mfma_f32_16x16x32_bf16 v[240:243], a[64:67], a[44:47], v[240:243]\n"
  "ds_read_b64_tr_b16 a[120:121], v14 offset:40960\n"
  "buffer_load_dwordx4 v30, s[40:43], s64 offen offset:2048 lds\n"
  "v_mfma_f32_16x16x32_bf16 v[148:151], a[68:71], a[32:35], v[148:151]\n"
  "ds_read_b64_tr_b16 a[122:123], v14 offset:41984\n"
  "v_mfma_f32_16x16x32_bf16 v[180:183], a[68:71], a[36:39], v[180:183]\n"
  "ds_read_b64_tr_b16 a[124:125], v15 offset:40960\n"
  "v_mfma_f32_16x16x32_bf16 v[212:215], a[68:71], a[40:43], v[212:215]\n"
  "ds_read_b64_tr_b16 a[126:127], v15 offset:41984\n"
  "v_mfma_f32_16x16x32_bf16 v[244:247], a[68:71], a[44:47], v[244:247]\n"
  "buffer_load_dwordx4 v31, s[40:43], s64 offen offset:3072 lds\n"
  "v_mfma_f32_16x16x32_bf16 v[152:155], a[72:75], a[32:35], v[152:155]\n"
  "s_add_u32 m0, s62, 147456\n"
  "s_mov_b32 s64, 1572864\n"
  "v_add_u32 v2, 0xc000, v2\n"
  "v_mfma_f32_16x16x32_bf16 v[184:187], a[72:75], a[36:39], v[184:187]\n"
  "v_add_u32 v3, 0xc000, v3\n"
  "v_mfma_f32_16x16x32_bf16 v[216:219], a[72:75], a[40:43], v[216:219]\n"
  "v_add_u32 v8, 0xc000, v8\n"
  "v_mfma_f32_16x16x32_bf16 v[248:251], a[72:75], a[44:47], v[248:251]\n"
  "buffer_load_dwordx4 v24, s[44:47], s64 offen offset:0 lds\n"
  "v_add_u32 v9, 0xc000, v9\n"
  "v_add_u32 v10, 0xc000, v10\n"
  "v_mfma_f32_16x16x32_bf16 v[156:159], a[76:79], a[32:35], v[156:159]\n"
  "v_add_u32 v11, 0xc000, v11\n"
  "v_mfma_f32_16x16x32_bf16 v[188:191], a[76:79], a[36:39], v[188:191]\n"
  "v_add_u32 v12, 0xc000, v12\n"
  "v_mfma_f32_16x16x32_bf16 v[220:223], a[76:79], a[40:43], v[220:223]\n"
  "v_add_u32 v13, 0xc000, v13\n"
  "v_mfma_f32_16x16x32_bf16 v[252:255], a[76:79], a[44:47], v[252:255]\n"
  "buffer_load_dwordx4 v25, s[44:47], s64 offen offset:1024 lds\n"
  "v_add_u32 v14, 0xc000, v14\n"
  "v_add_u32 v15, 0xc000, v15\n"
  "s_waitcnt vmcnt(7) lgkmcnt(0)\n"
  "s_barrier\n"
  "v_mfma_f32_16x16x32_bf16 v[128:131], a[96:99], a[80:83], v[128:131]\n"
  "ds_read_b64_tr_b16 a[48:49], v8 offset:32768\n"
  "buffer_store_dwordx4 v[84:87], v53, s[88:91], s65 offen offset:1024 sc1\n"
  "v_mfma_f32_16x16x32_bf16 v[160:163], a[96:99], a[84:87], v[160:163]\n"
  "ds_read_b64_tr_b16 a[50:51], v8 offset:33792\n"
  "v_mfma_f32_16x16x32_bf16 v[192:195], a[96:99], a[88:91], v[192:195]\n"
  "ds_read_b128 a[32:35], v2 offset:0\n"
  "v_mfma_f32_16x16x32_bf16 v[224:227], a[96:99], a[92:95], v[224:227]\n"
  "ds_read_b64_tr_b16 a[52:53], v9 offset:32768\n"
  "buffer_load_dwordx4 v26, s[44:47], s64 offen offset:2048 lds\n"
  "v_mfma_f32_16x16x32_bf16 v[132:135], a[100:103], a[80:83], v[132:135]\n"
  "ds_read_b64_tr_b16 a[54:55], v9 offset:33792\n"
  "v_mfma_f32_16x16x32_bf16 v[164:167], a[100:103], a[84:87], v[164:167]\n"
  "v_mfma_f32_16x16x32_bf16 v[196:199], a[100:103], a[88:91], v[196:199]\n"
  "ds_read_b128 a[36:39], v2 offset:2048\n"
  "v_mfma_f32_16x16x32_bf16 v[228:231], a[100:103], a[92:95], v[228:231]\n"
  "ds_read_b64_tr_b16 a[56:57], v10 offset:32768\n"
  "buffer_load_dwordx4 v27, s[44:47], s64 offen offset:3072 lds\n"
  "v_mfma_f32_16x16x32_bf16 v[136:139], a[104:107], a[80:83], v[136:139]\n"
  "ds_read_b64_tr_b16 a[58:59], v10 offset:33792\n"
  "s_add_u32 m0, s60, 118784\n"
  "s_mov_b32 s64, 384\n"
  "v_mfma_f32_16x16x32_bf16 v[168:171], a[104:107], a[84:87], v[168:171]\n"
  "ds_read_b128 a[40:43], v2 offset:4096\n"
  "v_mfma_f32_16x16x32_bf16 v[200:203], a[104:107], a[88:91], v[200:203]\n"
  "ds_read_b64_tr_b16 a[60:61], v11 offset:32768\n"
  "buffer_store_dwordx4 v[88:91], v53, s[88:91], s65 offen offset:2048 sc1\n"
  "v_mfma_f32_16x16x32_bf16 v[232:235], a[104:107], a[92:95], v[232:235]\n"
  "buffer_load_dwordx4 v32, s[40:43], s64 offen offset:0 lds\n"
  "v_mfma_f32_16x16x32_bf16 v[140:143], a[108:111], a[80:83], v[140:143]\n"
  "ds_read_b64_tr_b16 a[62:63], v11 offset:33792\n"
  "v_mfma_f32_16x16x32_bf16 v[172:175], a[108:111], a[84:87], v[172:175]\n"
  "ds_read_b128 a[44:47], v2 offset:6144\n"
  "v_mfma_f32_16x16x32_bf16 v[204:207], a[108:111], a[88:91], v[204:207]\n"
  "ds_read_b64_tr_b16 a[64:65], v12 offset:32768\n"
  "v_mfma_f32_16x16x32_bf16 v[236:239], a[108:111], a[92:95], v[236:239]\n"
  "ds_read_b64_tr_b16 a[66:67], v12 offset:33792\n"
  "buffer_load_dwordx4 v33, s[40:43], s64 offen offset:1024 lds\n"
  "v_mfma_f32_16x16x32_bf16 v[144:147], a[112:115], a[80:83], v[144:147]\n"
  "ds_read_b64_tr_b16 a[68:69], v13 offset:32768\n"
  "v_mfma_f32_16x16x32_bf16 v[176:179], a[112:115], a[84:87], v[176:179]\n"
  "v_mfma_f32_16x16x32_bf16 v[208:211], a[112:115], a[88:91], v[208:211]\n"
  "ds_read_b64_tr_b16 a[70:71], v13 offset:33792\n"
  "v_mfma_f32_16x16x32_bf16 v[240:243], a[112:115], a[92:95], v[240:243]\n"
  "ds_read_b64_tr_b16 a[72:73], v14 offset:32768\n"
  "buffer_load_dwordx4 v34, s[40:43], s64 offen offset:2048 lds\n"
  "v_mfma_f32_16x16x32_bf16 v[148:151], a[116:119], a[80:83], v[148:151]\n"
  "ds_read_b64_tr_b16 a[74:75], v14 offset:33792\n"
  "v_mfma_f32_16x16x32_bf16 v[180:183], a[116:119], a[84:87], v[180:183]\n"
  "ds_read_b64_tr_b16 a[76:77], v15 offset:32768\n"
  "v_mfma_f32_16x16x32_bf16 v[212:215], a[116:119], a[88:91], v[212:215]\n"
  "ds_read_b64_tr_b16 a[78:79], v15 offset:33792\n"
  "v_mfma_f32_16x16x32_bf16 v[244:247], a[116:119], a[92:95], v[244:247]\n"
  "buffer_load_dwordx4 v35, s[40:43], s64 offen offset:3072 lds\n"
  "v_mfma_f32_16x16x32_bf16 v[152:155], a[120:123], a[80:83], v[152:155]\n"
  "s_add_u32 m0, s60, 16384\n"
  "s_mov_b32 s64, 512\n"
  "buffer_store_dwordx4 v[92:95], v53, s[88:91], s65 offen offset:3072 sc1\n"
  "v_mfma_f32_16x16x32_bf16 v[184:187], a[120:123], a[84:87], v[184:187]\n"
  "v_mfma_f32_16x16x32_bf16 v[216:219], a[120:123], a[88:91], v[216:219]\n"
  "v_mfma_f32_16x16x32_bf16 v[248:251], a[120:123], a[92:95], v[248:251]\n"
  "buffer_load_dwordx4 v28, s[40:43], s64 offen offset:0 lds\n"
  "v_mfma_f32_16x16x32_bf16 v[156:159], a[124:127], a[80:83], v[156:159]\n"
  "v_mfma_f32_16x16x32_bf16 v[188:191], a[124:127], a[84:87], v[188:191]\n"
  "v_mfma_f32_16x16x32_bf16 v[220:223], a[124:127], a[88:91], v[220:223]\n"
  "v_mfma_f32_16x16x32_bf16 v[252:255], a[124:127], a[92:95], v[252:255]\n"
  "buffer_load_dwordx4 v29, s[40:43], s64 offen offset:1024 lds\n"
  "s_waitcnt lgkmcnt(0)\n"
  "v_mfma_f32_16x16x32_bf16 v[128:131], a[48:51], a[32:35], v[128:131]\n"
  "ds_read_b64_tr_b16 a[96:97], v8 offset:40960\n"
  "v_mfma_f32_16x16x32_bf16 v[160:163], a[48:51], a[36:39], v[160:163]\n"
  "ds_read_b64_tr_b16 a[98:99], v8 offset:41984\n"
  "v_mfma_f32_16x16x32_bf16 v[192:195], a[48:51], a[40:43], v[192:195]\n"
  "ds_read_b128 a[80:83], v3 offset:0\n"
  "v_mfma_f32_16x16x32_bf16 v[224:227], a[48:51], a[44:47], v[224:227]\n"
  "ds_read_b64_tr_b16 a[100:101], v9 offset:40960\n"
  "buffer_load_dwordx4 v30, s[40:43], s64 offen offset:2048 lds\n"
  "v_mfma_f32_16x16x32_bf16 v[132:135], a[52:55], a[32:35], v[132:135]\n"
  "ds_read_b64_tr_b16 a[102:103], v9 offset:41984\n"
  "v_mfma_f32_16x16x32_bf16 v[164:167], a[52:55], a[36:39], v[164:167]\n"
  "v_mfma_f32_16x16x32_bf16 v[196:199], a[52:55], a[40:43], v[196:199]\n"
  "ds_read_b128 a[84:87], v3 offset:2048\n"
  "s_add_u32 s65, s74, 8192\n"
  "v_mfma_f32_16x16x32_bf16 v[228:231], a[52:55], a[44:47], v[228:231]\n"
  "ds_read_b64_tr_b16 a[104:105], v10 offset:40960\n"
  "buffer_load_dwordx4 v31, s[40:43], s64 offen offset:3072 lds\n"
  "v_mfma_f32_16x16x32_bf16 v[136:139], a[56:59], a[32:35], v[136:139]\n"
  "ds_read_b64_tr_b16 a[106:107], v10 offset:41984\n"
  "s_add_u32 m0, s62, 49152\n"
  "s_mov_b32 s64, 2097152\n"
  "v_mfma_f32_16x16x32_bf16 v[168:171], a[56:59], a[36:39], v[168:171]\n"
  "ds_read_b128 a[88:91], v3 offset:4096\n"
  "v_mfma_f32_16x16x32_bf16 v[200:203], a[56:59], a[40:43], v[200:203]\n"
  "ds_read_b64_tr_b16 a[108:109], v11 offset:40960\n"
  "v_mfma_f32_16x16x32_bf16 v[232:235], a[56:59], a[44:47], v[232:235]\n"
  "buffer_load_dwordx4 v24, s[44:47], s64 offen offset:0 lds\n"
  "v_mfma_f32_16x16x32_bf16 v[140:143], a[60:63], a[32:35], v[140:143]\n"
  "ds_read_b64_tr_b16 a[110:111], v11 offset:41984\n"
  "v_mfma_f32_16x16x32_bf16 v[172:175], a[60:63], a[36:39], v[172:175]\n"
  "ds_read_b128 a[92:95], v3 offset:6144\n"
  "v_mfma_f32_16x16x32_bf16 v[204:207], a[60:63], a[40:43], v[204:207]\n"
  "ds_read_b64_tr_b16 a[112:113], v12 offset:40960\n"
  "v_mfma_f32_16x16x32_bf16 v[236:239], a[60:63], a[44:47], v[236:239]\n"
  "ds_read_b64_tr_b16 a[114:115], v12 offset:41984\n"
  "buffer_load_dwordx4 v25, s[44:47], s64 offen offset:1024 lds\n"
  "v_mfma_f32_16x16x32_bf16 v[144:147], a[64:67], a[32:35], v[144:147]\n"
  "ds_read_b64_tr_b16 a[116:117], v13 offset:40960\n"
  "v_mfma_f32_16x16x32_bf16 v[176:179], a[64:67], a[36:39], v[176:179]\n"
  "v_mfma_f32_16x16x32_bf16 v[208:211], a[64:67], a[40:43], v[208:211]\n"
  "ds_read_b64_tr_b16 a[118:119], v13 offset:41984\n"
  "v_mfma_f32_16x16x32_bf16 v[240:243], a[64:67], a[44:47], v[240:243]\n"
  "ds_read_b64_tr_b16 a[120:121], v14 offset:40960\n"
  "buffer_load_dwordx4 v26, s[44:47], s64 offen offset:2048 lds\n"
  "v_mfma_f32_16x16x32_bf16 v[148:151], a[68:71], a[32:35], v[148:151]\n"
  "ds_read_b64_tr_b16 a[122:123], v14 offset:41984\n"
  "buffer_store_dwordx4 v[96:99], v52, s[88:91], s65 offen offset:0 sc1\n"
  "v_mfma_f32_16x16x32_bf16 v[180:183], a[68:71], a[36:39], v[180:183]\n"
  "ds_read_b64_tr_b16 a[124:125], v15 offset:40960\n"
  "v_mfma_f32_16x16x32_bf16 v[212:215], a[68:71], a[40:43], v[212:215]\n"
  "ds_read_b64_tr_b16 a[126:127], v15 offset:41984\n"
  "v_mfma_f32_16x16x32_bf16 v[244:247], a[68:71], a[44:47], v[244:247]\n"
  "buffer_load_dwordx4 v27, s[44:47], s64 offen offset:3072 lds\n"
  "v_mfma_f32_16x16x32_bf16 v[152:155], a[72:75], a[32:35], v[152:155]\n"
  "s_add_u32 m0, s60, 20480\n"
  "s_mov_b32 s64, 512\n"
  "v_add_u32 v2, 0xc000, v2\n"
  "v_mfma_f32_16x16x32_bf16 v[184:187], a[72:75], a[36:39], v[184:187]\n"
  "v_add_u32 v3, 0xc000, v3\n"
  "v_mfma_f32_16x16x32_bf16 v[216:219], a[72:75], a[40:43], v[216:219]\n"
  "v_add_u32 v8, 0xc000, v8\n"
  "v_mfma_f32_16x16x32_bf16 v[248:251], a[72:75], a[44:47], v[248:251]\n"
  "buffer_load_dwordx4 v32, s[40:43], s64 offen offset:0 lds\n"
  "v_add_u32 v9, 0xc000, v9\n"
  "v_add_u32 v10, 0xc000, v10\n"
  "v_mfma_f32_16x16x32_bf16 v[156:159], a[76:79], a[32:35], v[156:159]\n"
  "v_add_u32 v11, 0xc000, v11\n"
  "v_mfma_f32_16x16x32_bf16 v[188:191], a[76:79], a[36:39], v[188:191]\n"
  "v_add_u32 v12, 0xc000, v12\n"
  "v_mfma_f32_16x16x32_bf16 v[220:223], a[76:79], a[40:43], v[220:223]\n"
  "v_add_u32 v13, 0xc000, v13\n"
  "v_mfma_f32_16x16x32_bf16 v[252:255], a[76:79], a[44:47], v[252:255]\n"
  "buffer_load_dwordx4 v33, s[40:43], s64 offen offset:1024 lds\n"
  "v_add_u32 v14, 0xc000, v14\n"
  "v_add_u32 v15, 0xc000, v15\n"
  "s_waitcnt vmcnt(12) lgkmcnt(0)\n"
  "s_barrier\n"
  "v_mfma_f32_16x16x32_bf16 v[128:131], a[96:99], a[80:83], v[128:131]\n"
  "ds_read_b64_tr_b16 a[48:49], v8 offset:32768\n"
  "v_mfma_f32_16x16x32_bf16 v[160:163], a[96:99], a[84:87], v[160:163]\n"
  "ds_read_b64_tr_b16 a[50:51], v8 offset:33792\n"
  "v_mfma_f32_16x16x32_bf16 v[192:195], a[96:99], a[88:91], v[192:195]\n"
  "ds_read_b128 a[32:35], v2 offset:0\n"
  "buffer_store_dwordx4 v[100:103], v52, s[88:91], s65 offen offset:1024 sc1\n"
  "v_mfma_f32_16x16x32_bf16 v[224:227], a[96:99], a[92:95], v[224:227]\n"
  "ds_read_b64_tr_b16 a[52:53], v9 offset:32768\n"
  "buffer_load_dwordx4 v34, s[40:43], s64 offen offset:2048 lds\n"
  "v_mfma_f32_16x16x32_bf16 v[132:135], a[100:103], a[80:83], v[132:135]\n"
  "ds_read_b64_tr_b16 a[54:55], v9 offset:33792\n"
  "v_mfma_f32_16x16x32_bf16 v[164:167], a[100:103], a[84:87], v[164:167]\n"
  "v_mfma_f32_16x16x32_bf16 v[196:199], a[100:103], a[88:91], v[196:199]\n"
  "ds_read_b128 a[36:39], v2 offset:2048\n"
  "v_mfma_f32_16x16x32_bf16 v[228:231], a[100:103], a[92:95], v[228:231]\n"
  "ds_read_b64_tr_b16 a[56:57], v10 offset:32768\n"
  "buffer_load_dwordx4 v35, s[40:43], s64 offen offset:3072 lds\n"
  "v_mfma_f32_16x16x32_bf16 v[136:139], a[104:107], a[80:83], v[136:139]\n"
  "ds_read_b64_tr_b16 a[58:59], v10 offset:33792\n"
  "s_add_u32 m0, s60, 65536\n"
  "s_mov_b32 s64, 640\n"
  "v_mfma_f32_16x16x32_bf16 v[168:171], a[104:107], a[84:87], v[168:171]\n"
  "ds_read_b128 a[40:43], v2 offset:4096\n"
  "v_mfma_f32_16x16x32_bf16 v[200:203], a[104:107], a[88:91], v[200:203]\n"
  "ds_read_b64_tr_b16 a[60:61], v11 offset:32768\n"
  "v_mfma_f32_16x16x32_bf16 v[232:235], a[104:107], a[92:95], v[232:235]\n"
  "buffer_load_dwordx4 v28, s[40:43], s64 offen offset:0 lds\n"
  "v_mfma_f32_16x16x32_bf16 v[140:143], a[108:111], a[80:83], v[140:143]\n"
  "ds_read_b64_tr_b16 a[62:63], v11 offset:33792\n"
  "v_mfma_f32_16x16x32_bf16 v[172:175], a[108:111], a[84:87], v[172:175]\n"
  "ds_read_b128 a[44:47], v2 offset:6144\n"
  "v_mfma_f32_16x16x32_bf16 v[204:207], a[108:111], a[88:91], v[204:207]\n"
  "ds_read_b64_tr_b16 a[64:65], v12 offset:32768\n"
  "v_mfma_f32_16x16x32_bf16 v[236:239], a[108:111], a[92:95], v[236:239]\n"
  "ds_read_b64_tr_b16 a[66:67], v12 offset:33792\n"
  "buffer_load_dwordx4 v29, s[40:43], s64 offen offset:1024 lds\n"
  "v_mfma_f32_16x16x32_bf16 v[144:147], a[112:115], a[80:83], v[144:147]\n"
  "ds_read_b64_tr_b16 a[68:69], v13 offset:32768\n"
  "buffer_store_dwordx4 v[104:107], v52, s[88:91], s65 offen offset:2048 sc1\n"
  "v_mfma_f32_16x16x32_bf16 v[176:179], a[112:115], a[84:87], v[176:179]\n"
  "v_mfma_f32_16x16x32_bf16 v[208:211], a[112:115], a[88:91], v[208:211]\n"
  "ds_read_b64_tr_b16 a[70:71], v13 offset:33792\n"
  "v_mfma_f32_16x16x32_bf16 v[240:243], a[112:115], a[92:95], v[240:243]\n"
  "ds_read_b64_tr_b16 a[72:73], v14 offset:32768\n"
  "buffer_load_dwordx4 v30, s[40:43], s64 offen offset:2048 lds\n"
  "v_mfma_f32_16x16x32_bf16 v[148:151], a[116:119], a[80:83], v[148:151]\n"
  "ds_read_b64_tr_b16 a[74:75], v14 offset:33792\n"
  "v_mfma_f32_16x16x32_bf16 v[180:183], a[116:119], a[84:87], v[180:183]\n"
  "ds_read_b64_tr_b16 a[76:77], v15 offset:32768\n"
  "v_mfma_f32_16x16x32_bf16 v[212:215], a[116:119], a[88:91], v[212:215]\n"
  "ds_read_b64_tr_b16 a[78:79], v15 offset:33792\n"
  "v_mfma_f32_16x16x32_bf16 v[244:247], a[116:119], a[92:95], v[244:247]\n"
  "buffer_load_dwordx4 v31, s[40:43], s64 offen offset:3072 lds\n"
  "v_mfma_f32_16x16x32_bf16 v[152:155], a[120:123], a[80:83], v[152:155]\n"
  "s_add_u32 m0, s62, 98304\n"
  "s_mov_b32 s64, 2621440\n"
  "v_mfma_f32_16x16x32_bf16 v[184:187], a[120:123], a[84:87], v[184:187]\n"
  "v_mfma_f32_16x16x32_bf16 v[216:219], a[120:123], a[88:91], v[216:219]\n"
  "v_mfma_f32_16x16x32_bf16 v[248:251], a[120:123], a[92:95], v[248:251]\n"
  "buffer_load_dwordx4 v24, s[44:47], s64 offen offset:0 lds\n"
  "v_mfma_f32_16x16x32_bf16 v[156:159], a[124:127], a[80:83], v[156:159]\n"
  "v_mfma_f32_16x16x32_bf16 v[188:191], a[124:127], a[84:87], v[188:191]\n"
  "v_mfma_f32_16x16x32_bf16 v[220:223], a[124:127], a[88:91], v[220:223]\n"
  "buffer_store_dwordx4 v[108:111], v52, s[88:91], s65 offen offset:3072 sc1\n"
  "v_mfma_f32_16x16x32_bf16 v[252:255], a[124:127], a[92:95], v[252:255]\n"
  "buffer_load_dwordx4 v25, s[44:47], s64 offen offset:1024 lds\n"
  "s_waitcnt lgkmcnt(0)\n"
  "v_mfma_f32_16x16x32_bf16 v[128:131], a[48:51], a[32:35], v[128:131]\n"
  "ds_read_b64_tr_b16 a[96:97], v8 offset:40960\n"
  "v_mfma_f32_16x16x32_bf16 v[160:163], a[48:51], a[36:39], v[160:163]\n"
  "ds_read_b64_tr_b16 a[98:99], v8 offset:41984\n"
  "v_mfma_f32_16x16x32_bf16 v[192:195], a[48:51], a[40:43], v[192:195]\n"
  "ds_read_b128 a[80:83], v3 offset:0\n"
  "v_mfma_f32_16x16x32_bf16 v[224:227], a[48:51], a[44:47], v[224:227]\n"
  "ds_read_b64_tr_b16 a[100:101], v9 offset:40960\n"
  "buffer_load_dwordx4 v26, s[44:47], s64 offen offset:2048 lds\n"
  "v_mfma_f32_16x16x32_bf16 v[132:135], a[52:55], a[32:35], v[132:135]\n"
  "ds_read_b64_tr_b16 a[102:103], v9 offset:41984\n"
  "v_mfma_f32_16x16x32_bf16 v[164:167], a[52:55], a[36:39], v[164:167]\n"
  "v_mfma_f32_16x16x32_bf16 v[196:199], a[52:55], a[40:43], v[196:199]\n"
  "ds_read_b128 a[84:87], v3 offset:2048\n"
  "v_mfma_f32_16x16x32_bf16 v[228:231], a[52:55], a[44:47], v[228:231]\n"
  "ds_read_b64_tr_b16 a[104:105], v10 offset:40960\n"
  "buffer_load_dwordx4 v27, s[44:47], s64 offen offset:3072 lds\n"
  "v_mfma_f32_16x16x32_bf16 v[136:139], a[56:59], a[32:35], v[136:139]\n"
  "ds_read_b64_tr_b16 a[106:107], v10 offset:41984\n"
  "s_add_u32 m0, s60, 69632\n"
  "s_mov_b32 s64, 640\n"
  "v_mfma_f32_16x16x32_bf16 v[168:171], a[56:59], a[36:39], v[168:171]\n"
  "ds_read_b128 a[88:91], v3 offset:4096\n"
  "v_mfma_f32_16x16x32_bf16 v[200:203], a[56:59], a[40:43], v[200:203]\n"
  "ds_read_b64_tr_b16 a[108:109], v11 offset:40960\n"
  "v_mfma_f32_16x16x32_bf16 v[232:235], a[56:59], a[44:47], v[232:235]\n"
  "buffer_load_dwordx4 v32, s[40:43], s64 offen offset:0 lds\n"
  "v_mfma_f32_16x16x32_bf16 v[140:143], a[60:63], a[32:35], v[140:143]\n"
  "ds_read_b64_tr_b16 a[110:111], v11 offset:41984\n"
  "buffer_store_dwordx4 v[112:115], v53, s[88:91], s65 offen offset:0 sc1\n"
  "v_mfma_f32_16x16x32_bf16 v[172:175], a[60:63], a[36:39], v[172:175]\n"
  "ds_read_b128 a[92:95], v3 offset:6144\n"
  "v_mfma_f32_16x16x32_bf16 v[204:207], a[60:63], a[40:43], v[204:207]\n"
  "ds_read_b64_tr_b16 a[112:113], v12 offset:40960\n"
  "v_mfma_f32_16x16x32_bf16 v[236:239], a[60:63], a[44:47], v[236:239]\n"
  "ds_read_b64_tr_b16 a[114:115], v12 offset:41984\n"
  "buffer_load_dwordx4 v33, s[40:43], s64 offen offset:1024 lds\n"
  "v_mfma_f32_16x16x32_bf16 v[144:147], a[64:67], a[32:35], v[144:147]\n"
  "ds_read_b64_tr_b16 a[116:117], v13 offset:40960\n"
  "v_mfma_f32_16x16x32_bf16 v[176:179], a[64:67], a[36:39], v[176:179]\n"
  "v_mfma_f32_16x16x32_bf16 v[208:211], a[64:67], a[40:43], v[208:211]\n"
  "ds_read_b64_tr_b16 a[118:119], v13 offset:41984\n"
  "v_mfma_f32_16x16x32_bf16 v[240:243], a[64:67], a[44:47], v[240:243]\n"
  "ds_read_b64_tr_b16 a[120:121], v14 offset:40960\n"
  "buffer_load_dwordx4 v34, s[40:43], s64 offen offset:2048 lds\n"
  "v_mfma_f32_16x16x32_bf16 v[148:151], a[68:71], a[32:35], v[148:151]\n"
  "ds_read_b64_tr_b16 a[122:123], v14 offset:41984\n"
  "v_mfma_f32_16x16x32_bf16 v[180:183], a[68:71], a[36:39], v[180:183]\n"
  "ds_read_b64_tr_b16 a[124:125], v15 offset:40960\n"
  "v_mfma_f32_16x16x32_bf16 v[212:215], a[68:71], a[40:43], v[212:215]\n"
  "ds_read_b64_tr_b16 a[126:127], v15 offset:41984\n"
  "v_mfma_f32_16x16x32_bf16 v[244:247], a[68:71], a[44:47], v[244:247]\n"
  "buffer_load_dwordx4 v35, s[40:43], s64 offen offset:3072 lds\n"
  "v_mfma_f32_16x16x32_bf16 v[152:155], a[72:75], a[32:35], v[152:155]\n"
  "v_add_u32 v2, 0xfffe8000, v2\n"
  "v_mfma_f32_16x16x32_bf16 v[184:187], a[72:75], a[36:39], v[184:187]\n"
  "v_add_u32 v3, 0xfffe8000, v3\n"
  "v_mfma_f32_16x16x32_bf16 v[216:219], a[72:75], a[40:43], v[216:219]\n"
  "v_add_u32 v8, 0xfffe8000, v8\n"
  "v_mfma_f32_16x16x32_bf16 v[248:251], a[72:75], a[44:47], v[248:251]\n"
  "v_add_u32 v9, 0xfffe8000, v9\n"
  "v_add_u32 v10, 0xfffe8000, v10\n"
  "v_mfma_f32_16x16x32_bf16 v[156:159], a[76:79], a[32:35], v[156:159]\n"
  "v_add_u32 v11, 0xfffe8000, v11\n"
  "v_mfma_f32_16x16x32_bf16 v[188:191], a[76:79], a[36:39], v[188:191]\n"
  "v_add_u32 v12, 0xfffe8000, v12\n"
  "v_mfma_f32_16x16x32_bf16 v[220:223], a[76:79], a[40:43], v[220:223]\n"
  "v_add_u32 v13, 0xfffe8000, v13\n"
  "v_mfma_f32_16x16x32_bf16 v[252:255], a[76:79], a[44:47], v[252:255]\n"
  "v_add_u32 v14, 0xfffe8000, v14\n"
  "v_add_u32 v15, 0xfffe8000, v15\n"
  "s_waitcnt vmcnt(15) lgkmcnt(0)\n"
  "s_barrier\n"
  "buffer_atomic_add v7, v6, s[92:95], s69 offen sc1\n"
  "v_mfma_f32_16x16x32_bf16 v[128:131], a[96:99], a[80:83], v[128:131]\n"
  "ds_read_b64_tr_b16 a[48:49], v8 offset:32768\n"
  "s_add_u32 m0, s60, 114688\n"
  "s_mov_b32 s64, 768\n"
  "buffer_store_dwordx4 v[116:119], v53, s[88:91], s65 offen offset:1024 sc1\n"
  "v_mfma_f32_16x16x32_bf16 v[160:163], a[96:99], a[84:87], v[160:163]\n"
  "ds_read_b64_tr_b16 a[50:51], v8 offset:33792\n"
  "buffer_load_dwordx4 v28, s[40:43], s64 offen offset:0 lds\n"
  "v_mfma_f32_16x16x32_bf16 v[192:195], a[96:99], a[88:91], v[192:195]\n"
  "ds_read_b128 a[32:35], v2 offset:0\n"
  "v_mfma_f32_16x16x32_bf16 v[224:227], a[96:99], a[92:95], v[224:227]\n"
  "ds_read_b64_tr_b16 a[52:53], v9 offset:32768\n"
  "v_mfma_f32_16x16x32_bf16 v[132:135], a[100:103], a[80:83], v[132:135]\n"
  "ds_read_b64_tr_b16 a[54:55], v9 offset:33792\n"
  "v_mfma_f32_16x16x32_bf16 v[164:167], a[100:103], a[84:87], v[164:167]\n"
  "buffer_load_dwordx4 v29, s[40:43], s64 offen offset:1024 lds\n"
  "v_mfma_f32_16x16x32_bf16 v[196:199], a[100:103], a[88:91], v[196:199]\n"
  "ds_read_b128 a[36:39], v2 offset:2048\n"
  "v_mfma_f32_16x16x32_bf16 v[228:231], a[100:103], a[92:95], v[228:231]\n"
  "ds_read_b64_tr_b16 a[56:57], v10 offset:32768\n"
  "v_mfma_f32_16x16x32_bf16 v[136:139], a[104:107], a[80:83], v[136:139]\n"
  "ds_read_b64_tr_b16 a[58:59], v10 offset:33792\n"
  "buffer_store_dwordx4 v[120:123], v53, s[88:91], s65 offen offset:2048 sc1\n"
  "v_mfma_f32_16x16x32_bf16 v[168:171], a[104:107], a[84:87], v[168:171]\n"
  "ds_read_b128 a[40:43], v2 offset:4096\n"
  "buffer_load_dwordx4 v30, s[40:43], s64 offen offset:2048 lds\n"
  "v_mfma_f32_16x16x32_bf16 v[200:203], a[104:107], a[88:91], v[200:203]\n"
  "ds_read_b64_tr_b16 a[60:61], v11 offset:32768\n"
  "v_mfma_f32_16x16x32_bf16 v[232:235], a[104:107], a[92:95], v[232:235]\n"
  "v_mfma_f32_16x16x32_bf16 v[140:143], a[108:111], a[80:83], v[140:143]\n"
  "ds_read_b64_tr_b16 a[62:63], v11 offset:33792\n"
  "v_mfma_f32_16x16x32_bf16 v[172:175], a[108:111], a[84:87], v[172:175]\n"
  "ds_read_b128 a[44:47], v2 offset:6144\n"
  "buffer_load_dwordx4 v31, s[40:43], s64 offen offset:3072 lds\n"
  "v_mfma_f32_16x16x32_bf16 v[204:207], a[108:111], a[88:91], v[204:207]\n"
  "ds_read_b64_tr_b16 a[64:65], v12 offset:32768\n"
  "s_add_u32 m0, s62, 147456\n"
  "s_mov_b32 s64, 3145728\n"
  "v_mfma_f32_16x16x32_bf16 v[236:239], a[108:111], a[92:95], v[236:239]\n"
  "ds_read_b64_tr_b16 a[66:67], v12 offset:33792\n"
  "v_mfma_f32_16x16x32_bf16 v[144:147], a[112:115], a[80:83], v[144:147]\n"
  "ds_read_b64_tr_b16 a[68:69], v13 offset:32768\n"
  "v_mfma_f32_16x16x32_bf16 v[176:179], a[112:115], a[84:87], v[176:179]\n"
  "buffer_load_dwordx4 v24, s[44:47], s64 offen offset:0 lds\n"
  "v_mfma_f32_16x16x32_bf16 v[208:211], a[112:115], a[88:91], v[208:211]\n"
  "ds_read_b64_tr_b16 a[70:71], v13 offset:33792\n"
  "v_mfma_f32_16x16x32_bf16 v[240:243], a[112:115], a[92:95], v[240:243]\n"
  "ds_read_b64_tr_b16 a[72:73], v14 offset:32768\n"
  "v_mfma_f32_16x16x32_bf16 v[148:151], a[116:119], a[80:83], v[148:151]\n"
  "ds_read_b64_tr_b16 a[74:75], v14 offset:33792\n"
  "v_mfma_f32_16x16x32_bf16 v[180:183], a[116:119], a[84:87], v[180:183]\n"
  "ds_read_b64_tr_b16 a[76:77], v15 offset:32768\n"
  "buffer_load_dwordx4 v25, s[44:47], s64 offen offset:1024 lds\n"
  "v_mfma_f32_16x16x32_bf16 v[212:215], a[116:119], a[88:91], v[212:215]\n"
  "ds_read_b64_tr_b16 a[78:79], v15 offset:33792\n"
  "buffer_store_dwordx4 v[124:127], v53, s[88:91], s65 offen offset:3072 sc1\n"
  "v_mfma_f32_16x16x32_bf16 v[244:247], a[116:119], a[92:95], v[244:247]\n"
  "v_mfma_f32_16x16x32_bf16 v[152:155], a[120:123], a[80:83], v[152:155]\n"
  "v_mfma_f32_16x16x32_bf16 v[184:187], a[120:123], a[84:87], v[184:187]\n"
  "buffer_load_dwordx4 v26, s[44:47], s64 offen offset:2048 lds\n"
  "v_mfma_f32_16x16x32_bf16 v[216:219], a[120:123], a[88:91], v[216:219]\n"
  "v_mfma_f32_16x16x32_bf16 v[248:251], a[120:123], a[92:95], v[248:251]\n"
  "v_mfma_f32_16x16x32_bf16 v[156:159], a[124:127], a[80:83], v[156:159]\n"
  "v_mfma_f32_16x16x32_bf16 v[188:191], a[124:127], a[84:87], v[188:191]\n"
  "buffer_load_dwordx4 v27, s[44:47], s64 offen offset:3072 lds\n"
  "v_mfma_f32_16x16x32_bf16 v[220:223], a[124:127], a[88:91], v[220:223]\n"
  "s_add_u32 m0, s60, 118784\n"
  "s_mov_b32 s64, 768\n"
  "v_mfma_f32_16x16x32_bf16 v[252:255], a[124:127], a[92:95], v[252:255]\n"
  "s_waitcnt lgkmcnt(0)\n"
  "v_mfma_f32_16x16x32_bf16 v[128:131], a[48:51], a[32:35], v[128:131]\n"
  "ds_read_b64_tr_b16 a[96:97], v8 offset:40960\n"
  "v_mfma_f32_16x16x32_bf16 v[160:163], a[48:51], a[36:39], v[160:163]\n"
  "ds_read_b64_tr_b16 a[98:99], v8 offset:41984\n"
  "buffer_load_dwordx4 v32, s[40:43], s64 offen offset:0 lds\n"
  "v_mfma_f32_16x16x32_bf16 v[192:195], a[48:51], a[40:43], v[192:195]\n"
  "ds_read_b128 a[80:83], v3 offset:0\n"
  "v_mfma_f32_16x16x32_bf16 v[224:227], a[48:51], a[44:47], v[224:227]\n"
  "ds_read_b64_tr_b16 a[100:101], v9 offset:40960\n"
  "v_mfma_f32_16x16x32_bf16 v[132:135], a[52:55], a[32:35], v[132:135]\n"
  "ds_read_b64_tr_b16 a[102:103], v9 offset:41984\n"
  "s_add_u32 s65, s74, 16384\n"
  "v_mfma_f32_16x16x32_bf16 v[164:167], a[52:55], a[36:39], v[164:167]\n"
  "buffer_load_dwordx4 v33, s[40:43], s64 offen offset:1024 lds\n"
  "v_mfma_f32_16x16x32_bf16 v[196:199], a[52:55], a[40:43], v[196:199]\n"
  "ds_read_b128 a[84:87], v3 offset:2048\n"
  "v_accvgpr_read_b32 v64, a128\n"
  "v_mfma_f32_16x16x32_bf16 v[228:231], a[52:55], a[44:47], v[228:231]\n"
  "ds_read_b64_tr_b16 a[104:105], v10 offset:40960\n"
  "v_accvgpr_read_b32 v65, a129\n"
  "v_mfma_f32_16x16x32_bf16 v[136:139], a[56:59], a[32:35], v[136:139]\n"
  "ds_read_b64_tr_b16 a[106:107], v10 offset:41984\n"
  "v_mfma_f32_16x16x32_bf16 v[168:171], a[56:59], a[36:39], v[168:171]\n"
  "ds_read_b128 a[88:91], v3 offset:4096\n"
  "buffer_load_dwordx4 v34, s[40:43], s64 offen offset:2048 lds\n"
  "v_cvt_pk_bf16_f32 v64, v64, v65\n"
  "v_mfma_f32_16x16x32_bf16 v[200:203], a[56:59], a[40:43], v[200:203]\n"
  "ds_read_b64_tr_b16 a[108:109], v11 offset:40960\n"
  "v_accvgpr_read_b32 v66, a130\n"
  "v_mfma_f32_16x16x32_bf16 v[232:235], a[56:59], a[44:47], v[232:235]\n"
  "v_mfma_f32_16x16x32_bf16 v[140:143], a[60:63], a[32:35], v[140:143]\n"
  "ds_read_b64_tr_b16 a[110:111], v11 offset:41984\n"
  "v_accvgpr_read_b32 v67, a131\n"
  "v_mfma_f32_16x16x32_bf16 v[172:175], a[60:63], a[36:39], v[172:175]\n"
  "ds_read_b128 a[92:95], v3 offset:6144\n"
  "buffer_load_dwordx4 v35, s[40:43], s64 offen offset:3072 lds\n"
  "v_cvt_pk_bf16_f32 v65, v66, v67\n"
  "v_mfma_f32_16x16x32_bf16 v[204:207], a[60:63], a[40:43], v[204:207]\n"
  "ds_read_b64_tr_b16 a[112:113], v12 offset:40960\n"
  "v_accvgpr_read_b32 v68, a132\n"
  "v_mfma_f32_16x16x32_bf16 v[236:239], a[60:63], a[44:47], v[236:239]\n"
  "ds_read_b64_tr_b16 a[114:115], v12 offset:41984\n"
  "v_mfma_f32_16x16x32_bf16 v[144:147], a[64:67], a[32:35], v[144:147]\n"
  "ds_read_b64_tr_b16 a[116:117], v13 offset:40960\n"
  "v_accvgpr_read_b32 v69, a133\n"
  "v_mfma_f32_16x16x32_bf16 v[176:179], a[64:67], a[36:39], v[176:179]\n"
  "v_cvt_pk_bf16_f32 v66, v68, v69\n"
  "v_mfma_f32_16x16x32_bf16 v[208:211], a[64:67], a[40:43], v[208:211]\n"
  "ds_read_b64_tr_b16 a[118:119], v13 offset:41984\n"
  "v_mfma_f32_16x16x32_bf16 v[240:243], a[64:67], a[44:47], v[240:243]\n"
  "ds_read_b64_tr_b16 a[120:121], v14 offset:40960\n"
  "v_accvgpr_read_b32 v70, a134\n"
  "v_mfma_f32_16x16x32_bf16 v[148:151], a[68:71], a[32:35], v[148:151]\n"
  "ds_read_b64_tr_b16 a[122:123], v14 offset:41984\n"
  "v_accvgpr_read_b32 v71, a135\n"
  "v_mfma_f32_16x16x32_bf16 v[180:183], a[68:71], a[36:39], v[180:183]\n"
  "ds_read_b64_tr_b16 a[124:125], v15 offset:40960\n"
  "v_mfma_f32_16x16x32_bf16 v[212:215], a[68:71], a[40:43], v[212:215]\n"
  "ds_read_b64_tr_b16 a[126:127], v15 offset:41984\n"
  "v_cvt_pk_bf16_f32 v67, v70, v71\n"
  "v_mfma_f32_16x16x32_bf16 v[244:247], a[68:71], a[44:47], v[244:247]\n"
  "buffer_store_dwordx4 v[64:67], v52, s[88:91], s65 offen offset:0 sc1\n"
  "v_mfma_f32_16x16x32_bf16 v[152:155], a[72:75], a[32:35], v[152:155]\n"
  "v_add_u32 v2, 0xc000, v2\n"
  "v_accvgpr_read_b32 v72, a136\n"
  "v_mfma_f32_16x16x32_bf16 v[184:187], a[72:75], a[36:39], v[184:187]\n"
  "v_add_u32 v3, 0xc000, v3\n"
  "v_mfma_f32_16x16x32_bf16 v[216:219], a[72:75], a[40:43], v[216:219]\n"
  "v_add_u32 v8, 0xc000, v8\n"
  "v_accvgpr_read_b32 v73, a137\n"
  "v_mfma_f32_16x16x32_bf16 v[248:251], a[72:75], a[44:47], v[248:251]\n"
  "v_add_u32 v9, 0xc000, v9\n"
  "v_add_u32 v10, 0xc000, v10\n"
  "v_mfma_f32_16x16x32_bf16 v[156:159], a[76:79], a[32:35], v[156:159]\n"
  "v_add_u32 v11, 0xc000, v11\n"
  "v_cvt_pk_bf16_f32 v72, v72, v73\n"
  "v_mfma_f32_16x16x32_bf16 v[188:191], a[76:79], a[36:39], v[188:191]\n"
  "v_add_u32 v12, 0xc000, v12\n"
  "v_accvgpr_read_b32 v74, a138\n"
  "v_mfma_f32_16x16x32_bf16 v[220:223], a[76:79], a[40:43], v[220:223]\n"
  "v_add_u32 v13, 0xc000, v13\n"
  "v_accvgpr_read_b32 v75, a139\n"
  "v_mfma_f32_16x16x32_bf16 v[252:255], a[76:79], a[44:47], v[252:255]\n"
  "v_add_u32 v14, 0xc000, v14\n"
  "v_add_u32 v15, 0xc000, v15\n"
  "s_waitcnt vmcnt(17) lgkmcnt(0)\n"
  "s_barrier\n"
  "v_mfma_f32_16x16x32_bf16 v[128:131], a[96:99], a[80:83], v[128:131]\n"
  "ds_read_b64_tr_b16 a[48:49], v8 offset:32768\n"
  "s_add_u32 m0, s60, 16384\n"
  "s_mov_b32 s64, 896\n"
  "v_cvt_pk_bf16_f32 v73, v74, v75\n"
  "v_mfma_f32_16x16x32_bf16 v[160:163], a[96:99], a[84:87], v[160:163]\n"
  "ds_read_b64_tr_b16 a[50:51], v8 offset:33792\n"
  "buffer_load_dwordx4 v28, s[40:43], s64 offen offset:0 lds\n"
  "v_accvgpr_read_b32 v76, a140\n"
  "v_mfma_f32_16x16x32_bf16 v[192:195], a[96:99], a[88:91], v[192:195]\n"
  "ds_read_b128 a[32:35], v2 offset:0\n"
  "v_accvgpr_read_b32 v77, a141\n"
  "v_mfma_f32_16x16x32_bf16 v[224:227], a[96:99], a[92:95], v[224:227]\n"
  "ds_read_b64_tr_b16 a[52:53], v9 offset:32768\n"
  "v_mfma_f32_16x16x32_bf16 v[132:135], a[100:103], a[80:83], v[132:135]\n"
  "ds_read_b64_tr_b16 a[54:55], v9 offset:33792\n"
  "v_cvt_pk_bf16_f32 v74, v76, v77\n"
  "v_mfma_f32_16x16x32_bf16 v[164:167], a[100:103], a[84:87], v[164:167]\n"
  "buffer_load_dwordx4 v29, s[40:43], s64 offen offset:1024 lds\n"
  "v_accvgpr_read_b32 v78, a142\n"
  "v_mfma_f32_16x16x32_bf16 v[196:199], a[100:103], a[88:91], v[196:199]\n"
  "ds_read_b128 a[36:39], v2 offset:2048\n"
  "v_mfma_f32_16x16x32_bf16 v[228:231], a[100:103], a[92:95], v[228:231]\n"
  "ds_read_b64_tr_b16 a[56:57], v10 offset:32768\n"
  "v_accvgpr_read_b32 v79, a143\n"
  "v_mfma_f32_16x16x32_bf16 v[136:139], a[104:107], a[80:83], v[136:139]\n"
  "ds_read_b64_tr_b16 a[58:59], v10 offset:33792\n"
  "v_cvt_pk_bf16_f32 v75, v78, v79\n"
  "v_mfma_f32_16x16x32_bf16 v[168:171], a[104:107], a[84:87], v[168:171]\n"
  "ds_read_b128 a[40:43], v2 offset:4096\n"
  "buffer_load_dwordx4 v30, s[40:43], s64 offen offset:2048 lds\n"
  "v_mfma_f32_16x16x32_bf16 v[200:203], a[104:107], a[88:91], v[200:203]\n"
  "ds_read_b64_tr_b16 a[60:61], v11 offset:32768\n"
  "buffer_store_dwordx4 v[72:75], v52, s[88:91], s65 offen offset:1024 sc1\n"
  "v_mfma_f32_16x16x32_bf16 v[232:235], a[104:107], a[92:95], v[232:235]\n"
  "v_accvgpr_read_b32 v80, a144\n"
  "v_mfma_f32_16x16x32_bf16 v[140:143], a[108:111], a[80:83], v[140:143]\n"
  "ds_read_b64_tr_b16 a[62:63], v11 offset:33792\n"
  "v_accvgpr_read_b32 v81, a145\n"
  "v_mfma_f32_16x16x32_bf16 v[172:175], a[108:111], a[84:87], v[172:175]\n"
  "ds_read_b128 a[44:47], v2 offset:6144\n"
  "buffer_load_dwordx4 v31, s[40:43], s64 offen offset:3072 lds\n"
  "v_mfma_f32_16x16x32_bf16 v[204:207], a[108:111], a[88:91], v[204:207]\n"
  "ds_read_b64_tr_b16 a[64:65], v12 offset:32768\n"
  "s_add_u32 m0, s62, 49152\n"
  "s_mov_b32 s64, 3670016\n"
  "v_cvt_pk_bf16_f32 v80, v80, v81\n"
  "v_mfma_f32_16x16x32_bf16 v[236:239], a[108:111], a[92:95], v[236:239]\n"
  "ds_read_b64_tr_b16 a[66:67], v12 offset:33792\n"
  "v_accvgpr_read_b32 v82, a146\n"
  "v_mfma_f32_16x16x32_bf16 v[144:147], a[112:115], a[80:83], v[144:147]\n"
  "ds_read_b64_tr_b16 a[68:69], v13 offset:32768\n"
  "v_mfma_f32_16x16x32_bf16 v[176:179], a[112:115], a[84:87], v[176:179]\n"
  "buffer_load_dwordx4 v24, s[44:47], s64 offen offset:0 lds\n"
  "v_accvgpr_read_b32 v83, a147\n"
  "v_mfma_f32_16x16x32_bf16 v[208:211], a[112:115], a[88:91], v[208:211]\n"
  "ds_read_b64_tr_b16 a[70:71], v13 offset:33792\n"
  "v_cvt_pk_bf16_f32 v81, v82, v83\n"
  "v_mfma_f32_16x16x32_bf16 v[240:243], a[112:115], a[92:95], v[240:243]\n"
  "ds_read_b64_tr_b16 a[72:73], v14 offset:32768\n"
  "v_mfma_f32_16x16x32_bf16 v[148:151], a[116:119], a[80:83], v[148:151]\n"
  "ds_read_b64_tr_b16 a[74:75], v14 offset:33792\n"
  "v_accvgpr_read_b32 v84, a148\n"
  "v_mfma_f32_16x16x32_bf16 v[180:183], a[116:119], a[84:87], v[180:183]\n"
  "ds_read_b64_tr_b16 a[76:77], v15 offset:32768\n"
  "buffer_load_dwordx4 v25, s[44:47], s64 offen offset:1024 lds\n"
  "v_accvgpr_read_b32 v85, a149\n"
  "v_mfma_f32_16x16x32_bf16 v[212:215], a[116:119], a[88:91], v[212:215]\n"
  "ds_read_b64_tr_b16 a[78:79], v15 offset:33792\n"
  "v_cvt_pk_bf16_f32 v82, v84, v85\n"
  "v_mfma_f32_16x16x32_bf16 v[244:247], a[116:119], a[92:95], v[244:247]\n"
  "v_mfma_f32_16x16x32_bf16 v[152:155], a[120:123], a[80:83], v[152:155]\n"
  "v_accvgpr_read_b32 v86, a150\n"
  "v_mfma_f32_16x16x32_bf16 v[184:187], a[120:123], a[84:87], v[184:187]\n"
  "buffer_load_dwordx4 v26, s[44:47], s64 offen offset:2048 lds\n"
  "v_accvgpr_read_b32 v87, a151\n"
  "v_mfma_f32_16x16x32_bf16 v[216:219], a[120:123], a[88:91], v[216:219]\n"
  "v_mfma_f32_16x16x32_bf16 v[248:251], a[120:123], a[92:95], v[248:251]\n"
  "v_cvt_pk_bf16_f32 v83, v86, v87\n"
  "v_mfma_f32_16x16x32_bf16 v[156:159], a[124:127], a[80:83], v[156:159]\n"
  "buffer_store_dwordx4 v[80:83], v52, s[88:91], s65 offen offset:2048 sc1\n"
  "v_mfma_f32_16x16x32_bf16 v[188:191], a[124:127], a[84:87], v[188:191]\n"
  "buffer_load_dwordx4 v27, s[44:47], s64 offen offset:3072 lds\n"
  "v_mfma_f32_16x16x32_bf16 v[220:223], a[124:127], a[88:91], v[220:223]\n"
  "s_add_u32 m0, s60, 20480\n"
  "s_mov_b32 s64, 896\n"
  "v_accvgpr_read_b32 v88, a152\n"
  "v_mfma_f32_16x16x32_bf16 v[252:255], a[124:127], a[92:95], v[252:255]\n"
  "v_accvgpr_read_b32 v89, a153\n"
  "s_waitcnt lgkmcnt(0)\n"
  "v_mfma_f32_16x16x32_bf16 v[128:131], a[48:51], a[32:35], v[128:131]\n"
  "ds_read_b64_tr_b16 a[96:97], v8 offset:40960\n"
  "v_cvt_pk_bf16_f32 v88, v88, v89\n"
  "v_mfma_f32_16x16x32_bf16 v[160:163], a[48:51], a[36:39], v[160:163]\n"
  "ds_read_b64_tr_b16 a[98:99], v8 offset:41984\n"
  "buffer_load_dwordx4 v32, s[40:43], s64 offen offset:0 lds\n"
  "v_mfma_f32_16x16x32_bf16 v[192:195], a[48:51], a[40:43], v[192:195]\n"
  "ds_read_b128 a[80:83], v3 offset:0\n"
  "v_accvgpr_read_b32 v90, a154\n"
  "v_mfma_f32_16x16x32_bf16 v[224:227], a[48:51], a[44:47], v[224:227]\n"
  "ds_read_b64_tr_b16 a[100:101], v9 offset:40960\n"
  "v_accvgpr_read_b32 v91, a155\n"
  "v_mfma_f32_16x16x32_bf16 v[132:135], a[52:55], a[32:35], v[132:135]\n"
  "ds_read_b64_tr_b16 a[102:103], v9 offset:41984\n"
  "v_mfma_f32_16x16x32_bf16 v[164:167], a[52:55], a[36:39], v[164:167]\n"
  "buffer_load_dwordx4 v33, s[40:43], s64 offen offset:1024 lds\n"
  "v_cvt_pk_bf16_f32 v89, v90, v91\n"
  "v_mfma_f32_16x16x32_bf16 v[196:199], a[52:55], a[40:43], v[196:199]\n"
  "ds_read_b128 a[84:87], v3 offset:2048\n"
  "v_accvgpr_read_b32 v92, a156\n"
  "v_mfma_f32_16x16x32_bf16 v[228:231], a[52:55], a[44:47], v[228:231]\n"
  "ds_read_b64_tr_b16 a[104:105], v10 offset:40960\n"
  "v_mfma_f32_16x16x32_bf16 v[136:139], a[56:59], a[32:35], v[136:139]\n"
  "ds_read_b64_tr_b16 a[106:107], v10 offset:41984\n"
  "v_accvgpr_read_b32 v93, a157\n"
  "v_mfma_f32_16x16x32_bf16 v[168:171], a[56:59], a[36:39], v[168:171]\n"
  "ds_read_b128 a[88:91], v3 offset:4096\n"
  "buffer_load_dwordx4 v34, s[40:43], s64 offen offset:2048 lds\n"
  "v_cvt_pk_bf16_f32 v90, v92, v93\n"
  "v_mfma_f32_16x16x32_bf16 v[200:203], a[56:59], a[40:43], v[200:203]\n"
  "ds_read_b64_tr_b16 a[108:109], v11 offset:40960\n"
  "v_accvgpr_read_b32 v94, a158\n"
  "v_mfma_f32_16x16x32_bf16 v[232:235], a[56:59], a[44:47], v[232:235]\n"
  "v_mfma_f32_16x16x32_bf16 v[140:143], a[60:63], a[32:35], v[140:143]\n"
  "ds_read_b64_tr_b16 a[110:111], v11 offset:41984\n"
  "v_accvgpr_read_b32 v95, a159\n"
  "v_mfma_f32_16x16x32_bf16 v[172:175], a[60:63], a[36:39], v[172:175]\n"
  "ds_read_b128 a[92:95], v3 offset:6144\n"
  "buffer_load_dwordx4 v35, s[40:43], s64 offen offset:3072 lds\n"
  "v_cvt_pk_bf16_f32 v91, v94, v95\n"
  "v_mfma_f32_16x16x32_bf16 v[204:207], a[60:63], a[40:43], v[204:207]\n"
  "ds_read_b64_tr_b16 a[112:113], v12 offset:40960\n"
  "v_mfma_f32_16x16x32_bf16 v[236:239], a[60:63], a[44:47], v[236:239]\n"
  "ds_read_b64_tr_b16 a[114:115], v12 offset:41984\n"
  "buffer_store_dwordx4 v[88:91], v52, s[88:91], s65 offen offset:3072 sc1\n"
  "v_mfma_f32_16x16x32_bf16 v[144:147], a[64:67], a[32:35], v[144:147]\n"
  "ds_read_b64_tr_b16 a[116:117], v13 offset:40960\n"
  "v_accvgpr_read_b32 v96, a160\n"
  "v_mfma_f32_16x16x32_bf16 v[176:179], a[64:67], a[36:39], v[176:179]\n"
  "v_mfma_f32_16x16x32_bf16 v[208:211], a[64:67], a[40:43], v[208:211]\n"
  "ds_read_b64_tr_b16 a[118:119], v13 offset:41984\n"
  "v_accvgpr_read_b32 v97, a161\n"
  "v_mfma_f32_16x16x32_bf16 v[240:243], a[64:67], a[44:47], v[240:243]\n"
  "ds_read_b64_tr_b16 a[120:121], v14 offset:40960\n"
  "v_cvt_pk_bf16_f32 v96, v96, v97\n"
  "v_mfma_f32_16x16x32_bf16 v[148:151], a[68:71], a[32:35], v[148:151]\n"
  "ds_read_b64_tr_b16 a[122:123], v14 offset:41984\n"
  "v_accvgpr_read_b32 v98, a162\n"
  "v_mfma_f32_16x16x32_bf16 v[180:183], a[68:71], a[36:39], v[180:183]\n"
  "ds_read_b64_tr_b16 a[124:125], v15 offset:40960\n"
  "v_mfma_f32_16x16x32_bf16 v[212:215], a[68:71], a[40:43], v[212:215]\n"
  "ds_read_b64_tr_b16 a[126:127], v15 offset:41984\n"
  "v_accvgpr_read_b32 v99, a163\n"
  "v_mfma_f32_16x16x32_bf16 v[244:247], a[68:71], a[44:47], v[244:247]\n"
  "v_cvt_pk_bf16_f32 v97, v98, v99\n"
  "v_mfma_f32_16x16x32_bf16 v[152:155], a[72:75], a[32:35], v[152:155]\n"
  "v_add_u32 v2, 0xc000, v2\n"
  "v_mfma_f32_16x16x32_bf16 v[184:187], a[72:75], a[36:39], v[184:187]\n"
  "v_add_u32 v3, 0xc000, v3\n"
  "v_accvgpr_read_b32 v100, a164\n"
  "v_mfma_f32_16x16x32_bf16 v[216:219], a[72:75], a[40:43], v[216:219]\n"
  "v_add_u32 v8, 0xc000, v8\n"
  "v_accvgpr_read_b32 v101, a165\n"
  "v_mfma_f32_16x16x32_bf16 v[248:251], a[72:75], a[44:47], v[248:251]\n"
  "v_add_u32 v9, 0xc000, v9\n"
  "v_add_u32 v10, 0xc000, v10\n"
  "v_mfma_f32_16x16x32_bf16 v[156:159], a[76:79], a[32:35], v[156:159]\n"
  "v_add_u32 v11, 0xc000, v11\n"
  "v_cvt_pk_bf16_f32 v98, v100, v101\n"
  "v_mfma_f32_16x16x32_bf16 v[188:191], a[76:79], a[36:39], v[188:191]\n"
  "v_add_u32 v12, 0xc000, v12\n"
  "v_accvgpr_read_b32 v102, a166\n"
  "v_mfma_f32_16x16x32_bf16 v[220:223], a[76:79], a[40:43], v[220:223]\n"
  "v_add_u32 v13, 0xc000, v13\n"
  "v_accvgpr_read_b32 v103, a167\n"
  "v_mfma_f32_16x16x32_bf16 v[252:255], a[76:79], a[44:47], v[252:255]\n"
  "v_add_u32 v14, 0xc000, v14\n"
  "v_add_u32 v15, 0xc000, v15\n"
  "s_waitcnt vmcnt(16) lgkmcnt(0)\n"
  "s_barrier\n"
  "v_mfma_f32_16x16x32_bf16 v[128:131], a[96:99], a[80:83], v[128:131]\n"
  "ds_read_b64_tr_b16 a[48:49], v8 offset:32768\n"
  "s_add_u32 m0, s60, 65536\n"
  "s_mov_b32 s64, 1024\n"
  "v_cvt_pk_bf16_f32 v99, v102, v103\n"
  "v_mfma_f32_16x16x32_bf16 v[160:163], a[96:99], a[84:87], v[160:163]\n"
  "ds_read_b64_tr_b16 a[50:51], v8 offset:33792\n"
  "buffer_load_dwordx4 v28, s[40:43], s64 offen offset:0 lds\n"
  "v_mfma_f32_16x16x32_bf16 v[192:195], a[96:99], a[88:91], v[192:195]\n"
  "ds_read_b128 a[32:35], v2 offset:0\n"
  "buffer_store_dwordx4 v[96:99], v53, s[88:91], s65 offen offset:0 sc1\n"
  "v_mfma_f32_16x16x32_bf16 v[224:227], a[96:99], a[92:95], v[224:227]\n"
  "ds_read_b64_tr_b16 a[52:53], v9 offset:32768\n"
  "v_accvgpr_read_b32 v104, a168\n"
  "v_mfma_f32_16x16x32_bf16 v[132:135], a[100:103], a[80:83], v[132:135]\n"
  "ds_read_b64_tr_b16 a[54:55], v9 offset:33792\n"
  "v_accvgpr_read_b32 v105, a169\n"
  "v_mfma_f32_16x16x32_bf16 v[164:167], a[100:103], a[84:87], v[164:167]\n"
  "buffer_load_dwordx4 v29, s[40:43], s64 offen offset:1024 lds\n"
  "v_mfma_f32_16x16x32_bf16 v[196:199], a[100:103], a[88:91], v[196:199]\n"
  "ds_read_b128 a[36:39], v2 offset:2048\n"
  "v_cvt_pk_bf16_f32 v104, v104, v105\n"
  "v_mfma_f32_16x16x32_bf16 v[228:231], a[100:103], a[92:95], v[228:231]\n"
  "ds_read_b64_tr_b16 a[56:57], v10 offset:32768\n"
  "v_accvgpr_read_b32 v106, a170\n"
  "v_mfma_f32_16x16x32_bf16 v[136:139], a[104:107], a[80:83], v[136:139]\n"
  "ds_read_b64_tr_b16 a[58:59], v10 offset:33792\n"
  "v_accvgpr_read_b32 v107, a171\n"
  "v_mfma_f32_16x16x32_bf16 v[168:171], a[104:107], a[84:87], v[168:171]\n"
  "ds_read_b128 a[40:43], v2 offset:4096\n"
  "buffer_load_dwordx4 v30, s[40:43], s64 offen offset:2048 lds\n"
  "v_mfma_f32_16x16x32_bf16 v[200:203], a[104:107], a[88:91], v[200:203]\n"
  "ds_read_b64_tr_b16 a[60:61], v11 offset:32768\n"
  "v_cvt_pk_bf16_f32 v105, v106, v107\n"
  "v_mfma_f32_16x16x32_bf16 v[232:235], a[104:107], a[92:95], v[232:235]\n"
  "v_accvgpr_read_b32 v108, a172\n"
  "v_mfma_f32_16x16x32_bf16 v[140:143], a[108:111], a[80:83], v[140:143]\n"
  "ds_read_b64_tr_b16 a[62:63], v11 offset:33792\n"
  "v_mfma_f32_16x16x32_bf16 v[172:175], a[108:111], a[84:87], v[172:175]\n"
  "ds_read_b128 a[44:47], v2 offset:6144\n"
  "buffer_load_dwordx4 v31, s[40:43], s64 offen offset:3072 lds\n"
  "v_accvgpr_read_b32 v109, a173\n"
  "v_mfma_f32_16x16x32_bf16 v[204:207], a[108:111], a[88:91], v[204:207]\n"
  "ds_read_b64_tr_b16 a[64:65], v12 offset:32768\n"
  "s_add_u32 m0, s62, 98304\n"
  "s_mov_b32 s64, 4194304\n"
  "v_cvt_pk_bf16_f32 v106, v108, v109\n"
  "v_mfma_f32_16x16x32_bf16 v[236:239], a[108:111], a[92:95], v[236:239]\n"
  "ds_read_b64_tr_b16 a[66:67], v12 offset:33792\n"
  "v_mfma_f32_16x16x32_bf16 v[144:147], a[112:115], a[80:83], v[144:147]\n"
  "ds_read_b64_tr_b16 a[68:69], v13 offset:32768\n"
  "v_accvgpr_read_b32 v110, a174\n"
  "v_mfma_f32_16x16x32_bf16 v[176:179], a[112:115], a[84:87], v[176:179]\n"
  "buffer_load_dwordx4 v24, s[44:47], s64 offen offset:0 lds\n"
  "v_accvgpr_read_b32 v111, a175\n"
  "v_mfma_f32_16x16x32_bf16 v[208:211], a[112:115], a[88:91], v[208:211]\n"
  "ds_read_b64_tr_b16 a[70:71], v13 offset:33792\n"
  "v_cvt_pk_bf16_f32 v107, v110, v111\n"
  "v_mfma_f32_16x16x32_bf16 v[240:243], a[112:115], a[92:95], v[240:243]\n"
  "ds_read_b64_tr_b16 a[72:73], v14 offset:32768\n"
  "v_mfma_f32_16x16x32_bf16 v[148:151], a[116:119], a[80:83], v[148:151]\n"
  "ds_read_b64_tr_b16 a[74:75], v14 offset:33792\n"
  "buffer_store_dwordx4 v[104:107], v53, s[88:91], s65 offen offset:1024 sc1\n"
  "v_mfma_f32_16x16x32_bf16 v[180:183], a[116:119], a[84:87], v[180:183]\n"
  "ds_read_b64_tr_b16 a[76:77], v15 offset:32768\n"
  "buffer_load_dwordx4 v25, s[44:47], s64 offen offset:1024 lds\n"
  "v_accvgpr_read_b32 v112, a176\n"
  "v_mfma_f32_16x16x32_bf16 v[212:215], a[116:119], a[88:91], v[212:215]\n"
  "ds_read_b64_tr_b16 a[78:79], v15 offset:33792\n"
  "v_mfma_f32_16x16x32_bf16 v[244:247], a[116:119], a[92:95], v[244:247]\n"
  "v_accvgpr_read_b32 v113, a177\n"
  "v_mfma_f32_16x16x32_bf16 v[152:155], a[120:123], a[80:83], v[152:155]\n"
  "v_cvt_pk_bf16_f32 v112, v112, v113\n"
  "v_mfma_f32_16x16x32_bf16 v[184:187], a[120:123], a[84:87], v[184:187]\n"
  "buffer_load_dwordx4 v26, s[44:47], s64 offen offset:2048 lds\n"
  "v_mfma_f32_16x16x32_bf16 v[216:219], a[120:123], a[88:91], v[216:219]\n"
  "v_accvgpr_read_b32 v114, a178\n"
  "v_mfma_f32_16x16x32_bf16 v[248:251], a[120:123], a[92:95], v[248:251]\n"
  "v_accvgpr_read_b32 v115, a179\n"
  "v_mfma_f32_16x16x32_bf16 v[156:159], a[124:127], a[80:83], v[156:159]\n"
  "v_cvt_pk_bf16_f32 v113, v114, v115\n"
  "v_mfma_f32_16x16x32_bf16 v[188:191], a[124:127], a[84:87], v[188:191]\n"
  "buffer_load_dwordx4 v27, s[44:47], s64 offen offset:3072 lds\n"
  "v_mfma_f32_16x16x32_bf16 v[220:223], a[124:127], a[88:91], v[220:223]\n"
  "s_add_u32 m0, s60, 69632\n"
  "s_mov_b32 s64, 1024\n"
  "v_accvgpr_read_b32 v116, a180\n"
  "v_mfma_f32_16x16x32_bf16 v[252:255], a[124:127], a[92:95], v[252:255]\n"
  "v_accvgpr_read_b32 v117, a181\n"
  "s_waitcnt lgkmcnt(0)\n"
  "v_mfma_f32_16x16x32_bf16 v[128:131], a[48:51], a[32:35], v[128:131]\n"
  "ds_read_b64_tr_b16 a[96:97], v8 offset:40960\n"
  "v_mfma_f32_16x16x32_bf16 v[160:163], a[48:51], a[36:39], v[160:163]\n"
  "ds_read_b64_tr_b16 a[98:99], v8 offset:41984\n"
  "buffer_load_dwordx4 v32, s[40:43], s64 offen offset:0 lds\n"
  "v_cvt_pk_bf16_f32 v114, v116, v117\n"
  "v_mfma_f32_16x16x32_bf16 v[192:195], a[48:51], a[40:43], v[192:195]\n"
  "ds_read_b128 a[80:83], v3 offset:0\n"
  "v_accvgpr_read_b32 v118, a182\n"
  "v_mfma_f32_16x16x32_bf16 v[224:227], a[48:51], a[44:47], v[224:227]\n"
  "ds_read_b64_tr_b16 a[100:101], v9 offset:40960\n"
  "v_mfma_f32_16x16x32_bf16 v[132:135], a[52:55], a[32:35], v[132:135]\n"
  "ds_read_b64_tr_b16 a[102:103], v9 offset:41984\n"
  "v_accvgpr_read_b32 v119, a183\n"
  "v_mfma_f32_16x16x32_bf16 v[164:167], a[52:55], a[36:39], v[164:167]\n"
  "buffer_load_dwordx4 v33, s[40:43], s64 offen offset:1024 lds\n"
  "v_cvt_pk_bf16_f32 v115, v118, v119\n"
  "v_mfma_f32_16x16x32_bf16 v[196:199], a[52:55], a[40:43], v[196:199]\n"
  "ds_read_b128 a[84:87], v3 offset:2048\n"
  "buffer_store_dwordx4 v[112:115], v53, s[88:91], s65 offen offset:2048 sc1\n"
  "v_mfma_f32_16x16x32_bf16 v[228:231], a[52:55], a[44:47], v[228:231]\n"
  "ds_read_b64_tr_b16 a[104:105], v10 offset:40960\n"
  "v_mfma_f32_16x16x32_bf16 v[136:139], a[56:59], a[32:35], v[136:139]\n"
  "ds_read_b64_tr_b16 a[106:107], v10 offset:41984\n"
  "v_accvgpr_read_b32 v120, a184\n"
  "v_mfma_f32_16x16x32_bf16 v[168:171], a[56:59], a[36:39], v[168:171]\n"
  "ds_read_b128 a[88:91], v3 offset:4096\n"
  "buffer_load_dwordx4 v34, s[40:43], s64 offen offset:2048 lds\n"
  "v_accvgpr_read_b32 v121, a185\n"
  "v_mfma_f32_16x16x32_bf16 v[200:203], a[56:59], a[40:43], v[200:203]\n"
  "ds_read_b64_tr_b16 a[108:109], v11 offset:40960\n"
  "v_mfma_f32_16x16x32_bf16 v[232:235], a[56:59], a[44:47], v[232:235]\n"
  "v_cvt_pk_bf16_f32 v120, v120, v121\n"
  "v_mfma_f32_16x16x32_bf16 v[140:143], a[60:63], a[32:35], v[140:143]\n"
  "ds_read_b64_tr_b16 a[110:111], v11 offset:41984\n"
  "v_accvgpr_read_b32 v122, a186\n"
  "v_mfma_f32_16x16x32_bf16 v[172:175], a[60:63], a[36:39], v[172:175]\n"
  "ds_read_b128 a[92:95], v3 offset:6144\n"
  "buffer_load_dwordx4 v35, s[40:43], s64 offen offset:3072 lds\n"
  "v_mfma_f32_16x16x32_bf16 v[204:207], a[60:63], a[40:43], v[204:207]\n"
  "ds_read_b64_tr_b16 a[112:113], v12 offset:40960\n"
  "v_accvgpr_read_b32 v123, a187\n"
  "v_mfma_f32_16x16x32_bf16 v[236:239], a[60:63], a[44:47], v[236:239]\n"
  "ds_read_b64_tr_b16 a[114:115], v12 offset:41984\n"
  "v_cvt_pk_bf16_f32 v121, v122, v123\n"
  "v_mfma_f32_16x16x32_bf16 v[144:147], a[64:67], a[32:35], v[144:147]\n"
  "ds_read_b64_tr_b16 a[116:117], v13 offset:40960\n"
  "v_accvgpr_read_b32 v124, a188\n"
  "v_mfma_f32_16x16x32_bf16 v[176:179], a[64:67], a[36:39], v[176:179]\n"
  "v_mfma_f32_16x16x32_bf16 v[208:211], a[64:67], a[40:43], v[208:211]\n"
  "ds_read_b64_tr_b16 a[118:119], v13 offset:41984\n"
  "v_accvgpr_read_b32 v125, a189\n"
  "v_mfma_f32_16x16x32_bf16 v[240:243], a[64:67], a[44:47], v[240:243]\n"
  "ds_read_b64_tr_b16 a[120:121], v14 offset:40960\n"
  "v_cvt_pk_bf16_f32 v122, v124, v125\n"
  "v_mfma_f32_16x16x32_bf16 v[148:151], a[68:71], a[32:35], v[148:151]\n"
  "ds_read_b64_tr_b16 a[122:123], v14 offset:41984\n"
  "v_mfma_f32_16x16x32_bf16 v[180:183], a[68:71], a[36:39], v[180:183]\n"
  "ds_read_b64_tr_b16 a[124:125], v15 offset:40960\n"
  "v_accvgpr_read_b32 v126, a190\n"
  "v_mfma_f32_16x16x32_bf16 v[212:215], a[68:71], a[40:43], v[212:215]\n"
  "ds_read_b64_tr_b16 a[126:127], v15 offset:41984\n"
  "v_accvgpr_read_b32 v127, a191\n"
  "v_mfma_f32_16x16x32_bf16 v[244:247], a[68:71], a[44:47], v[244:247]\n"
  "v_mfma_f32_16x16x32_bf16 v[152:155], a[72:75], a[32:35], v[152:155]\n"
  "v_add_u32 v2, 0xfffe8000, v2\n"
  "v_cvt_pk_bf16_f32 v123, v126, v127\n"
  "v_mfma_f32_16x16x32_bf16 v[184:187], a[72:75], a[36:39], v[184:187]\n"
  "v_add_u32 v3, 0xfffe8000, v3\n"
  "v_mfma_f32_16x16x32_bf16 v[216:219], a[72:75], a[40:43], v[216:219]\n"
  "v_add_u32 v8, 0xfffe8000, v8\n"
  "v_mfma_f32_16x16x32_bf16 v[248:251], a[72:75], a[44:47], v[248:251]\n"
  "v_add_u32 v9, 0xfffe8000, v9\n"
  "v_add_u32 v10, 0xfffe8000, v10\n"
  "v_mfma_f32_16x16x32_bf16 v[156:159], a[76:79], a[32:35], v[156:159]\n"
  "v_add_u32 v11, 0xfffe8000, v11\n"
  "v_mfma_f32_16x16x32_bf16 v[188:191], a[76:79], a[36:39], v[188:191]\n"
  "v_add_u32 v12, 0xfffe8000, v12\n"
  "v_mfma_f32_16x16x32_bf16 v[220:223], a[76:79], a[40:43], v[220:223]\n"
  "v_add_u32 v13, 0xfffe8000, v13\n"
  "v_mfma_f32_16x16x32_bf16 v[252:255], a[76:79], a[44:47], v[252:255]\n"
  "v_add_u32 v14, 0xfffe8000, v14\n"
  "v_add_u32 v15, 0xfffe8000, v15\n"
  "s_waitcnt vmcnt(16) lgkmcnt(0)\n"
  "s_barrier\n"
  "buffer_load_dword v62, off, s[88:91], s69 sc1\n"
  "v_mfma_f32_16x16x32_bf16 v[128:131], a[96:99], a[80:83], v[128:131]\n"
  "ds_read_b64_tr_b16 a[48:49], v8 offset:32768\n"
  "s_add_u32 m0, s60, 114688\n"
  "s_mov_b32 s64, 1152\n"
  "buffer_store_dwordx4 v[120:123], v53, s[88:91], s65 offen offset:3072 sc1\n"
  "s_add_u32 s65, s74, 24576\n"
  "v_accvgpr_read_b32 v64, a192\n"
  "v_accvgpr_read_b32 v65, a193\n"
  "v_mfma_f32_16x16x32_bf16 v[160:163], a[96:99], a[84:87], v[160:163]\n"
  "ds_read_b64_tr_b16 a[50:51], v8 offset:33792\n"
  "buffer_load_dwordx4 v28, s[40:43], s64 offen offset:0 lds\n"
  "v_cvt_pk_bf16_f32 v64, v64, v65\n"
  "v_accvgpr_read_b32 v66, a194\n"
  "v_mfma_f32_16x16x32_bf16 v[192:195], a[96:99], a[88:91], v[192:195]\n"
  "ds_read_b128 a[32:35], v2 offset:0\n"
  "v_accvgpr_read_b32 v67, a195\n"
  "v_mfma_f32_16x16x32_bf16 v[224:227], a[96:99], a[92:95], v[224:227]\n"
  "ds_read_b64_tr_b16 a[52:53], v9 offset:32768\n"
  "v_cvt_pk_bf16_f32 v65, v66, v67\n"
  "v_mfma_f32_16x16x32_bf16 v[132:135], a[100:103], a[80:83], v[132:135]\n"
  "ds_read_b64_tr_b16 a[54:55], v9 offset:33792\n"
  "v_accvgpr_read_b32 v68, a196\n"
  "v_mfma_f32_16x16x32_bf16 v[164:167], a[100:103], a[84:87], v[164:167]\n"
  "buffer_load_dwordx4 v29, s[40:43], s64 offen offset:1024 lds\n"
  "v_mfma_f32_16x16x32_bf16 v[196:199], a[100:103], a[88:91], v[196:199]\n"
  "ds_read_b128 a[36:39], v2 offset:2048\n"
  "v_accvgpr_read_b32 v69, a197\n"
  "v_mfma_f32_16x16x32_bf16 v[228:231], a[100:103], a[92:95], v[228:231]\n"
  "ds_read_b64_tr_b16 a[56:57], v10 offset:32768\n"
  "v_cvt_pk_bf16_f32 v66, v68, v69\n"
  "v_mfma_f32_16x16x32_bf16 v[136:139], a[104:107], a[80:83], v[136:139]\n"
  "ds_read_b64_tr_b16 a[58:59], v10 offset:33792\n"
  "v_mfma_f32_16x16x32_bf16 v[168:171], a[104:107], a[84:87], v[168:171]\n"
  "ds_read_b128 a[40:43], v2 offset:4096\n"
  "buffer_load_dwordx4 v30, s[40:43], s64 offen offset:2048 lds\n"
  "v_accvgpr_read_b32 v70, a198\n"
  "v_mfma_f32_16x16x32_bf16 v[200:203], a[104:107], a[88:91], v[200:203]\n"
  "ds_read_b64_tr_b16 a[60:61], v11 offset:32768\n"
  "v_accvgpr_read_b32 v71, a199\n"
  "v_mfma_f32_16x16x32_bf16 v[232:235], a[104:107], a[92:95], v[232:235]\n"
  "v_mfma_f32_16x16x32_bf16 v[140:143], a[108:111], a[80:83], v[140:143]\n"
  "ds_read_b64_tr_b16 a[62:63], v11 offset:33792\n"
  "v_cvt_pk_bf16_f32 v67, v70, v71\n"
  "v_mfma_f32_16x16x32_bf16 v[172:175], a[108:111], a[84:87], v[172:175]\n"
  "ds_read_b128 a[44:47], v2 offset:6144\n"
  "buffer_load_dwordx4 v31, s[40:43], s64 offen offset:3072 lds\n"
  "v_mfma_f32_16x16x32_bf16 v[204:207], a[108:111], a[88:91], v[204:207]\n"
  "ds_read_b64_tr_b16 a[64:65], v12 offset:32768\n"
  "s_add_u32 m0, s62, 147456\n"
  "s_mov_b32 s64, 4718592\n"
  "buffer_store_dwordx4 v[64:67], v52, s[88:91], s65 offen offset:0 sc1\n"
  "v_accvgpr_read_b32 v72, a200\n"
  "v_mfma_f32_16x16x32_bf16 v[236:239], a[108:111], a[92:95], v[236:239]\n"
  "ds_read_b64_tr_b16 a[66:67], v12 offset:33792\n"
  "v_mfma_f32_16x16x32_bf16 v[144:147], a[112:115], a[80:83], v[144:147]\n"
  "ds_read_b64_tr_b16 a[68:69], v13 offset:32768\n"
  "v_accvgpr_read_b32 v73, a201\n"
  "v_mfma_f32_16x16x32_bf16 v[176:179], a[112:115], a[84:87], v[176:179]\n"
  "buffer_load_dwordx4 v24, s[44:47], s64 offen offset:0 lds\n"
  "v_cvt_pk_bf16_f32 v72, v72, v73\n"
  "v_mfma_f32_16x16x32_bf16 v[208:211], a[112:115], a[88:91], v[208:211]\n"
  "ds_read_b64_tr_b16 a[70:71], v13 offset:33792\n"
  "v_mfma_f32_16x16x32_bf16 v[240:243], a[112:115], a[92:95], v[240:243]\n"
  "ds_read_b64_tr_b16 a[72:73], v14 offset:32768\n"
  "v_accvgpr_read_b32 v74, a202\n"
  "v_mfma_f32_16x16x32_bf16 v[148:151], a[116:119], a[80:83], v[148:151]\n"
  "ds_read_b64_tr_b16 a[74:75], v14 offset:33792\n"
  "v_accvgpr_read_b32 v75, a203\n"
  "v_mfma_f32_16x16x32_bf16 v[180:183], a[116:119], a[84:87], v[180:183]\n"
  "ds_read_b64_tr_b16 a[76:77], v15 offset:32768\n"
  "buffer_load_dwordx4 v25, s[44:47], s64 offen offset:1024 lds\n"
  "v_mfma_f32_16x16x32_bf16 v[212:215], a[116:119], a[88:91], v[212:215]\n"
  "ds_read_b64_tr_b16 a[78:79], v15 offset:33792\n"
  "v_cvt_pk_bf16_f32 v73, v74, v75\n"
  "v_mfma_f32_16x16x32_bf16 v[244:247], a[116:119], a[92:95], v[244:247]\n"
  "v_accvgpr_read_b32 v76, a204\n"
  "v_mfma_f32_16x16x32_bf16 v[152:155], a[120:123], a[80:83], v[152:155]\n"
  "v_accvgpr_read_b32 v77, a205\n"
  "v_mfma_f32_16x16x32_bf16 v[184:187], a[120:123], a[84:87], v[184:187]\n"
  "buffer_load_dwordx4 v26, s[44:47], s64 offen offset:2048 lds\n"
  "v_mfma_f32_16x16x32_bf16 v[216:219], a[120:123], a[88:91], v[216:219]\n"
  "v_cvt_pk_bf16_f32 v74, v76, v77\n"
  "v_mfma_f32_16x16x32_bf16 v[248:251], a[120:123], a[92:95], v[248:251]\n"
  "v_accvgpr_read_b32 v78, a206\n"
  "v_mfma_f32_16x16x32_bf16 v[156:159], a[124:127], a[80:83], v[156:159]\n"
  "v_mfma_f32_16x16x32_bf16 v[188:191], a[124:127], a[84:87], v[188:191]\n"
  "buffer_load_dwordx4 v27, s[44:47], s64 offen offset:3072 lds\n"
  "v_accvgpr_read_b32 v79, a207\n"
  "v_mfma_f32_16x16x32_bf16 v[220:223], a[124:127], a[88:91], v[220:223]\n"
  "s_add_u32 m0, s60, 118784\n"
  "s_mov_b32 s64, 1152\n"
  "v_cvt_pk_bf16_f32 v75, v78, v79\n"
  "v_mfma_f32_16x16x32_bf16 v[252:255], a[124:127], a[92:95], v[252:255]\n"
  "s_waitcnt lgkmcnt(0)\n"
  "v_mfma_f32_16x16x32_bf16 v[128:131], a[48:51], a[32:35], v[128:131]\n"
  "ds_read_b64_tr_b16 a[96:97], v8 offset:40960\n"
  "buffer_store_dwordx4 v[72:75], v52, s[88:91], s65 offen offset:1024 sc1\n"
  "v_mfma_f32_16x16x32_bf16 v[160:163], a[48:51], a[36:39], v[160:163]\n"
  "ds_read_b64_tr_b16 a[98:99], v8 offset:41984\n"
  "buffer_load_dwordx4 v32, s[40:43], s64 offen offset:0 lds\n"
  "v_accvgpr_read_b32 v80, a208\n"
  "v_mfma_f32_16x16x32_bf16 v[192:195], a[48:51], a[40:43], v[192:195]\n"
  "ds_read_b128 a[80:83], v3 offset:0\n"
  "v_accvgpr_read_b32 v81, a209\n"
  "v_mfma_f32_16x16x32_bf16 v[224:227], a[48:51], a[44:47], v[224:227]\n"
  "ds_read_b64_tr_b16 a[100:101], v9 offset:40960\n"
  "v_mfma_f32_16x16x32_bf16 v[132:135], a[52:55], a[32:35], v[132:135]\n"
  "ds_read_b64_tr_b16 a[102:103], v9 offset:41984\n"
  "v_cvt_pk_bf16_f32 v80, v80, v81\n"
  "v_mfma_f32_16x16x32_bf16 v[164:167], a[52:55], a[36:39], v[164:167]\n"
  "buffer_load_dwordx4 v33, s[40:43], s64 offen offset:1024 lds\n"
  "v_accvgpr_read_b32 v82, a210\n"
  "v_mfma_f32_16x16x32_bf16 v[196:199], a[52:55], a[40:43], v[196:199]\n"
  "ds_read_b128 a[84:87], v3 offset:2048\n"
  "v_mfma_f32_16x16x32_bf16 v[228:231], a[52:55], a[44:47], v[228:231]\n"
  "ds_read_b64_tr_b16 a[104:105], v10 offset:40960\n"
  "v_accvgpr_read_b32 v83, a211\n"
  "v_mfma_f32_16x16x32_bf16 v[136:139], a[56:59], a[32:35], v[136:139]\n"
  "ds_read_b64_tr_b16 a[106:107], v10 offset:41984\n"
  "v_cvt_pk_bf16_f32 v81, v82, v83\n"
  "v_mfma_f32_16x16x32_bf16 v[168:171], a[56:59], a[36:39], v[168:171]\n"
  "ds_read_b128 a[88:91], v3 offset:4096\n"
  "buffer_load_dwordx4 v34, s[40:43], s64 offen offset:2048 lds\n"
  "v_mfma_f32_16x16x32_bf16 v[200:203], a[56:59], a[40:43], v[200:203]\n"
  "ds_read_b64_tr_b16 a[108:109], v11 offset:40960\n"
  "v_accvgpr_read_b32 v84, a212\n"
  "v_mfma_f32_16x16x32_bf16 v[232:235], a[56:59], a[44:47], v[232:235]\n"
  "v_accvgpr_read_b32 v85, a213\n"
  "v_mfma_f32_16x16x32_bf16 v[140:143], a[60:63], a[32:35], v[140:143]\n"
  "ds_read_b64_tr_b16 a[110:111], v11 offset:41984\n"
  "v_cvt_pk_bf16_f32 v82, v84, v85\n"
  "v_mfma_f32_16x16x32_bf16 v[172:175], a[60:63], a[36:39], v[172:175]\n"
  "ds_read_b128 a[92:95], v3 offset:6144\n"
  "buffer_load_dwordx4 v35, s[40:43], s64 offen offset:3072 lds\n"
  "v_mfma_f32_16x16x32_bf16 v[204:207], a[60:63], a[40:43], v[204:207]\n"
  "ds_read_b64_tr_b16 a[112:113], v12 offset:40960\n"
  "v_accvgpr_read_b32 v86, a214\n"
  "v_mfma_f32_16x16x32_bf16 v[236:239], a[60:63], a[44:47], v[236:239]\n"
  "ds_read_b64_tr_b16 a[114:115], v12 offset:41984\n"
  "v_accvgpr_read_b32 v87, a215\n"
  "v_mfma_f32_16x16x32_bf16 v[144:147], a[64:67], a[32:35], v[144:147]\n"
  "ds_read_b64_tr_b16 a[116:117], v13 offset:40960\n"
  "v_mfma_f32_16x16x32_bf16 v[176:179], a[64:67], a[36:39], v[176:179]\n"
  "v_cvt_pk_bf16_f32 v83, v86, v87\n"
  "v_mfma_f32_16x16x32_bf16 v[208:211], a[64:67], a[40:43], v[208:211]\n"
  "ds_read_b64_tr_b16 a[118:119], v13 offset:41984\n"
  "buffer_store_dwordx4 v[80:83], v52, s[88:91], s65 offen offset:2048 sc1\n"
  "v_mfma_f32_16x16x32_bf16 v[240:243], a[64:67], a[44:47], v[240:243]\n"
  "ds_read_b64_tr_b16 a[120:121], v14 offset:40960\n"
  "v_mfma_f32_16x16x32_bf16 v[148:151], a[68:71], a[32:35], v[148:151]\n"
  "ds_read_b64_tr_b16 a[122:123], v14 offset:41984\n"
  "v_accvgpr_read_b32 v88, a216\n"
  "v_mfma_f32_16x16x32_bf16 v[180:183], a[68:71], a[36:39], v[180:183]\n"
  "ds_read_b64_tr_b16 a[124:125], v15 offset:40960\n"
  "v_accvgpr_read_b32 v89, a217\n"
  "v_mfma_f32_16x16x32_bf16 v[212:215], a[68:71], a[40:43], v[212:215]\n"
  "ds_read_b64_tr_b16 a[126:127], v15 offset:41984\n"
  "v_cvt_pk_bf16_f32 v88, v88, v89\n"
  "v_mfma_f32_16x16x32_bf16 v[244:247], a[68:71], a[44:47], v[244:247]\n"
  "v_mfma_f32_16x16x32_bf16 v[152:155], a[72:75], a[32:35], v[152:155]\n"
  "v_add_u32 v2, 0xc000, v2\n"
  "v_accvgpr_read_b32 v90, a218\n"
  "v_mfma_f32_16x16x32_bf16 v[184:187], a[72:75], a[36:39], v[184:187]\n"
  "v_add_u32 v3, 0xc000, v3\n"
  "v_accvgpr_read_b32 v91, a219\n"
  "v_mfma_f32_16x16x32_bf16 v[216:219], a[72:75], a[40:43], v[216:219]\n"
  "v_add_u32 v8, 0xc000, v8\n"
  "v_mfma_f32_16x16x32_bf16 v[248:251], a[72:75], a[44:47], v[248:251]\n"
  "v_add_u32 v9, 0xc000, v9\n"
  "v_add_u32 v10, 0xc000, v10\n"
  "v_mfma_f32_16x16x32_bf16 v[156:159], a[76:79], a[32:35], v[156:159]\n"
  "v_add_u32 v11, 0xc000, v11\n"
  "v_cvt_pk_bf16_f32 v89, v90, v91\n"
  "v_mfma_f32_16x16x32_bf16 v[188:191], a[76:79], a[36:39], v[188:191]\n"
  "v_add_u32 v12, 0xc000, v12\n"
  "v_accvgpr_read_b32 v92, a220\n"
  "v_mfma_f32_16x16x32_bf16 v[220:223], a[76:79], a[40:43], v[220:223]\n"
  "v_add_u32 v13, 0xc000, v13\n"
  "v_accvgpr_read_b32 v93, a221\n"
  "v_mfma_f32_16x16x32_bf16 v[252:255], a[76:79], a[44:47], v[252:255]\n"
  "v_add_u32 v14, 0xc000, v14\n"
  "v_add_u32 v15, 0xc000, v15\n"
  "s_waitcnt vmcnt(17) lgkmcnt(0)\n"
  "s_barrier\n"
  "v_mfma_f32_16x16x32_bf16 v[128:131], a[96:99], a[80:83], v[128:131]\n"
  "ds_read_b64_tr_b16 a[48:49], v8 offset:32768\n"
  "s_add_u32 m0, s62, 0\n"
  "s_mov_b32 s64, 0\n"
  "v_cvt_pk_bf16_f32 v90, v92, v93\n"
  "v_accvgpr_read_b32 v94, a222\n"
  "v_mfma_f32_16x16x32_bf16 v[160:163], a[96:99], a[84:87], v[160:163]\n"
  "ds_read_b64_tr_b16 a[50:51], v8 offset:33792\n"
  "buffer_load_dwordx4 v42, s[48:51], s64 offen offset:0 lds\n"
  "v_mfma_f32_16x16x32_bf16 v[192:195], a[96:99], a[88:91], v[192:195]\n"
  "ds_read_b128 a[32:35], v2 offset:0\n"
  "v_accvgpr_read_b32 v95, a223\n"
  "v_mfma_f32_16x16x32_bf16 v[224:227], a[96:99], a[92:95], v[224:227]\n"
  "ds_read_b64_tr_b16 a[52:53], v9 offset:32768\n"
  "v_cvt_pk_bf16_f32 v91, v94, v95\n"
  "v_mfma_f32_16x16x32_bf16 v[132:135], a[100:103], a[80:83], v[132:135]\n"
  "ds_read_b64_tr_b16 a[54:55], v9 offset:33792\n"
  "v_mfma_f32_16x16x32_bf16 v[164:167], a[100:103], a[84:87], v[164:167]\n"
  "buffer_load_dwordx4 v43, s[48:51], s64 offen offset:1024 lds\n"
  "v_mfma_f32_16x16x32_bf16 v[196:199], a[100:103], a[88:91], v[196:199]\n"
  "ds_read_b128 a[36:39], v2 offset:2048\n"
  "buffer_store_dwordx4 v[88:91], v52, s[88:91], s65 offen offset:3072 sc1\n"
  "v_accvgpr_read_b32 v96, a224\n"
  "v_mfma_f32_16x16x32_bf16 v[228:231], a[100:103], a[92:95], v[228:231]\n"
  "ds_read_b64_tr_b16 a[56:57], v10 offset:32768\n"
  "v_mfma_f32_16x16x32_bf16 v[136:139], a[104:107], a[80:83], v[136:139]\n"
  "ds_read_b64_tr_b16 a[58:59], v10 offset:33792\n"
  "v_accvgpr_read_b32 v97, a225\n"
  "v_mfma_f32_16x16x32_bf16 v[168:171], a[104:107], a[84:87], v[168:171]\n"
  "ds_read_b128 a[40:43], v2 offset:4096\n"
  "buffer_load_dwordx4 v44, s[48:51], s64 offen offset:2048 lds\n"
  "v_cvt_pk_bf16_f32 v96, v96, v97\n"
  "v_mfma_f32_16x16x32_bf16 v[200:203], a[104:107], a[88:91], v[200:203]\n"
  "ds_read_b64_tr_b16 a[60:61], v11 offset:32768\n"
  "v_accvgpr_read_b32 v98, a226\n"
  "v_mfma_f32_16x16x32_bf16 v[232:235], a[104:107], a[92:95], v[232:235]\n"
  "v_mfma_f32_16x16x32_bf16 v[140:143], a[108:111], a[80:83], v[140:143]\n"
  "ds_read_b64_tr_b16 a[62:63], v11 offset:33792\n"
  "v_accvgpr_read_b32 v99, a227\n"
  "v_mfma_f32_16x16x32_bf16 v[172:175], a[108:111], a[84:87], v[172:175]\n"
  "ds_read_b128 a[44:47], v2 offset:6144\n"
  "buffer_load_dwordx4 v45, s[48:51], s64 offen offset:3072 lds\n"
  "v_cvt_pk_bf16_f32 v97, v98, v99\n"
  "v_mfma_f32_16x16x32_bf16 v[204:207], a[108:111], a[88:91], v[204:207]\n"
  "ds_read_b64_tr_b16 a[64:65], v12 offset:32768\n"
  "s_add_u32 m0, s62, 16384\n"
  "s_mov_b32 s64, 0\n"
  "v_mfma_f32_16x16x32_bf16 v[236:239], a[108:111], a[92:95], v[236:239]\n"
  "ds_read_b64_tr_b16 a[66:67], v12 offset:33792\n"
  "v_accvgpr_read_b32 v100, a228\n"
  "v_mfma_f32_16x16x32_bf16 v[144:147], a[112:115], a[80:83], v[144:147]\n"
  "ds_read_b64_tr_b16 a[68:69], v13 offset:32768\n"
  "v_accvgpr_read_b32 v101, a229\n"
  "v_mfma_f32_16x16x32_bf16 v[176:179], a[112:115], a[84:87], v[176:179]\n"
  "buffer_load_dwordx4 v48, s[84:87], s64 offen offset:0 lds\n"
  "v_mfma_f32_16x16x32_bf16 v[208:211], a[112:115], a[88:91], v[208:211]\n"
  "ds_read_b64_tr_b16 a[70:71], v13 offset:33792\n"
  "v_cvt_pk_bf16_f32 v98, v100, v101\n"
  "v_mfma_f32_16x16x32_bf16 v[240:243], a[112:115], a[92:95], v[240:243]\n"
  "ds_read_b64_tr_b16 a[72:73], v14 offset:32768\n"
  "v_accvgpr_read_b32 v102, a230\n"
  "v_mfma_f32_16x16x32_bf16 v[148:151], a[116:119], a[80:83], v[148:151]\n"
  "ds_read_b64_tr_b16 a[74:75], v14 offset:33792\n"
  "v_accvgpr_read_b32 v103, a231\n"
  "v_mfma_f32_16x16x32_bf16 v[180:183], a[116:119], a[84:87], v[180:183]\n"
  "ds_read_b64_tr_b16 a[76:77], v15 offset:32768\n"
  "buffer_load_dwordx4 v49, s[84:87], s64 offen offset:1024 lds\n"
  "v_mfma_f32_16x16x32_bf16 v[212:215], a[116:119], a[88:91], v[212:215]\n"
  "ds_read_b64_tr_b16 a[78:79], v15 offset:33792\n"
  "v_cvt_pk_bf16_f32 v99, v102, v103\n"
  "v_mfma_f32_16x16x32_bf16 v[244:247], a[116:119], a[92:95], v[244:247]\n"
  "buffer_store_dwordx4 v[96:99], v53, s[88:91], s65 offen offset:0 sc1\n"
  "v_mfma_f32_16x16x32_bf16 v[152:155], a[120:123], a[80:83], v[152:155]\n"
  "v_mfma_f32_16x16x32_bf16 v[184:187], a[120:123], a[84:87], v[184:187]\n"
  "buffer_load_dwordx4 v50, s[84:87], s64 offen offset:2048 lds\n"
  "v_accvgpr_read_b32 v104, a232\n"
  "v_mfma_f32_16x16x32_bf16 v[216:219], a[120:123], a[88:91], v[216:219]\n"
  "v_accvgpr_read_b32 v105, a233\n"
  "v_mfma_f32_16x16x32_bf16 v[248:251], a[120:123], a[92:95], v[248:251]\n"
  "v_mfma_f32_16x16x32_bf16 v[156:159], a[124:127], a[80:83], v[156:159]\n"
  "v_cvt_pk_bf16_f32 v104, v104, v105\n"
  "v_mfma_f32_16x16x32_bf16 v[188:191], a[124:127], a[84:87], v[188:191]\n"
  "buffer_load_dwordx4 v51, s[84:87], s64 offen offset:3072 lds\n"
  "v_accvgpr_read_b32 v106, a234\n"
  "v_mfma_f32_16x16x32_bf16 v[220:223], a[124:127], a[88:91], v[220:223]\n"
  "s_add_u32 m0, s62, 32768\n"
  "s_mov_b32 s64, 128\n"
  "v_accvgpr_read_b32 v107, a235\n"
  "v_mfma_f32_16x16x32_bf16 v[252:255], a[124:127], a[92:95], v[252:255]\n"
  "s_waitcnt lgkmcnt(0)\n"
  "v_mfma_f32_16x16x32_bf16 v[128:131], a[48:51], a[32:35], v[128:131]\n"
  "ds_read_b64_tr_b16 a[96:97], v8 offset:40960\n"
  "v_cvt_pk_bf16_f32 v105, v106, v107\n"
  "v_mfma_f32_16x16x32_bf16 v[160:163], a[48:51], a[36:39], v[160:163]\n"
  "ds_read_b64_tr_b16 a[98:99], v8 offset:41984\n"
  "buffer_load_dwordx4 v42, s[48:51], s64 offen offset:0 lds\n"
  "v_accvgpr_read_b32 v108, a236\n"
  "v_mfma_f32_16x16x32_bf16 v[192:195], a[48:51], a[40:43], v[192:195]\n"
  "ds_read_b128 a[80:83], v3 offset:0\n"
  "v_mfma_f32_16x16x32_bf16 v[224:227], a[48:51], a[44:47], v[224:227]\n"
  "ds_read_b64_tr_b16 a[100:101], v9 offset:40960\n"
  "v_accvgpr_read_b32 v109, a237\n"
  "v_mfma_f32_16x16x32_bf16 v[132:135], a[52:55], a[32:35], v[132:135]\n"
  "ds_read_b64_tr_b16 a[102:103], v9 offset:41984\n"
  "v_cvt_pk_bf16_f32 v106, v108, v109\n"
  "v_mfma_f32_16x16x32_bf16 v[164:167], a[52:55], a[36:39], v[164:167]\n"
  "buffer_load_dwordx4 v43, s[48:51], s64 offen offset:1024 lds\n"
  "v_mfma_f32_16x16x32_bf16 v[196:199], a[52:55], a[40:43], v[196:199]\n"
  "ds_read_b128 a[84:87], v3 offset:2048\n"
  "v_accvgpr_read_b32 v110, a238\n"
  "v_mfma_f32_16x16x32_bf16 v[228:231], a[52:55], a[44:47], v[228:231]\n"
  "ds_read_b64_tr_b16 a[104:105], v10 offset:40960\n"
  "v_accvgpr_read_b32 v111, a239\n"
  "v_mfma_f32_16x16x32_bf16 v[136:139], a[56:59], a[32:35], v[136:139]\n"
  "ds_read_b64_tr_b16 a[106:107], v10 offset:41984\n"
  "v_cvt_pk_bf16_f32 v107, v110, v111\n"
  "v_mfma_f32_16x16x32_bf16 v[168:171], a[56:59], a[36:39], v[168:171]\n"
  "ds_read_b128 a[88:91], v3 offset:4096\n"
  "buffer_load_dwordx4 v44, s[48:51], s64 offen offset:2048 lds\n"
  "v_mfma_f32_16x16x32_bf16 v[200:203], a[56:59], a[40:43], v[200:203]\n"
  "ds_read_b64_tr_b16 a[108:109], v11 offset:40960\n"
  "buffer_store_dwordx4 v[104:107], v53, s[88:91], s65 offen offset:1024 sc1\n"
  "v_mfma_f32_16x16x32_bf16 v[232:235], a[56:59], a[44:47], v[232:235]\n"
  "v_accvgpr_read_b32 v112, a240\n"
  "v_mfma_f32_16x16x32_bf16 v[140:143], a[60:63], a[32:35], v[140:143]\n"
  "ds_read_b64_tr_b16 a[110:111], v11 offset:41984\n"
  "v_mfma_f32_16x16x32_bf16 v[172:175], a[60:63], a[36:39], v[172:175]\n"
  "ds_read_b128 a[92:95], v3 offset:6144\n"
  "buffer_load_dwordx4 v45, s[48:51], s64 offen offset:3072 lds\n"
  "v_accvgpr_read_b32 v113, a241\n"
  "v_mfma_f32_16x16x32_bf16 v[204:207], a[60:63], a[40:43], v[204:207]\n"
  "ds_read_b64_tr_b16 a[112:113], v12 offset:40960\n"
  "s_add_u32 m0, s62, 49152\n"
  "s_mov_b32 s64, 245760\n"
  "v_cvt_pk_bf16_f32 v112, v112, v113\n"
  "v_mfma_f32_16x16x32_bf16 v[236:239], a[60:63], a[44:47], v[236:239]\n"
  "ds_read_b64_tr_b16 a[114:115], v12 offset:41984\n"
  "v_mfma_f32_16x16x32_bf16 v[144:147], a[64:67], a[32:35], v[144:147]\n"
  "ds_read_b64_tr_b16 a[116:117], v13 offset:40960\n"
  "v_accvgpr_read_b32 v114, a242\n"
  "v_mfma_f32_16x16x32_bf16 v[176:179], a[64:67], a[36:39], v[176:179]\n"
  "buffer_load_dwordx4 v48, s[84:87], s64 offen offset:0 lds\n"
  "v_accvgpr_read_b32 v115, a243\n"
  "v_mfma_f32_16x16x32_bf16 v[208:211], a[64:67], a[40:43], v[208:211]\n"
  "ds_read_b64_tr_b16 a[118:119], v13 offset:41984\n"
  "v_cvt_pk_bf16_f32 v113, v114, v115\n"
  "v_mfma_f32_16x16x32_bf16 v[240:243], a[64:67], a[44:47], v[240:243]\n"
  "ds_read_b64_tr_b16 a[120:121], v14 offset:40960\n"
  "v_mfma_f32_16x16x32_bf16 v[148:151], a[68:71], a[32:35], v[148:151]\n"
  "ds_read_b64_tr_b16 a[122:123], v14 offset:41984\n"
  "v_accvgpr_read_b32 v116, a244\n"
  "v_mfma_f32_16x16x32_bf16 v[180:183], a[68:71], a[36:39], v[180:183]\n"
  "ds_read_b64_tr_b16 a[124:125], v15 offset:40960\n"
  "buffer_load_dwordx4 v49, s[84:87], s64 offen offset:1024 lds\n"
  "v_accvgpr_read_b32 v117, a245\n"
  "v_mfma_f32_16x16x32_bf16 v[212:215], a[68:71], a[40:43], v[212:215]\n"
  "ds_read_b64_tr_b16 a[126:127], v15 offset:41984\n"
  "v_mfma_f32_16x16x32_bf16 v[244:247], a[68:71], a[44:47], v[244:247]\n"
  "v_cvt_pk_bf16_f32 v114, v116, v117\n"
  "v_mfma_f32_16x16x32_bf16 v[152:155], a[72:75], a[32:35], v[152:155]\n"
  "v_add_u32 v2, 0xc000, v2\n"
  "v_accvgpr_read_b32 v118, a246\n"
  "v_mfma_f32_16x16x32_bf16 v[184:187], a[72:75], a[36:39], v[184:187]\n"
  "buffer_load_dwordx4 v50, s[84:87], s64 offen offset:2048 lds\n"
  "v_add_u32 v3, 0xc000, v3\n"
  "v_mfma_f32_16x16x32_bf16 v[216:219], a[72:75], a[40:43], v[216:219]\n"
  "v_add_u32 v8, 0xc000, v8\n"
  "v_accvgpr_read_b32 v119, a247\n"
  "v_mfma_f32_16x16x32_bf16 v[248:251], a[72:75], a[44:47], v[248:251]\n"
  "v_add_u32 v9, 0xc000, v9\n"
  "v_add_u32 v10, 0xc000, v10\n"
  "v_mfma_f32_16x16x32_bf16 v[156:159], a[76:79], a[32:35], v[156:159]\n"
  "v_add_u32 v11, 0xc000, v11\n"
  "v_cvt_pk_bf16_f32 v115, v118, v119\n"
  "v_mfma_f32_16x16x32_bf16 v[188:191], a[76:79], a[36:39], v[188:191]\n"
  "buffer_load_dwordx4 v51, s[84:87], s64 offen offset:3072 lds\n"
  "v_add_u32 v12, 0xc000, v12\n"
  "v_mfma_f32_16x16x32_bf16 v[220:223], a[76:79], a[40:43], v[220:223]\n"
  "v_add_u32 v13, 0xc000, v13\n"
  "v_mfma_f32_16x16x32_bf16 v[252:255], a[76:79], a[44:47], v[252:255]\n"
  "v_add_u32 v14, 0xc000, v14\n"
  "v_add_u32 v15, 0xc000, v15\n"
  "s_waitcnt vmcnt(20) lgkmcnt(0)\n"
  "s_barrier\n"
  "v_mfma_f32_16x16x32_bf16 v[128:131], a[96:99], a[80:83], v[128:131]\n"
  "ds_read_b64_tr_b16 a[48:49], v8 offset:32768\n"
  "s_add_u32 m0, s62, 65536\n"
  "s_mov_b32 s64, 256\n"
  "buffer_store_dwordx4 v[112:115], v53, s[88:91], s65 offen offset:2048 sc1\n"
  "v_accvgpr_read_b32 v120, a248\n"
  "v_accvgpr_read_b32 v121, a249\n"
  "v_mfma_f32_16x16x32_bf16 v[160:163], a[96:99], a[84:87], v[160:163]\n"
  "ds_read_b64_tr_b16 a[50:51], v8 offset:33792\n"
  "buffer_load_dwordx4 v42, s[48:51], s64 offen offset:0 lds\n"
  "v_cvt_pk_bf16_f32 v120, v120, v121\n"
  "v_mfma_f32_16x16x32_bf16 v[192:195], a[96:99], a[88:91], v[192:195]\n"
  "ds_read_b128 a[32:35], v2 offset:0\n"
  "v_accvgpr_read_b32 v122, a250\n"
  "v_mfma_f32_16x16x32_bf16 v[224:227], a[96:99], a[92:95], v[224:227]\n"
  "ds_read_b64_tr_b16 a[52:53], v9 offset:32768\n"
  "v_mfma_f32_16x16x32_bf16 v[132:135], a[100:103], a[80:83], v[132:135]\n"
  "ds_read_b64_tr_b16 a[54:55], v9 offset:33792\n"
  "v_accvgpr_read_b32 v123, a251\n"
  "v_mfma_f32_16x16x32_bf16 v[164:167], a[100:103], a[84:87], v[164:167]\n"
  "buffer_load_dwordx4 v43, s[48:51], s64 offen offset:1024 lds\n"
  "v_cvt_pk_bf16_f32 v121, v122, v123\n"
  "v_mfma_f32_16x16x32_bf16 v[196:199], a[100:103], a[88:91], v[196:199]\n"
  "ds_read_b128 a[36:39], v2 offset:2048\n"
  "v_accvgpr_read_b32 v124, a252\n"
  "v_mfma_f32_16x16x32_bf16 v[228:231], a[100:103], a[92:95], v[228:231]\n"
  "ds_read_b64_tr_b16 a[56:57], v10 offset:32768\n"
  "v_mfma_f32_16x16x32_bf16 v[136:139], a[104:107], a[80:83], v[136:139]\n"
  "ds_read_b64_tr_b16 a[58:59], v10 offset:33792\n"
  "v_accvgpr_read_b32 v125, a253\n"
  "v_mfma_f32_16x16x32_bf16 v[168:171], a[104:107], a[84:87], v[168:171]\n"
  "ds_read_b128 a[40:43], v2 offset:4096\n"
  "buffer_load_dwordx4 v44, s[48:51], s64 offen offset:2048 lds\n"
  "v_cvt_pk_bf16_f32 v122, v124, v125\n"
  "v_mfma_f32_16x16x32_bf16 v[200:203], a[104:107], a[88:91], v[200:203]\n"
  "ds_read_b64_tr_b16 a[60:61], v11 offset:32768\n"
  "v_mfma_f32_16x16x32_bf16 v[232:235], a[104:107], a[92:95], v[232:235]\n"
  "v_accvgpr_read_b32 v126, a254\n"
  "v_mfma_f32_16x16x32_bf16 v[140:143], a[108:111], a[80:83], v[140:143]\n"
  "ds_read_b64_tr_b16 a[62:63], v11 offset:33792\n"
  "v_accvgpr_read_b32 v127, a255\n"
  "v_mfma_f32_16x16x32_bf16 v[172:175], a[108:111], a[84:87], v[172:175]\n"
  "ds_read_b128 a[44:47], v2 offset:6144\n"
  "buffer_load_dwordx4 v45, s[48:51], s64 offen offset:3072 lds\n"
  "v_mfma_f32_16x16x32_bf16 v[204:207], a[108:111], a[88:91], v[204:207]\n"
  "ds_read_b64_tr_b16 a[64:65], v12 offset:32768\n"
  "s_add_u32 m0, s62, 81920\n"
  "s_mov_b32 s64, 491520\n"
  "v_cvt_pk_bf16_f32 v123, v126, v127\n"
  "v_mfma_f32_16x16x32_bf16 v[236:239], a[108:111], a[92:95], v[236:239]\n"
  "ds_read_b64_tr_b16 a[66:67], v12 offset:33792\n"
  "buffer_store_dwordx4 v[120:123], v53, s[88:91], s65 offen offset:3072 sc1\n"
  "v_mfma_f32_16x16x32_bf16 v[144:147], a[112:115], a[80:83], v[144:147]\n"
  "ds_read_b64_tr_b16 a[68:69], v13 offset:32768\n"
  "v_mfma_f32_16x16x32_bf16 v[176:179], a[112:115], a[84:87], v[176:179]\n"
  "buffer_load_dwordx4 v48, s[84:87], s64 offen offset:0 lds\n"
  "v_mfma_f32_16x16x32_bf16 v[208:211], a[112:115], a[88:91], v[208:211]\n"
  "ds_read_b64_tr_b16 a[70:71], v13 offset:33792\n"
  "v_mfma_f32_16x16x32_bf16 v[240:243], a[112:115], a[92:95], v[240:243]\n"
  "ds_read_b64_tr_b16 a[72:73], v14 offset:32768\n"
  "v_mfma_f32_16x16x32_bf16 v[148:151], a[116:119], a[80:83], v[148:151]\n"
  "ds_read_b64_tr_b16 a[74:75], v14 offset:33792\n"
  "v_mfma_f32_16x16x32_bf16 v[180:183], a[116:119], a[84:87], v[180:183]\n"
  "ds_read_b64_tr_b16 a[76:77], v15 offset:32768\n"
  "buffer_load_dwordx4 v49, s[84:87], s64 offen offset:1024 lds\n"
  "v_mfma_f32_16x16x32_bf16 v[212:215], a[116:119], a[88:91], v[212:215]\n"
  "ds_read_b64_tr_b16 a[78:79], v15 offset:33792\n"
  "v_mfma_f32_16x16x32_bf16 v[244:247], a[116:119], a[92:95], v[244:247]\n"
  "v_mfma_f32_16x16x32_bf16 v[152:155], a[120:123], a[80:83], v[152:155]\n"
  "v_mfma_f32_16x16x32_bf16 v[184:187], a[120:123], a[84:87], v[184:187]\n"
  "buffer_load_dwordx4 v50, s[84:87], s64 offen offset:2048 lds\n"
  "v_mfma_f32_16x16x32_bf16 v[216:219], a[120:123], a[88:91], v[216:219]\n"
  "v_mfma_f32_16x16x32_bf16 v[248:251], a[120:123], a[92:95], v[248:251]\n"
  "v_mfma_f32_16x16x32_bf16 v[156:159], a[124:127], a[80:83], v[156:159]\n"
  "v_mfma_f32_16x16x32_bf16 v[188:191], a[124:127], a[84:87], v[188:191]\n"
  "buffer_load_dwordx4 v51, s[84:87], s64 offen offset:3072 lds\n"
  "v_mfma_f32_16x16x32_bf16 v[220:223], a[124:127], a[88:91], v[220:223]\n"
  "v_mfma_f32_16x16x32_bf16 v[252:255], a[124:127], a[92:95], v[252:255]\n"
  "s_waitcnt lgkmcnt(0)\n"
  "v_mfma_f32_16x16x32_bf16 v[128:131], a[48:51], a[32:35], v[128:131]\n"
  "ds_read_b64_tr_b16 a[96:97], v8 offset:40960\n"
  "v_mfma_f32_16x16x32_bf16 v[160:163], a[48:51], a[36:39], v[160:163]\n"
  "ds_read_b64_tr_b16 a[98:99], v8 offset:41984\n"
  "v_mfma_f32_16x16x32_bf16 v[192:195], a[48:51], a[40:43], v[192:195]\n"
  "ds_read_b128 a[80:83], v3 offset:0\n"
  "v_mfma_f32_16x16x32_bf16 v[224:227], a[48:51], a[44:47], v[224:227]\n"
  "ds_read_b64_tr_b16 a[100:101], v9 offset:40960\n"
  "v_mfma_f32_16x16x32_bf16 v[132:135], a[52:55], a[32:35], v[132:135]\n"
  "ds_read_b64_tr_b16 a[102:103], v9 offset:41984\n"
  "v_mfma_f32_16x16x32_bf16 v[164:167], a[52:55], a[36:39], v[164:167]\n"
  "v_mfma_f32_16x16x32_bf16 v[196:199], a[52:55], a[40:43], v[196:199]\n"
  "ds_read_b128 a[84:87], v3 offset:2048\n"
  "v_mfma_f32_16x16x32_bf16 v[228:231], a[52:55], a[44:47], v[228:231]\n"
  "ds_read_b64_tr_b16 a[104:105], v10 offset:40960\n"
  "v_mfma_f32_16x16x32_bf16 v[136:139], a[56:59], a[32:35], v[136:139]\n"
  "ds_read_b64_tr_b16 a[106:107], v10 offset:41984\n"
  "v_mfma_f32_16x16x32_bf16 v[168:171], a[56:59], a[36:39], v[168:171]\n"
  "ds_read_b128 a[88:91], v3 offset:4096\n"
  "v_mfma_f32_16x16x32_bf16 v[200:203], a[56:59], a[40:43], v[200:203]\n"
  "ds_read_b64_tr_b16 a[108:109], v11 offset:40960\n"
  "v_mfma_f32_16x16x32_bf16 v[232:235], a[56:59], a[44:47], v[232:235]\n"
  "v_mfma_f32_16x16x32_bf16 v[140:143], a[60:63], a[32:35], v[140:143]\n"
  "ds_read_b64_tr_b16 a[110:111], v11 offset:41984\n"
  "v_mfma_f32_16x16x32_bf16 v[172:175], a[60:63], a[36:39], v[172:175]\n"
  "ds_read_b128 a[92:95], v3 offset:6144\n"
  "v_mfma_f32_16x16x32_bf16 v[204:207], a[60:63], a[40:43], v[204:207]\n"
  "ds_read_b64_tr_b16 a[112:113], v12 offset:40960\n"
  "v_mfma_f32_16x16x32_bf16 v[236:239], a[60:63], a[44:47], v[236:239]\n"
  "ds_read_b64_tr_b16 a[114:115], v12 offset:41984\n"
  "v_mfma_f32_16x16x32_bf16 v[144:147], a[64:67], a[32:35], v[144:147]\n"
  "ds_read_b64_tr_b16 a[116:117], v13 offset:40960\n"
  "v_mfma_f32_16x16x32_bf16 v[176:179], a[64:67], a[36:39], v[176:179]\n"
  "v_mfma_f32_16x16x32_bf16 v[208:211], a[64:67], a[40:43], v[208:211]\n"
  "ds_read_b64_tr_b16 a[118:119], v13 offset:41984\n"
  "v_mfma_f32_16x16x32_bf16 v[240:243], a[64:67], a[44:47], v[240:243]\n"
  "ds_read_b64_tr_b16 a[120:121], v14 offset:40960\n"
  "v_mfma_f32_16x16x32_bf16 v[148:151], a[68:71], a[32:35], v[148:151]\n"
  "ds_read_b64_tr_b16 a[122:123], v14 offset:41984\n"
  "v_mfma_f32_16x16x32_bf16 v[180:183], a[68:71], a[36:39], v[180:183]\n"
  "ds_read_b64_tr_b16 a[124:125], v15 offset:40960\n"
  "v_mfma_f32_16x16x32_bf16 v[212:215], a[68:71], a[40:43], v[212:215]\n"
  "ds_read_b64_tr_b16 a[126:127], v15 offset:41984\n"
  "v_mfma_f32_16x16x32_bf16 v[244:247], a[68:71], a[44:47], v[244:247]\n"
  "v_mfma_f32_16x16x32_bf16 v[152:155], a[72:75], a[32:35], v[152:155]\n"
  "v_mfma_f32_16x16x32_bf16 v[184:187], a[72:75], a[36:39], v[184:187]\n"
  "v_mfma_f32_16x16x32_bf16 v[216:219], a[72:75], a[40:43], v[216:219]\n"
  "v_mfma_f32_16x16x32_bf16 v[248:251], a[72:75], a[44:47], v[248:251]\n"
  "v_mfma_f32_16x16x32_bf16 v[156:159], a[76:79], a[32:35], v[156:159]\n"
  "v_mfma_f32_16x16x32_bf16 v[188:191], a[76:79], a[36:39], v[188:191]\n"
  "v_mfma_f32_16x16x32_bf16 v[220:223], a[76:79], a[40:43], v[220:223]\n"
  "v_mfma_f32_16x16x32_bf16 v[252:255], a[76:79], a[44:47], v[252:255]\n"
  "s_waitcnt vmcnt(19) lgkmcnt(0)\n"
  "s_barrier\n"
  "v_mfma_f32_16x16x32_bf16 v[128:131], a[96:99], a[80:83], v[128:131]\n"
  "ds_read_b64_tr_b16 a[48:49], v38 offset:16384\n"
  "s_add_u32 m0, s62, 98304\n"
  "s_mov_b32 s64, 384\n"
  "v_mfma_f32_16x16x32_bf16 v[160:163], a[96:99], a[84:87], v[160:163]\n"
  "buffer_load_dwordx4 v42, s[48:51], s64 offen offset:0 lds\n"
  "v_mfma_f32_16x16x32_bf16 v[192:195], a[96:99], a[88:91], v[192:195]\n"
  "ds_read_b64_tr_b16 a[50:51], v38 offset:17408\n"
  "v_mfma_f32_16x16x32_bf16 v[224:227], a[96:99], a[92:95], v[224:227]\n"
  "v_mfma_f32_16x16x32_bf16 v[132:135], a[100:103], a[80:83], v[132:135]\n"
  "ds_read_b128 a[32:35], v36 offset:0\n"
  "v_mfma_f32_16x16x32_bf16 v[164:167], a[100:103], a[84:87], v[164:167]\n"
  "buffer_load_dwordx4 v43, s[48:51], s64 offen offset:1024 lds\n"
  "v_mfma_f32_16x16x32_bf16 v[196:199], a[100:103], a[88:91], v[196:199]\n"
  "ds_read_b64_tr_b16 a[52:53], v39 offset:16384\n"
  "v_mfma_f32_16x16x32_bf16 v[228:231], a[100:103], a[92:95], v[228:231]\n"
  "v_mfma_f32_16x16x32_bf16 v[136:139], a[104:107], a[80:83], v[136:139]\n"
  "ds_read_b64_tr_b16 a[54:55], v39 offset:17408\n"
  "v_mfma_f32_16x16x32_bf16 v[168:171], a[104:107], a[84:87], v[168:171]\n"
  "buffer_load_dwordx4 v44, s[48:51], s64 offen offset:2048 lds\n"
  "v_mfma_f32_16x16x32_bf16 v[200:203], a[104:107], a[88:91], v[200:203]\n"
  "ds_read_b128 a[36:39], v36 offset:2048\n"
  "v_mfma_f32_16x16x32_bf16 v[232:235], a[104:107], a[92:95], v[232:235]\n"
  "v_mfma_f32_16x16x32_bf16 v[140:143], a[108:111], a[80:83], v[140:143]\n"
  "ds_read_b64_tr_b16 a[56:57], v40 offset:16384\n"
  "v_mfma_f32_16x16x32_bf16 v[172:175], a[108:111], a[84:87], v[172:175]\n"
  "buffer_load_dwordx4 v45, s[48:51], s64 offen offset:3072 lds\n"
  "v_mfma_f32_16x16x32_bf16 v[204:207], a[108:111], a[88:91], v[204:207]\n"
  "ds_read_b64_tr_b16 a[58:59], v40 offset:17408\n"
  "s_add_u32 m0, s62, 114688\n"
  "s_mov_b32 s64, 737280\n"
  "v_mfma_f32_16x16x32_bf16 v[236:239], a[108:111], a[92:95], v[236:239]\n"
  "v_mfma_f32_16x16x32_bf16 v[144:147], a[112:115], a[80:83], v[144:147]\n"
  "ds_read_b128 a[40:43], v36 offset:4096\n"
  "v_mfma_f32_16x16x32_bf16 v[176:179], a[112:115], a[84:87], v[176:179]\n"
  "buffer_load_dwordx4 v48, s[84:87], s64 offen offset:0 lds\n"
  "v_mfma_f32_16x16x32_bf16 v[208:211], a[112:115], a[88:91], v[208:211]\n"
  "ds_read_b64_tr_b16 a[60:61], v41 offset:16384\n"
  "v_mfma_f32_16x16x32_bf16 v[240:243], a[112:115], a[92:95], v[240:243]\n"
  "v_mfma_f32_16x16x32_bf16 v[148:151], a[116:119], a[80:83], v[148:151]\n"
  "ds_read_b64_tr_b16 a[62:63], v41 offset:17408\n"
  "v_mfma_f32_16x16x32_bf16 v[180:183], a[116:119], a[84:87], v[180:183]\n"
  "buffer_load_dwordx4 v49, s[84:87], s64 offen offset:1024 lds\n"
  "v_mfma_f32_16x16x32_bf16 v[212:215], a[116:119], a[88:91], v[212:215]\n"
  "ds_read_b128 a[44:47], v36 offset:6144\n"
  "v_mfma_f32_16x16x32_bf16 v[244:247], a[116:119], a[92:95], v[244:247]\n"
  "v_mfma_f32_16x16x32_bf16 v[152:155], a[120:123], a[80:83], v[152:155]\n"
  "v_mfma_f32_16x16x32_bf16 v[184:187], a[120:123], a[84:87], v[184:187]\n"
  "buffer_load_dwordx4 v50, s[84:87], s64 offen offset:2048 lds\n"
  "v_mfma_f32_16x16x32_bf16 v[216:219], a[120:123], a[88:91], v[216:219]\n"
  "v_mfma_f32_16x16x32_bf16 v[248:251], a[120:123], a[92:95], v[248:251]\n"
  "v_mfma_f32_16x16x32_bf16 v[156:159], a[124:127], a[80:83], v[156:159]\n"
  "v_mfma_f32_16x16x32_bf16 v[188:191], a[124:127], a[84:87], v[188:191]\n"
  "buffer_load_dwordx4 v51, s[84:87], s64 offen offset:3072 lds\n"
  "v_mfma_f32_16x16x32_bf16 v[220:223], a[124:127], a[88:91], v[220:223]\n"
  "s_add_u32 m0, s62, 131072\n"
  "s_mov_b32 s64, 512\n"
  "v_mfma_f32_16x16x32_bf16 v[252:255], a[124:127], a[92:95], v[252:255]\n"
  "s_waitcnt lgkmcnt(0)\n"
  "v_mfma_f32_16x16x32_bf16 a[128:131], a[48:51], a[32:35], 0\n"
  "ds_read_b64_tr_b16 a[96:97], v38 offset:24576\n"
  "buffer_load_dwordx4 v42, s[48:51], s64 offen offset:0 lds\n"
  "v_mfma_f32_16x16x32_bf16 a[144:147], a[48:51], a[36:39], 0\n"
  "ds_read_b64_tr_b16 a[98:99], v38 offset:25600\n"
  "v_mfma_f32_16x16x32_bf16 a[160:163], a[48:51], a[40:43], 0\n"
  "ds_read_b128 a[80:83], v37 offset:0\n"
  "buffer_load_dwordx4 v43, s[48:51], s64 offen offset:1024 lds\n"
  "v_mfma_f32_16x16x32_bf16 a[176:179], a[48:51], a[44:47], 0\n"
  "ds_read_b64_tr_b16 a[100:101], v39 offset:24576\n"
  "v_mfma_f32_16x16x32_bf16 a[132:135], a[52:55], a[32:35], 0\n"
  "ds_read_b64_tr_b16 a[102:103], v39 offset:25600\n"
  "buffer_load_dwordx4 v44, s[48:51], s64 offen offset:2048 lds\n"
  "v_mfma_f32_16x16x32_bf16 a[148:151], a[52:55], a[36:39], 0\n"
  "ds_read_b128 a[84:87], v37 offset:2048\n"
  "v_mfma_f32_16x16x32_bf16 a[164:167], a[52:55], a[40:43], 0\n"
  "ds_read_b64_tr_b16 a[104:105], v40 offset:24576\n"
  "buffer_load_dwordx4 v45, s[48:51], s64 offen offset:3072 lds\n"
  "v_mfma_f32_16x16x32_bf16 a[180:183], a[52:55], a[44:47], 0\n"
  "ds_read_b64_tr_b16 a[106:107], v40 offset:25600\n"
  "s_add_u32 m0, s62, 147456\n"
  "s_mov_b32 s64, 983040\n"
  "v_mfma_f32_16x16x32_bf16 a[136:139], a[56:59], a[32:35], 0\n"
  "ds_read_b128 a[88:91], v37 offset:4096\n"
  "buffer_load_dwordx4 v48, s[84:87], s64 offen offset:0 lds\n"
  "v_mfma_f32_16x16x32_bf16 a[152:155], a[56:59], a[36:39], 0\n"
  "ds_read_b64_tr_b16 a[108:109], v41 offset:24576\n"
  "v_mfma_f32_16x16x32_bf16 a[168:171], a[56:59], a[40:43], 0\n"
  "ds_read_b64_tr_b16 a[110:111], v41 offset:25600\n"
  "buffer_load_dwordx4 v49, s[84:87], s64 offen offset:1024 lds\n"
  "v_mfma_f32_16x16x32_bf16 a[184:187], a[56:59], a[44:47], 0\n"
  "ds_read_b128 a[92:95], v37 offset:6144\n"
  "v_mfma_f32_16x16x32_bf16 a[140:143], a[60:63], a[32:35], 0\n"
  "buffer_load_dwordx4 v50, s[84:87], s64 offen offset:2048 lds\n"
  "v_add_u32 v36, 0x8000, v36\n"
  "v_mfma_f32_16x16x32_bf16 a[156:159], a[60:63], a[36:39], 0\n"
  "v_add_u32 v37, 0x8000, v37\n"
  "v_add_u32 v38, 0x8000, v38\n"
  "v_mfma_f32_16x16x32_bf16 a[172:175], a[60:63], a[40:43], 0\n"
  "buffer_load_dwordx4 v51, s[84:87], s64 offen offset:3072 lds\n"
  "v_add_u32 v39, 0x8000, v39\n"
  "v_mfma_f32_16x16x32_bf16 a[188:191], a[60:63], a[44:47], 0\n"
  "v_add_u32 v40, 0x8000, v40\n"
  "v_add_u32 v41, 0x8000, v41\n"
  "s_waitcnt vmcnt(26) lgkmcnt(0)\n"
  "s_barrier\n"
  "v_mfma_f32_16x16x32_bf16 a[128:131], a[96:99], a[80:83], a[128:131]\n"
  "ds_read_b64_tr_b16 a[48:49], v38 offset:16384\n"
  "s_add_u32 m0, s62, 0\n"
  "s_mov_b32 s64, 640\n"
  "v_mfma_f32_16x16x32_bf16 a[144:147], a[96:99], a[84:87], a[144:147]\n"
  "ds_read_b64_tr_b16 a[50:51], v38 offset:17408\n"
  "buffer_load_dwordx4 v42, s[48:51], s64 offen offset:0 lds\n"
  "v_mfma_f32_16x16x32_bf16 a[160:163], a[96:99], a[88:91], a[160:163]\n"
  "ds_read_b128 a[32:35], v36 offset:0\n"
  "v_mfma_f32_16x16x32_bf16 a[176:179], a[96:99], a[92:95], a[176:179]\n"
  "ds_read_b64_tr_b16 a[52:53], v39 offset:16384\n"
  "buffer_load_dwordx4 v43, s[48:51], s64 offen offset:1024 lds\n"
  "v_mfma_f32_16x16x32_bf16 a[132:135], a[100:103], a[80:83], a[132:135]\n"
  "ds_read_b64_tr_b16 a[54:55], v39 offset:17408\n"
  "v_mul_f32 v128, 0x3e0293ee, v128\n"
  "v_mfma_f32_16x16x32_bf16 a[148:151], a[100:103], a[84:87], a[148:151]\n"
  "ds_read_b128 a[36:39], v36 offset:2048\n"
  "buffer_load_dwordx4 v44, s[48:51], s64 offen offset:2048 lds\n"
  "v_mfma_f32_16x16x32_bf16 a[164:167], a[100:103], a[88:91], a[164:167]\n"
  "ds_read_b64_tr_b16 a[56:57], v40 offset:16384\n"
  "v_mul_f32 v129, 0x3e0293ee, v129\n"
  "v_mfma_f32_16x16x32_bf16 a[180:183], a[100:103], a[92:95], a[180:183]\n"
  "ds_read_b64_tr_b16 a[58:59], v40 offset:17408\n"
  "buffer_load_dwordx4 v45, s[48:51], s64 offen offset:3072 lds\n"
  "v_cvt_pk_bf16_f32 v64, v128, v129\n"
  "v_mfma_f32_16x16x32_bf16 a[136:139], a[104:107], a[80:83], a[136:139]\n"
  "ds_read_b128 a[40:43], v36 offset:4096\n"
  "s_add_u32 m0, s62, 16384\n"
  "s_mov_b32 s64, 1228800\n"
  "v_mul_f32 v130, 0x3e0293ee, v130\n"
  "v_mfma_f32_16x16x32_bf16 a[152:155], a[104:107], a[84:87], a[152:155]\n"
  "ds_read_b64_tr_b16 a[60:61], v41 offset:16384\n"
  "buffer_load_dwordx4 v48, s[84:87], s64 offen offset:0 lds\n"
  "v_mul_f32 v131, 0x3e0293ee, v131\n"
  "v_mfma_f32_16x16x32_bf16 a[168:171], a[104:107], a[88:91], a[168:171]\n"
  "ds_read_b64_tr_b16 a[62:63], v41 offset:17408\n"
  "v_cvt_pk_bf16_f32 v65, v130, v131\n"
  "v_mfma_f32_16x16x32_bf16 a[184:187], a[104:107], a[92:95], a[184:187]\n"
  "ds_read_b128 a[44:47], v36 offset:6144\n"
  "buffer_load_dwordx4 v49, s[84:87], s64 offen offset:1024 lds\n"
  "v_mfma_f32_16x16x32_bf16 a[140:143], a[108:111], a[80:83], a[140:143]\n"
  "v_mul_f32 v132, 0x3e0293ee, v132\n"
  "v_mfma_f32_16x16x32_bf16 a[156:159], a[108:111], a[84:87], a[156:159]\n"
  "buffer_load_dwordx4 v50, s[84:87], s64 offen offset:2048 lds\n"
  "v_mul_f32 v133, 0x3e0293ee, v133\n"
  "v_mfma_f32_16x16x32_bf16 a[172:175], a[108:111], a[88:91], a[172:175]\n"
  "v_cvt_pk_bf16_f32 v66, v132, v133\n"
  "v_mfma_f32_16x16x32_bf16 a[188:191], a[108:111], a[92:95], a[188:191]\n"
  "buffer_load_dwordx4 v51, s[84:87], s64 offen offset:3072 lds\n"
  "v_mul_f32 v134, 0x3e0293ee, v134\n"
  "s_waitcnt lgkmcnt(0)\n"
  "v_mfma_f32_16x16x32_bf16 a[128:131], a[48:51], a[32:35], a[128:131]\n"
  "ds_read_b64_tr_b16 a[96:97], v38 offset:24576\n"
  "v_mul_f32 v135, 0x3e0293ee, v135\n"
  "v_mfma_f32_16x16x32_bf16 a[144:147], a[48:51], a[36:39], a[144:147]\n"
  "ds_read_b64_tr_b16 a[98:99], v38 offset:25600\n"
  "v_mfma_f32_16x16x32_bf16 a[160:163], a[48:51], a[40:43], a[160:163]\n"
  "ds_read_b128 a[80:83], v37 offset:0\n"
  "v_cvt_pk_bf16_f32 v67, v134, v135\n"
  "v_mfma_f32_16x16x32_bf16 a[176:179], a[48:51], a[44:47], a[176:179]\n"
  "ds_read_b64_tr_b16 a[100:101], v39 offset:24576\n"
  "v_mul_f32 v136, 0x3e0293ee, v136\n"
  "v_mfma_f32_16x16x32_bf16 a[132:135], a[52:55], a[32:35], a[132:135]\n"
  "ds_read_b64_tr_b16 a[102:103], v39 offset:25600\n"
  "v_mul_f32 v137, 0x3e0293ee, v137\n"
  "v_mfma_f32_16x16x32_bf16 a[148:151], a[52:55], a[36:39], a[148:151]\n"
  "ds_read_b128 a[84:87], v37 offset:2048\n"
  "v_cvt_pk_bf16_f32 v68, v136, v137\n"
  "v_mfma_f32_16x16x32_bf16 a[164:167], a[52:55], a[40:43], a[164:167]\n"
  "ds_read_b64_tr_b16 a[104:105], v40 offset:24576\n"
  "v_mul_f32 v138, 0x3e0293ee, v138\n"
  "v_mfma_f32_16x16x32_bf16 a[180:183], a[52:55], a[44:47], a[180:183]\n"
  "ds_read_b64_tr_b16 a[106:107], v40 offset:25600\n"
  "v_mfma_f32_16x16x32_bf16 a[136:139], a[56:59], a[32:35], a[136:139]\n"
  "ds_read_b128 a[88:91], v37 offset:4096\n"
  "v_mul_f32 v139, 0x3e0293ee, v139\n"
  "v_mfma_f32_16x16x32_bf16 a[152:155], a[56:59], a[36:39], a[152:155]\n"
  "ds_read_b64_tr_b16 a[108:109], v41 offset:24576\n"
  "v_cvt_pk_bf16_f32 v69, v138, v139\n"
  "v_mfma_f32_16x16x32_bf16 a[168:171], a[56:59], a[40:43], a[168:171]\n"
  "ds_read_b64_tr_b16 a[110:111], v41 offset:25600\n"
  "v_mul_f32 v140, 0x3e0293ee, v140\n"
  "v_mfma_f32_16x16x32_bf16 a[184:187], a[56:59], a[44:47], a[184:187]\n"
  "ds_read_b128 a[92:95], v37 offset:6144\n"
  "v_mul_f32 v141, 0x3e0293ee, v141\n"
  "v_mfma_f32_16x16x32_bf16 a[140:143], a[60:63], a[32:35], a[140:143]\n"
  "v_add_u32 v36, 0x8000, v36\n"
  "v_cvt_pk_bf16_f32 v70, v140, v141\n"
  "v_mfma_f32_16x16x32_bf16 a[156:159], a[60:63], a[36:39], a[156:159]\n"
  "v_add_u32 v37, 0x8000, v37\n"
  "v_add_u32 v38, 0x8000, v38\n"
  "v_mfma_f32_16x16x32_bf16 a[172:175], a[60:63], a[40:43], a[172:175]\n"
  "v_add_u32 v39, 0x8000, v39\n"
  "v_mul_f32 v142, 0x3e0293ee, v142\n"
  "v_mfma_f32_16x16x32_bf16 a[188:191], a[60:63], a[44:47], a[188:191]\n"
  "v_add_u32 v40, 0x8000, v40\n"
  "v_add_u32 v41, 0x8000, v41\n"
  "s_waitcnt vmcnt(24) lgkmcnt(0)\n"
  "s_barrier\n"
  "v_readfirstlane_b32 s58, v62\n"
  "s_cmp_lg_u32 s58, s21\n"
  "s_cbranch_scc0 z_spin%=\n"
  "z_ok%=:\n"
  "buffer_atomic_add v7, v6, s[88:91], s69 offen offset:128 sc1\n"
  "v_mfma_f32_16x16x32_bf16 a[128:131], a[96:99], a[80:83], a[128:131]\n"
  "ds_read_b64_tr_b16 a[48:49], v38 offset:16384\n"
  "s_add_u32 m0, s62, 32768\n"
  "s_mov_b32 s64, 768\n"
  "v_mul_f32 v143, 0x3e0293ee, v143\n"
  "v_cvt_pk_bf16_f32 v71, v142, v143\n"
  "v_mfma_f32_16x16x32_bf16 a[144:147], a[96:99], a[84:87], a[144:147]\n"
  "ds_read_b64_tr_b16 a[50:51], v38 offset:17408\n"
  "buffer_load_dwordx4 v42, s[48:51], s64 offen offset:0 lds\n"
  "v_mul_f32 v144, 0x3e0293ee, v144\n"
  "v_mfma_f32_16x16x32_bf16 a[160:163], a[96:99], a[88:91], a[160:163]\n"
  "ds_read_b128 a[32:35], v36 offset:0\n"
  "v_mul_f32 v145, 0x3e0293ee, v145\n"
  "v_mfma_f32_16x16x32_bf16 a[176:179], a[96:99], a[92:95], a[176:179]\n"
  "ds_read_b64_tr_b16 a[52:53], v39 offset:16384\n"
  "buffer_load_dwordx4 v43, s[48:51], s64 offen offset:1024 lds\n"
  "v_mfma_f32_16x16x32_bf16 a[132:135], a[100:103], a[80:83], a[132:135]\n"
  "ds_read_b64_tr_b16 a[54:55], v39 offset:17408\n"
  "v_cvt_pk_bf16_f32 v72, v144, v145\n"
  "v_mfma_f32_16x16x32_bf16 a[148:151], a[100:103], a[84:87], a[148:151]\n"
  "ds_read_b128 a[36:39], v36 offset:2048\n"
  "buffer_load_dwordx4 v44, s[48:51], s64 offen offset:2048 lds\n"
  "v_mul_f32 v146, 0x3e0293ee, v146\n"
  "v_mfma_f32_16x16x32_bf16 a[164:167], a[100:103], a[88:91], a[164:167]\n"
  "ds_read_b64_tr_b16 a[56:57], v40 offset:16384\n"
  "v_mul_f32 v147, 0x3e0293ee, v147\n"
  "v_mfma_f32_16x16x32_bf16 a[180:183], a[100:103], a[92:95], a[180:183]\n"
  "ds_read_b64_tr_b16 a[58:59], v40 offset:17408\n"
  "buffer_load_dwordx4 v45, s[48:51], s64 offen offset:3072 lds\n"
  "v_cvt_pk_bf16_f32 v73, v146, v147\n"
  "v_mfma_f32_16x16x32_bf16 a[136:139], a[104:107], a[80:83], a[136:139]\n"
  "ds_read_b128 a[40:43], v36 offset:4096\n"
  "s_add_u32 m0, s62, 49152\n"
  "s_mov_b32 s64, 1474560\n"
  "v_mul_f32 v148, 0x3e0293ee, v148\n"
  "v_mfma_f32_16x16x32_bf16 a[152:155], a[104:107], a[84:87], a[152:155]\n"
  "ds_read_b64_tr_b16 a[60:61], v41 offset:16384\n"
  "buffer_load_dwordx4 v48, s[84:87], s64 offen offset:0 lds\n"
  "v_mfma_f32_16x16x32_bf16 a[168:171], a[104:107], a[88:91], a[168:171]\n"
  "ds_read_b64_tr_b16 a[62:63], v41 offset:17408\n"
  "v_mul_f32 v149, 0x3e0293ee, v149\n"
  "v_mfma_f32_16x16x32_bf16 a[184:187], a[104:107], a[92:95], a[184:187]\n"
  "ds_read_b128 a[44:47], v36 offset:6144\n"
  "buffer_load_dwordx4 v49, s[84:87], s64 offen offset:1024 lds\n"
  "v_cvt_pk_bf16_f32 v74, v148, v149\n"
  "v_mfma_f32_16x16x32_bf16 a[140:143], a[108:111], a[80:83], a[140:143]\n"
  "v_mul_f32 v150, 0x3e0293ee, v150\n"
  "v_mfma_f32_16x16x32_bf16 a[156:159], a[108:111], a[84:87], a[156:159]\n"
  "buffer_load_dwordx4 v50, s[84:87], s64 offen offset:2048 lds\n"
  "v_mul_f32 v151, 0x3e0293ee, v151\n"
  "v_mfma_f32_16x16x32_bf16 a[172:175], a[108:111], a[88:91], a[172:175]\n"
  "v_cvt_pk_bf16_f32 v75, v150, v151\n"
  "v_mfma_f32_16x16x32_bf16 a[188:191], a[108:111], a[92:95], a[188:191]\n"
  "buffer_load_dwordx4 v51, s[84:87], s64 offen offset:3072 lds\n"
  "s_waitcnt lgkmcnt(0)\n"
  "v_mfma_f32_16x16x32_bf16 a[128:131], a[48:51], a[32:35], a[128:131]\n"
  "ds_read_b64_tr_b16 a[96:97], v38 offset:24576\n"
  "v_mul_f32 v152, 0x3e0293ee, v152\n"
  "v_mfma_f32_16x16x32_bf16 a[144:147], a[48:51], a[36:39], a[144:147]\n"
  "ds_read_b64_tr_b16 a[98:99], v38 offset:25600\n"
  "v_mul_f32 v153, 0x3e0293ee, v153\n"
  "v_mfma_f32_16x16x32_bf16 a[160:163], a[48:51], a[40:43], a[160:163]\n"
  "ds_read_b128 a[80:83], v37 offset:0\n"
  "v_cvt_pk_bf16_f32 v76, v152, v153\n"
  "v_mfma_f32_16x16x32_bf16 a[176:179], a[48:51], a[44:47], a[176:179]\n"
  "ds_read_b64_tr_b16 a[100:101], v39 offset:24576\n"
  "v_mul_f32 v154, 0x3e0293ee, v154\n"
  "v_mfma_f32_16x16x32_bf16 a[132:135], a[52:55], a[32:35], a[132:135]\n"
  "ds_read_b64_tr_b16 a[102:103], v39 offset:25600\n"
  "v_mul_f32 v155, 0x3e0293ee, v155\n"
  "v_mfma_f32_16x16x32_bf16 a[148:151], a[52:55], a[36:39], a[148:151]\n"
  "ds_read_b128 a[84:87], v37 offset:2048\n"
  "v_mfma_f32_16x16x32_bf16 a[164:167], a[52:55], a[40:43], a[164:167]\n"
  "ds_read_b64_tr_b16 a[104:105], v40 offset:24576\n"
  "v_cvt_pk_bf16_f32 v77, v154, v155\n"
  "v_mfma_f32_16x16x32_bf16 a[180:183], a[52:55], a[44:47], a[180:183]\n"
  "ds_read_b64_tr_b16 a[106:107], v40 offset:25600\n"
  "v_mul_f32 v156, 0x3e0293ee, v156\n"
  "v_mfma_f32_16x16x32_bf16 a[136:139], a[56:59], a[32:35], a[136:139]\n"
  "ds_read_b128 a[88:91], v37 offset:4096\n"
  "v_mul_f32 v157, 0x3e0293ee, v157\n"
  "v_mfma_f32_16x16x32_bf16 a[152:155], a[56:59], a[36:39], a[152:155]\n"
  "ds_read_b64_tr_b16 a[108:109], v41 offset:24576\n"
  "v_cvt_pk_bf16_f32 v78, v156, v157\n"
  "v_mfma_f32_16x16x32_bf16 a[168:171], a[56:59], a[40:43], a[168:171]\n"
  "ds_read_b64_tr_b16 a[110:111], v41 offset:25600\n"
  "v_mul_f32 v158, 0x3e0293ee, v158\n"
  "v_mfma_f32_16x16x32_bf16 a[184:187], a[56:59], a[44:47], a[184:187]\n"
  "ds_read_b128 a[92:95], v37 offset:6144\n"
  "v_mfma_f32_16x16x32_bf16 a[140:143], a[60:63], a[32:35], a[140:143]\n"
  "v_add_u32 v36, 0x8000, v36\n"
  "v_mul_f32 v159, 0x3e0293ee, v159\n"
  "v_mfma_f32_16x16x32_bf16 a[156:159], a[60:63], a[36:39], a[156:159]\n"
  "v_add_u32 v37, 0x8000, v37\n"
  "v_add_u32 v38, 0x8000, v38\n"
  "v_mfma_f32_16x16x32_bf16 a[172:175], a[60:63], a[40:43], a[172:175]\n"
  "v_add_u32 v39, 0x8000, v39\n"
  "v_cvt_pk_bf16_f32 v79, v158, v159\n"
  "v_mfma_f32_16x16x32_bf16 a[188:191], a[60:63], a[44:47], a[188:191]\n"
  "v_add_u32 v40, 0x8000, v40\n"
  "v_add_u32 v41, 0x8000, v41\n"
  "s_waitcnt vmcnt(25) lgkmcnt(0)\n"
  "s_barrier\n"
  "v_mfma_f32_16x16x32_bf16 a[128:131], a[96:99], a[80:83], a[128:131]\n"
  "ds_read_b64_tr_b16 a[48:49], v38 offset:16384\n"
  "s_add_u32 m0, s62, 65536\n"
  "s_mov_b32 s64, 896\n"
  "v_mul_f32 v160, 0x3e0293ee, v160\n"
  "v_mul_f32 v161, 0x3e0293ee, v161\n"
  "v_mfma_f32_16x16x32_bf16 a[144:147], a[96:99], a[84:87], a[144:147]\n"
  "ds_read_b64_tr_b16 a[50:51], v38 offset:17408\n"
  "buffer_load_dwordx4 v42, s[48:51], s64 offen offset:0 lds\n"
  "v_cvt_pk_bf16_f32 v80, v160, v161\n"
  "v_mfma_f32_16x16x32_bf16 a[160:163], a[96:99], a[88:91], a[160:163]\n"
  "ds_read_b128 a[32:35], v36 offset:0\n"
  "v_mul_f32 v162, 0x3e0293ee, v162\n"
  "v_mfma_f32_16x16x32_bf16 a[176:179], a[96:99], a[92:95], a[176:179]\n"
  "ds_read_b64_tr_b16 a[52:53], v39 offset:16384\n"
  "buffer_load_dwordx4 v43, s[48:51], s64 offen offset:1024 lds\n"
  "v_mul_f32 v163, 0x3e0293ee, v163\n"
  "v_mfma_f32_16x16x32_bf16 a[132:135], a[100:103], a[80:83], a[132:135]\n"
  "ds_read_b64_tr_b16 a[54:55], v39 offset:17408\n"
  "v_cvt_pk_bf16_f32 v81, v162, v163\n"
  "v_mfma_f32_16x16x32_bf16 a[148:151], a[100:103], a[84:87], a[148:151]\n"
  "ds_read_b128 a[36:39], v36 offset:2048\n"
  "buffer_load_dwordx4 v44, s[48:51], s64 offen offset:2048 lds\n"
  "v_mul_f32 v164, 0x3e0293ee, v164\n"
  "v_mfma_f32_16x16x32_bf16 a[164:167], a[100:103], a[88:91], a[164:167]\n"
  "ds_read_b64_tr_b16 a[56:57], v40 offset:16384\n"
  "v_mul_f32 v165, 0x3e0293ee, v165\n"
  "v_mfma_f32_16x16x32_bf16 a[180:183], a[100:103], a[92:95], a[180:183]\n"
  "ds_read_b64_tr_b16 a[58:59], v40 offset:17408\n"
  "buffer_load_dwordx4 v45, s[48:51], s64 offen offset:3072 lds\n"
  "v_mfma_f32_16x16x32_bf16 a[136:139], a[104:107], a[80:83], a[136:139]\n"
  "ds_read_b128 a[40:43], v36 offset:4096\n"
  "s_add_u32 m0, s62, 81920\n"
  "s_mov_b32 s64, 1720320\n"
  "v_cvt_pk_bf16_f32 v82, v164, v165\n"
  "v_mfma_f32_16x16x32_bf16 a[152:155], a[104:107], a[84:87], a[152:155]\n"
  "ds_read_b64_tr_b16 a[60:61], v41 offset:16384\n"
  "buffer_load_dwordx4 v48, s[84:87], s64 offen offset:0 lds\n"
  "v_mul_f32 v166, 0x3e0293ee, v166\n"
  "v_mfma_f32_16x16x32_bf16 a[168:171], a[104:107], a[88:91], a[168:171]\n"
  "ds_read_b64_tr_b16 a[62:63], v41 offset:17408\n"
  "v_mul_f32 v167, 0x3e0293ee, v167\n"
  "v_mfma_f32_16x16x32_bf16 a[184:187], a[104:107], a[92:95], a[184:187]\n"
  "ds_read_b128 a[44:47], v36 offset:6144\n"
  "buffer_load_dwordx4 v49, s[84:87], s64 offen offset:1024 lds\n"
  "v_cvt_pk_bf16_f32 v83, v166, v167\n"
  "v_mfma_f32_16x16x32_bf16 a[140:143], a[108:111], a[80:83], a[140:143]\n"
  "v_mul_f32 v168, 0x3e0293ee, v168\n"
  "v_mfma_f32_16x16x32_bf16 a[156:159], a[108:111], a[84:87], a[156:159]\n"
  "buffer_load_dwordx4 v50, s[84:87], s64 offen offset:2048 lds\n"
  "v_mul_f32 v169, 0x3e0293ee, v169\n"
  "v_mfma_f32_16x16x32_bf16 a[172:175], a[108:111], a[88:91], a[172:175]\n"
  "v_mfma_f32_16x16x32_bf16 a[188:191], a[108:111], a[92:95], a[188:191]\n"
  "buffer_load_dwordx4 v51, s[84:87], s64 offen offset:3072 lds\n"
  "v_cvt_pk_bf16_f32 v84, v168, v169\n"
  "s_waitcnt lgkmcnt(0)\n"
  "v_mfma_f32_16x16x32_bf16 a[128:131], a[48:51], a[32:35], a[128:131]\n"
  "ds_read_b64_tr_b16 a[96:97], v38 offset:24576\n"
  "v_mul_f32 v170, 0x3e0293ee, v170\n"
  "v_mfma_f32_16x16x32_bf16 a[144:147], a[48:51], a[36:39], a[144:147]\n"
  "ds_read_b64_tr_b16 a[98:99], v38 offset:25600\n"
  "v_mul_f32 v171, 0x3e0293ee, v171\n"
  "v_mfma_f32_16x16x32_bf16 a[160:163], a[48:51], a[40:43], a[160:163]\n"
  "ds_read_b128 a[80:83], v37 offset:0\n"
  "v_cvt_pk_bf16_f32 v85, v170, v171\n"
  "v_mfma_f32_16x16x32_bf16 a[176:179], a[48:51], a[44:47], a[176:179]\n"
  "ds_read_b64_tr_b16 a[100:101], v39 offset:24576\n"
  "v_mul_f32 v172, 0x3e0293ee, v172\n"
  "v_mfma_f32_16x16x32_bf16 a[132:135], a[52:55], a[32:35], a[132:135]\n"
  "ds_read_b64_tr_b16 a[102:103], v39 offset:25600\n"
  "v_mfma_f32_16x16x32_bf16 a[148:151], a[52:55], a[36:39], a[148:151]\n"
  "ds_read_b128 a[84:87], v37 offset:2048\n"
  "v_mul_f32 v173, 0x3e0293ee, v173\n"
  "v_mfma_f32_16x16x32_bf16 a[164:167], a[52:55], a[40:43], a[164:167]\n"
  "ds_read_b64_tr_b16 a[104:105], v40 offset:24576\n"
  "v_cvt_pk_bf16_f32 v86, v172, v173\n"
  "v_mfma_f32_16x16x32_bf16 a[180:183], a[52:55], a[44:47], a[180:183]\n"
  "ds_read_b64_tr_b16 a[106:107], v40 offset:25600\n"
  "v_mul_f32 v174, 0x3e0293ee, v174\n"
  "v_mfma_f32_16x16x32_bf16 a[136:139], a[56:59], a[32:35], a[136:139]\n"
  "ds_read_b128 a[88:91], v37 offset:4096\n"
  "v_mul_f32 v175, 0x3e0293ee, v175\n"
  "v_mfma_f32_16x16x32_bf16 a[152:155], a[56:59], a[36:39], a[152:155]\n"
  "ds_read_b64_tr_b16 a[108:109], v41 offset:24576\n"
  "v_cvt_pk_bf16_f32 v87, v174, v175\n"
  "v_mfma_f32_16x16x32_bf16 a[168:171], a[56:59], a[40:43], a[168:171]\n"
  "ds_read_b64_tr_b16 a[110:111], v41 offset:25600\n"
  "v_mfma_f32_16x16x32_bf16 a[184:187], a[56:59], a[44:47], a[184:187]\n"
  "ds_read_b128 a[92:95], v37 offset:6144\n"
  "v_mul_f32 v176, 0x3e0293ee, v176\n"
  "v_mfma_f32_16x16x32_bf16 a[140:143], a[60:63], a[32:35], a[140:143]\n"
  "v_add_u32 v36, 0x8000, v36\n"
  "v_mul_f32 v177, 0x3e0293ee, v177\n"
  "v_mfma_f32_16x16x32_bf16 a[156:159], a[60:63], a[36:39], a[156:159]\n"
  "v_add_u32 v37, 0x8000, v37\n"
  "v_add_u32 v38, 0x8000, v38\n"
  "v_mfma_f32_16x16x32_bf16 a[172:175], a[60:63], a[40:43], a[172:175]\n"
  "v_add_u32 v39, 0x8000, v39\n"
  "v_cvt_pk_bf16_f32 v88, v176, v177\n"
  "v_mfma_f32_16x16x32_bf16 a[188:191], a[60:63], a[44:47], a[188:191]\n"
  "v_add_u32 v40, 0x8000, v40\n"
  "v_add_u32 v41, 0x8000, v41\n"
  "s_waitcnt vmcnt(25) lgkmcnt(0)\n"
  "s_barrier\n"
  "v_mfma_f32_16x16x32_bf16 a[128:131], a[96:99], a[80:83], a[128:131]\n"
  "ds_read_b64_tr_b16 a[48:49], v38 offset:16384\n"
  "s_add_u32 m0, s62, 98304\n"
  "s_mov_b32 s64, 1024\n"
  "v_mul_f32 v178, 0x3e0293ee, v178\n"
  "v_mul_f32 v179, 0x3e0293ee, v179\n"
  "v_mfma_f32_16x16x32_bf16 a[144:147], a[96:99], a[84:87], a[144:147]\n"
  "ds_read_b64_tr_b16 a[50:51], v38 offset:17408\n"
  "buffer_load_dwordx4 v42, s[48:51], s64 offen offset:0 lds\n"
  "v_cvt_pk_bf16_f32 v89, v178, v179\n"
  "v_mfma_f32_16x16x32_bf16 a[160:163], a[96:99], a[88:91], a[160:163]\n"
  "ds_read_b128 a[32:35], v36 offset:0\n"
  "v_mul_f32 v180, 0x3e0293ee, v180\n"
  "v_mfma_f32_16x16x32_bf16 a[176:179], a[96:99], a[92:95], a[176:179]\n"
  "ds_read_b64_tr_b16 a[52:53], v39 offset:16384\n"
  "buffer_load_dwordx4 v43, s[48:51], s64 offen offset:1024 lds\n"
  "v_mul_f32 v181, 0x3e0293ee, v181\n"
  "v_mfma_f32_16x16x32_bf16 a[132:135], a[100:103], a[80:83], a[132:135]\n"
  "ds_read_b64_tr_b16 a[54:55], v39 offset:17408\n"
  "v_cvt_pk_bf16_f32 v90, v180, v181\n"
  "v_mfma_f32_16x16x32_bf16 a[148:151], a[100:103], a[84:87], a[148:151]\n"
  "ds_read_b128 a[36:39], v36 offset:2048\n"
  "buffer_load_dwordx4 v44, s[48:51], s64 offen offset:2048 lds\n"
  "v_mul_f32 v182, 0x3e0293ee, v182\n"
  "v_mfma_f32_16x16x32_bf16 a[164:167], a[100:103], a[88:91], a[164:167]\n"
  "ds_read_b64_tr_b16 a[56:57], v40 offset:16384\n"
  "v_mfma_f32_16x16x32_bf16 a[180:183], a[100:103], a[92:95], a[180:183]\n"
  "ds_read_b64_tr_b16 a[58:59], v40 offset:17408\n"
  "buffer_load_dwordx4 v45, s[48:51], s64 offen offset:3072 lds\n"
  "v_mul_f32 v183, 0x3e0293ee, v183\n"
  "v_mfma_f32_16x16x32_bf16 a[136:139], a[104:107], a[80:83], a[136:139]\n"
  "ds_read_b128 a[40:43], v36 offset:4096\n"
  "s_add_u32 m0, s62, 114688\n"
  "s_mov_b32 s64, 1966080\n"
  "v_cvt_pk_bf16_f32 v91, v182, v183\n"
  "v_mfma_f32_16x16x32_bf16 a[152:155], a[104:107], a[84:87], a[152:155]\n"
  "ds_read_b64_tr_b16 a[60:61], v41 offset:16384\n"
  "buffer_load_dwordx4 v48, s[84:87], s64 offen offset:0 lds\n"
  "v_mul_f32 v184, 0x3e0293ee, v184\n"
  "v_mfma_f32_16x16x32_bf16 a[168:171], a[104:107], a[88:91], a[168:171]\n"
  "ds_read_b64_tr_b16 a[62:63], v41 offset:17408\n"
  "v_mul_f32 v185, 0x3e0293ee, v185\n"
  "v_mfma_f32_16x16x32_bf16 a[184:187], a[104:107], a[92:95], a[184:187]\n"
  "ds_read_b128 a[44:47], v36 offset:6144\n"
  "buffer_load_dwordx4 v49, s[84:87], s64 offen offset:1024 lds\n"
  "v_cvt_pk_bf16_f32 v92, v184, v185\n"
  "v_mfma_f32_16x16x32_bf16 a[140:143], a[108:111], a[80:83], a[140:143]\n"
  "v_mfma_f32_16x16x32_bf16 a[156:159], a[108:111], a[84:87], a[156:159]\n"
  "buffer_load_dwordx4 v50, s[84:87], s64 offen offset:2048 lds\n"
  "v_mul_f32 v186, 0x3e0293ee, v186\n"
  "v_mfma_f32_16x16x32_bf16 a[172:175], a[108:111], a[88:91], a[172:175]\n"
  "v_mul_f32 v187, 0x3e0293ee, v187\n"
  "v_mfma_f32_16x16x32_bf16 a[188:191], a[108:111], a[92:95], a[188:191]\n"
  "buffer_load_dwordx4 v51, s[84:87], s64 offen offset:3072 lds\n"
  "v_cvt_pk_bf16_f32 v93, v186, v187\n"
  "s_waitcnt lgkmcnt(0)\n"
  "v_mfma_f32_16x16x32_bf16 a[128:131], a[48:51], a[32:35], a[128:131]\n"
  "ds_read_b64_tr_b16 a[96:97], v38 offset:24576\n"
  "v_mul_f32 v188, 0x3e0293ee, v188\n"
  "v_mfma_f32_16x16x32_bf16 a[144:147], a[48:51], a[36:39], a[144:147]\n"
  "ds_read_b64_tr_b16 a[98:99], v38 offset:25600\n"
  "v_mul_f32 v189, 0x3e0293ee, v189\n"
  "v_mfma_f32_16x16x32_bf16 a[160:163], a[48:51], a[40:43], a[160:163]\n"
  "ds_read_b128 a[80:83], v37 offset:0\n"
  "v_mfma_f32_16x16x32_bf16 a[176:179], a[48:51], a[44:47], a[176:179]\n"
  "ds_read_b64_tr_b16 a[100:101], v39 offset:24576\n"
  "v_cvt_pk_bf16_f32 v94, v188, v189\n"
  "v_mfma_f32_16x16x32_bf16 a[132:135], a[52:55], a[32:35], a[132:135]\n"
  "ds_read_b64_tr_b16 a[102:103], v39 offset:25600\n"
  "v_mul_f32 v190, 0x3e0293ee, v190\n"
  "v_mfma_f32_16x16x32_bf16 a[148:151], a[52:55], a[36:39], a[148:151]\n"
  "ds_read_b128 a[84:87], v37 offset:2048\n"
  "v_mul_f32 v191, 0x3e0293ee, v191\n"
  "v_mfma_f32_16x16x32_bf16 a[164:167], a[52:55], a[40:43], a[164:167]\n"
  "ds_read_b64_tr_b16 a[104:105], v40 offset:24576\n"
  "v_cvt_pk_bf16_f32 v95, v190, v191\n"
  "v_mfma_f32_16x16x32_bf16 a[180:183], a[52:55], a[44:47], a[180:183]\n"
  "ds_read_b64_tr_b16 a[106:107], v40 offset:25600\n"
  "v_mul_f32 v192, 0x3e0293ee, v192\n"
  "v_mfma_f32_16x16x32_bf16 a[136:139], a[56:59], a[32:35], a[136:139]\n"
  "ds_read_b128 a[88:91], v37 offset:4096\n"
  "v_mfma_f32_16x16x32_bf16 a[152:155], a[56:59], a[36:39], a[152:155]\n"
  "ds_read_b64_tr_b16 a[108:109], v41 offset:24576\n"
  "v_mul_f32 v193, 0x3e0293ee, v193\n"
  "v_mfma_f32_16x16x32_bf16 a[168:171], a[56:59], a[40:43], a[168:171]\n"
  "ds_read_b64_tr_b16 a[110:111], v41 offset:25600\n"
  "v_cvt_pk_bf16_f32 v96, v192, v193\n"
  "v_mfma_f32_16x16x32_bf16 a[184:187], a[56:59], a[44:47], a[184:187]\n"
  "ds_read_b128 a[92:95], v37 offset:6144\n"
  "v_mul_f32 v194, 0x3e0293ee, v194\n"
  "v_mfma_f32_16x16x32_bf16 a[140:143], a[60:63], a[32:35], a[140:143]\n"
  "v_add_u32 v36, 0xfffe0000, v36\n"
  "v_mul_f32 v195, 0x3e0293ee, v195\n"
  "v_mfma_f32_16x16x32_bf16 a[156:159], a[60:63], a[36:39], a[156:159]\n"
  "v_add_u32 v37, 0xfffe0000, v37\n"
  "v_add_u32 v38, 0xfffe0000, v38\n"
  "v_mfma_f32_16x16x32_bf16 a[172:175], a[60:63], a[40:43], a[172:175]\n"
  "v_add_u32 v39, 0xfffe0000, v39\n"
  "v_cvt_pk_bf16_f32 v97, v194, v195\n"
  "v_mfma_f32_16x16x32_bf16 a[188:191], a[60:63], a[44:47], a[188:191]\n"
  "v_add_u32 v40, 0xfffe0000, v40\n"
  "v_add_u32 v41, 0xfffe0000, v41\n"
  "s_waitcnt vmcnt(25) lgkmcnt(0)\n"
  "s_barrier\n"
  "buffer_load_dword v62, v4, s[88:91], s69 offen offset:128 sc1\n"
  "v_mfma_f32_16x16x32_bf16 a[128:131], a[96:99], a[80:83], a[128:131]\n"
  "ds_read_b64_tr_b16 a[48:49], v38 offset:16384\n"
  "s_add_u32 m0, s62, 131072\n"
  "s_mov_b32 s64, 1152\n"
  "v_mul_f32 v196, 0x3e0293ee, v196\n"
  "v_mul_f32 v197, 0x3e0293ee, v197\n"
  "v_mfma_f32_16x16x32_bf16 a[144:147], a[96:99], a[84:87], a[144:147]\n"
  "ds_read_b64_tr_b16 a[50:51], v38 offset:17408\n"
  "buffer_load_dwordx4 v42, s[48:51], s64 offen offset:0 lds\n"
  "v_cvt_pk_bf16_f32 v98, v196, v197\n"
  "v_mfma_f32_16x16x32_bf16 a[160:163], a[96:99], a[88:91], a[160:163]\n"
  "ds_read_b128 a[32:35], v36 offset:0\n"
  "v_mul_f32 v198, 0x3e0293ee, v198\n"
  "v_mfma_f32_16x16x32_bf16 a[176:179], a[96:99], a[92:95], a[176:179]\n"
  "ds_read_b64_tr_b16 a[52:53], v39 offset:16384\n"
  "buffer_load_dwordx4 v43, s[48:51], s64 offen offset:1024 lds\n"
  "v_mul_f32 v199, 0x3e0293ee, v199\n"
  "v_mfma_f32_16x16x32_bf16 a[132:135], a[100:103], a[80:83], a[132:135]\n"
  "ds_read_b64_tr_b16 a[54:55], v39 offset:17408\n"
  "v_mfma_f32_16x16x32_bf16 a[148:151], a[100:103], a[84:87], a[148:151]\n"
  "ds_read_b128 a[36:39], v36 offset:2048\n"
  "buffer_load_dwordx4 v44, s[48:51], s64 offen offset:2048 lds\n"
  "v_cvt_pk_bf16_f32 v99, v198, v199\n"
  "v_mfma_f32_16x16x32_bf16 a[164:167], a[100:103], a[88:91], a[164:167]\n"
  "ds_read_b64_tr_b16 a[56:57], v40 offset:16384\n"
  "v_mul_f32 v200, 0x3e0293ee, v200\n"
  "v_mfma_f32_16x16x32_bf16 a[180:183], a[100:103], a[92:95], a[180:183]\n"
  "ds_read_b64_tr_b16 a[58:59], v40 offset:17408\n"
  "buffer_load_dwordx4 v45, s[48:51], s64 offen offset:3072 lds\n"
  "v_mul_f32 v201, 0x3e0293ee, v201\n"
  "v_mfma_f32_16x16x32_bf16 a[136:139], a[104:107], a[80:83], a[136:139]\n"
  "ds_read_b128 a[40:43], v36 offset:4096\n"
  "s_add_u32 m0, s62, 147456\n"
  "s_mov_b32 s64, 2211840\n"
  "v_cvt_pk_bf16_f32 v100, v200, v201\n"
  "v_mfma_f32_16x16x32_bf16 a[152:155], a[104:107], a[84:87], a[152:155]\n"
  "ds_read_b64_tr_b16 a[60:61], v41 offset:16384\n"
  "buffer_load_dwordx4 v48, s[84:87], s64 offen offset:0 lds\n"
  "v_mul_f32 v202, 0x3e0293ee, v202\n"
  "v_mfma_f32_16x16x32_bf16 a[168:171], a[104:107], a[88:91], a[168:171]\n"
  "ds_read_b64_tr_b16 a[62:63], v41 offset:17408\n"
  "v_mfma_f32_16x16x32_bf16 a[184:187], a[104:107], a[92:95], a[184:187]\n"
  "ds_read_b128 a[44:47], v36 offset:6144\n"
  "buffer_load_dwordx4 v49, s[84:87], s64 offen offset:1024 lds\n"
  "v_mul_f32 v203, 0x3e0293ee, v203\n"
  "v_mfma_f32_16x16x32_bf16 a[140:143], a[108:111], a[80:83], a[140:143]\n"
  "v_cvt_pk_bf16_f32 v101, v202, v203\n"
  "v_mfma_f32_16x16x32_bf16 a[156:159], a[108:111], a[84:87], a[156:159]\n"
  "buffer_load_dwordx4 v50, s[84:87], s64 offen offset:2048 lds\n"
  "v_mul_f32 v204, 0x3e0293ee, v204\n"
  "v_mfma_f32_16x16x32_bf16 a[172:175], a[108:111], a[88:91], a[172:175]\n"
  "v_mul_f32 v205, 0x3e0293ee, v205\n"
  "v_mfma_f32_16x16x32_bf16 a[188:191], a[108:111], a[92:95], a[188:191]\n"
  "buffer_load_dwordx4 v51, s[84:87], s64 offen offset:3072 lds\n"
  "v_cvt_pk_bf16_f32 v102, v204, v205\n"
  "s_waitcnt lgkmcnt(0)\n"
  "v_mfma_f32_16x16x32_bf16 a[128:131], a[48:51], a[32:35], a[128:131]\n"
  "ds_read_b64_tr_b16 a[96:97], v38 offset:24576\n"
  "v_mul_f32 v206, 0x3e0293ee, v206\n"
  "v_mfma_f32_16x16x32_bf16 a[144:147], a[48:51], a[36:39], a[144:147]\n"
  "ds_read_b64_tr_b16 a[98:99], v38 offset:25600\n"
  "v_mfma_f32_16x16x32_bf16 a[160:163], a[48:51], a[40:43], a[160:163]\n"
  "ds_read_b128 a[80:83], v37 offset:0\n"
  "v_mul_f32 v207, 0x3e0293ee, v207\n"
  "v_mfma_f32_16x16x32_bf16 a[176:179], a[48:51], a[44:47], a[176:179]\n"
  "ds_read_b64_tr_b16 a[100:101], v39 offset:24576\n"
  "v_cvt_pk_bf16_f32 v103, v206, v207\n"
  "v_mfma_f32_16x16x32_bf16 a[132:135], a[52:55], a[32:35], a[132:135]\n"
  "ds_read_b64_tr_b16 a[102:103], v39 offset:25600\n"
  "v_mul_f32 v208, 0x3e0293ee, v208\n"
  "v_mfma_f32_16x16x32_bf16 a[148:151], a[52:55], a[36:39], a[148:151]\n"
  "ds_read_b128 a[84:87], v37 offset:2048\n"
  "v_mul_f32 v209, 0x3e0293ee, v209\n"
  "v_mfma_f32_16x16x32_bf16 a[164:167], a[52:55], a[40:43], a[164:167]\n"
  "ds_read_b64_tr_b16 a[104:105], v40 offset:24576\n"
  "v_cvt_pk_bf16_f32 v104, v208, v209\n"
  "v_mfma_f32_16x16x32_bf16 a[180:183], a[52:55], a[44:47], a[180:183]\n"
  "ds_read_b64_tr_b16 a[106:107], v40 offset:25600\n"
  "v_mfma_f32_16x16x32_bf16 a[136:139], a[56:59], a[32:35], a[136:139]\n"
  "ds_read_b128 a[88:91], v37 offset:4096\n"
  "v_mul_f32 v210, 0x3e0293ee, v210\n"
  "v_mfma_f32_16x16x32_bf16 a[152:155], a[56:59], a[36:39], a[152:155]\n"
  "ds_read_b64_tr_b16 a[108:109], v41 offset:24576\n"
  "v_mul_f32 v211, 0x3e0293ee, v211\n"
  "v_mfma_f32_16x16x32_bf16 a[168:171], a[56:59], a[40:43], a[168:171]\n"
  "ds_read_b64_tr_b16 a[110:111], v41 offset:25600\n"
  "v_cvt_pk_bf16_f32 v105, v210, v211\n"
  "v_mfma_f32_16x16x32_bf16 a[184:187], a[56:59], a[44:47], a[184:187]\n"
  "ds_read_b128 a[92:95], v37 offset:6144\n"
  "v_mul_f32 v212, 0x3e0293ee, v212\n"
  "v_mfma_f32_16x16x32_bf16 a[140:143], a[60:63], a[32:35], a[140:143]\n"
  "v_add_u32 v36, 0x8000, v36\n"
  "v_mul_f32 v213, 0x3e0293ee, v213\n"
  "v_mfma_f32_16x16x32_bf16 a[156:159], a[60:63], a[36:39], a[156:159]\n"
  "v_add_u32 v37, 0x8000, v37\n"
  "v_add_u32 v38, 0x8000, v38\n"
  "v_mfma_f32_16x16x32_bf16 a[172:175], a[60:63], a[40:43], a[172:175]\n"
  "v_add_u32 v39, 0x8000, v39\n"
  "v_cvt_pk_bf16_f32 v106, v212, v213\n"
  "v_mfma_f32_16x16x32_bf16 a[188:191], a[60:63], a[44:47], a[188:191]\n"
  "v_add_u32 v40, 0x8000, v40\n"
  "v_add_u32 v41, 0x8000, v41\n"
  "s_waitcnt vmcnt(8) lgkmcnt(0)\n"
  "s_barrier\n"
  "v_cmp_ne_u32 vcc, v62, v5\n"
  "s_cmp_eq_u64 vcc, 0\n"
  "s_cbranch_scc0 p_spin%=\n"
  "p_ok%=:\n"
  "v_mfma_f32_16x16x32_bf16 a[128:131], a[96:99], a[80:83], a[128:131]\n"
  "ds_read_b64_tr_b16 a[48:49], v38 offset:16384\n"
  "s_add_u32 m0, s60, 0\n"
  "s_add_u32 s64, s57, 0\n"
  "v_mul_f32 v214, 0x3e0293ee, v214\n"
  "v_mul_f32 v215, 0x3e0293ee, v215\n"
  "v_mfma_f32_16x16x32_bf16 a[144:147], a[96:99], a[84:87], a[144:147]\n"
  "ds_read_b64_tr_b16 a[50:51], v38 offset:17408\n"
  "buffer_load_dwordx4 v59, s[88:91], s64 offen offset:0 sc1 lds\n"
  "v_cvt_pk_bf16_f32 v107, v214, v215\n"
  "v_mfma_f32_16x16x32_bf16 a[160:163], a[96:99], a[88:91], a[160:163]\n"
  "ds_read_b128 a[32:35], v36 offset:0\n"
  "v_mul_f32 v216, 0x3e0293ee, v216\n"
  "v_mfma_f32_16x16x32_bf16 a[176:179], a[96:99], a[92:95], a[176:179]\n"
  "ds_read_b64_tr_b16 a[52:53], v39 offset:16384\n"
  "buffer_load_dwordx4 v59, s[88:91], s64 offen offset:1024 sc1 lds\n"
  "v_mfma_f32_16x16x32_bf16 a[132:135], a[100:103], a[80:83], a[132:135]\n"
  "ds_read_b64_tr_b16 a[54:55], v39 offset:17408\n"
  "v_mul_f32 v217, 0x3e0293ee, v217\n"
  "v_mfma_f32_16x16x32_bf16 a[148:151], a[100:103], a[84:87], a[148:151]\n"
  "ds_read_b128 a[36:39], v36 offset:2048\n"
  "buffer_load_dwordx4 v59, s[88:91], s64 offen offset:2048 sc1 lds\n"
  "v_cvt_pk_bf16_f32 v108, v216, v217\n"
  "v_mfma_f32_16x16x32_bf16 a[164:167], a[100:103], a[88:91], a[164:167]\n"
  "ds_read_b64_tr_b16 a[56:57], v40 offset:16384\n"
  "v_mul_f32 v218, 0x3e0293ee, v218\n"
  "v_mfma_f32_16x16x32_bf16 a[180:183], a[100:103], a[92:95], a[180:183]\n"
  "ds_read_b64_tr_b16 a[58:59], v40 offset:17408\n"
  "buffer_load_dwordx4 v59, s[88:91], s64 offen offset:3072 sc1 lds\n"
  "v_mul_f32 v219, 0x3e0293ee, v219\n"
  "v_mfma_f32_16x16x32_bf16 a[136:139], a[104:107], a[80:83], a[136:139]\n"
  "ds_read_b128 a[40:43], v36 offset:4096\n"
  "s_add_u32 m0, s60, 4096\n"
  "s_add_u32 s64, s57, 0\n"
  "v_cvt_pk_bf16_f32 v109, v218, v219\n"
  "v_mfma_f32_16x16x32_bf16 a[152:155], a[104:107], a[84:87], a[152:155]\n"
  "ds_read_b64_tr_b16 a[60:61], v41 offset:16384\n"
  "buffer_load_dwordx4 v60, s[88:91], s64 offen offset:0 sc1 lds\n"
  "v_mfma_f32_16x16x32_bf16 a[168:171], a[104:107], a[88:91], a[168:171]\n"
  "ds_read_b64_tr_b16 a[62:63], v41 offset:17408\n"
  "v_mul_f32 v220, 0x3e0293ee, v220\n"
  "v_mfma_f32_16x16x32_bf16 a[184:187], a[104:107], a[92:95], a[184:187]\n"
  "ds_read_b128 a[44:47], v36 offset:6144\n"
  "buffer_load_dwordx4 v60, s[88:91], s64 offen offset:1024 sc1 lds\n"
  "v_mul_f32 v221, 0x3e0293ee, v221\n"
  "v_mfma_f32_16x16x32_bf16 a[140:143], a[108:111], a[80:83], a[140:143]\n"
  "v_cvt_pk_bf16_f32 v110, v220, v221\n"
  "v_mfma_f32_16x16x32_bf16 a[156:159], a[108:111], a[84:87], a[156:159]\n"
  "buffer_load_dwordx4 v60, s[88:91], s64 offen offset:2048 sc1 lds\n"
  "v_mul_f32 v222, 0x3e0293ee, v222\n"
  "v_mfma_f32_16x16x32_bf16 a[172:175], a[108:111], a[88:91], a[172:175]\n"
  "v_mul_f32 v223, 0x3e0293ee, v223\n"
  "v_mfma_f32_16x16x32_bf16 a[188:191], a[108:111], a[92:95], a[188:191]\n"
  "buffer_load_dwordx4 v60, s[88:91], s64 offen offset:3072 sc1 lds\n"
  "s_waitcnt lgkmcnt(0)\n"
  "v_mfma_f32_16x16x32_bf16 a[128:131], a[48:51], a[32:35], a[128:131]\n"
  "ds_read_b64_tr_b16 a[96:97], v38 offset:24576\n"
  "v_cvt_pk_bf16_f32 v111, v222, v223\n"
  "v_mfma_f32_16x16x32_bf16 a[144:147], a[48:51], a[36:39], a[144:147]\n"
  "ds_read_b64_tr_b16 a[98:99], v38 offset:25600\n"
  "v_mul_f32 v224, 0x3e0293ee, v224\n"
  "v_mfma_f32_16x16x32_bf16 a[160:163], a[48:51], a[40:43], a[160:163]\n"
  "ds_read_b128 a[80:83], v37 offset:0\n"
  "v_mul_f32 v225, 0x3e0293ee, v225\n"
  "v_mfma_f32_16x16x32_bf16 a[176:179], a[48:51], a[44:47], a[176:179]\n"
  "ds_read_b64_tr_b16 a[100:101], v39 offset:24576\n"
  "v_cvt_pk_bf16_f32 v112, v224, v225\n"
  "v_mfma_f32_16x16x32_bf16 a[132:135], a[52:55], a[32:35], a[132:135]\n"
  "ds_read_b64_tr_b16 a[102:103], v39 offset:25600\n"
  "v_mul_f32 v226, 0x3e0293ee, v226\n"
  "v_mfma_f32_16x16x32_bf16 a[148:151], a[52:55], a[36:39], a[148:151]\n"
  "ds_read_b128 a[84:87], v37 offset:2048\n"
  "v_mfma_f32_16x16x32_bf16 a[164:167], a[52:55], a[40:43], a[164:167]\n"
  "ds_read_b64_tr_b16 a[104:105], v40 offset:24576\n"
  "v_mul_f32 v227, 0x3e0293ee, v227\n"
  "v_mfma_f32_16x16x32_bf16 a[180:183], a[52:55], a[44:47], a[180:183]\n"
  "ds_read_b64_tr_b16 a[106:107], v40 offset:25600\n"
  "v_cvt_pk_bf16_f32 v113, v226, v227\n"
  "v_mfma_f32_16x16x32_bf16 a[136:139], a[56:59], a[32:35], a[136:139]\n"
  "ds_read_b128 a[88:91], v37 offset:4096\n"
  "v_mul_f32 v228, 0x3e0293ee, v228\n"
  "v_mfma_f32_16x16x32_bf16 a[152:155], a[56:59], a[36:39], a[152:155]\n"
  "ds_read_b64_tr_b16 a[108:109], v41 offset:24576\n"
  "v_mul_f32 v229, 0x3e0293ee, v229\n"
  "v_mfma_f32_16x16x32_bf16 a[168:171], a[56:59], a[40:43], a[168:171]\n"
  "ds_read_b64_tr_b16 a[110:111], v41 offset:25600\n"
  "v_cvt_pk_bf16_f32 v114, v228, v229\n"
  "v_mfma_f32_16x16x32_bf16 a[184:187], a[56:59], a[44:47], a[184:187]\n"
  "ds_read_b128 a[92:95], v37 offset:6144\n"
  "v_mfma_f32_16x16x32_bf16 a[140:143], a[60:63], a[32:35], a[140:143]\n"
  "v_add_u32 v36, 0x8000, v36\n"
  "v_mul_f32 v230, 0x3e0293ee, v230\n"
  "v_mfma_f32_16x16x32_bf16 a[156:159], a[60:63], a[36:39], a[156:159]\n"
  "v_add_u32 v37, 0x8000, v37\n"
  "v_add_u32 v38, 0x8000, v38\n"
  "v_mfma_f32_16x16x32_bf16 a[172:175], a[60:63], a[40:43], a[172:175]\n"
  "v_add_u32 v39, 0x8000, v39\n"
  "v_mul_f32 v231, 0x3e0293ee, v231\n"
  "v_mfma_f32_16x16x32_bf16 a[188:191], a[60:63], a[44:47], a[188:191]\n"
  "v_add_u32 v40, 0x8000, v40\n"
  "v_add_u32 v41, 0x8000, v41\n"
  "s_waitcnt vmcnt(25) lgkmcnt(0)\n"
  "s_barrier\n"
  "v_mfma_f32_16x16x32_bf16 a[128:131], a[96:99], a[80:83], a[128:131]\n"
  "ds_read_b64_tr_b16 a[48:49], v38 offset:16384\n"
  "s_add_u32 m0, s60, 32768\n"
  "s_add_u32 s64, s57, 32768\n"
  "v_cvt_pk_bf16_f32 v115, v230, v231\n"
  "v_mul_f32 v232, 0x3e0293ee, v232\n"
  "v_mfma_f32_16x16x32_bf16 a[144:147], a[96:99], a[84:87], a[144:147]\n"
  "ds_read_b64_tr_b16 a[50:51], v38 offset:17408\n"
  "buffer_load_dwordx4 v59, s[88:91], s64 offen offset:0 sc1 lds\n"
  "v_mul_f32 v233, 0x3e0293ee, v233\n"
  "v_mfma_f32_16x16x32_bf16 a[160:163], a[96:99], a[88:91], a[160:163]\n"
  "ds_read_b128 a[32:35], v36 offset:0\n"
  "v_cvt_pk_bf16_f32 v116, v232, v233\n"
  "v_mfma_f32_16x16x32_bf16 a[176:179], a[96:99], a[92:95], a[176:179]\n"
  "ds_read_b64_tr_b16 a[52:53], v39 offset:16384\n"
  "buffer_load_dwordx4 v59, s[88:91], s64 offen offset:1024 sc1 lds\n"
  "v_mul_f32 v234, 0x3e0293ee, v234\n"
  "v_mfma_f32_16x16x32_bf16 a[132:135], a[100:103], a[80:83], a[132:135]\n"
  "ds_read_b64_tr_b16 a[54:55], v39 offset:17408\n"
  "v_mul_f32 v235, 0x3e0293ee, v235\n"
  "v_mfma_f32_16x16x32_bf16 a[148:151], a[100:103], a[84:87], a[148:151]\n"
  "ds_read_b128 a[36:39], v36 offset:2048\n"
  "buffer_load_dwordx4 v59, s[88:91], s64 offen offset:2048 sc1 lds\n"
  "v_cvt_pk_bf16_f32 v117, v234, v235\n"
  "v_mfma_f32_16x16x32_bf16 a[164:167], a[100:103], a[88:91], a[164:167]\n"
  "ds_read_b64_tr_b16 a[56:57], v40 offset:16384\n"
  "v_mul_f32 v236, 0x3e0293ee, v236\n"
  "v_mfma_f32_16x16x32_bf16 a[180:183], a[100:103], a[92:95], a[180:183]\n"
  "ds_read_b64_tr_b16 a[58:59], v40 offset:17408\n"
  "buffer_load_dwordx4 v59, s[88:91], s64 offen offset:3072 sc1 lds\n"
  "v_mfma_f32_16x16x32_bf16 a[136:139], a[104:107], a[80:83], a[136:139]\n"
  "ds_read_b128 a[40:43], v36 offset:4096\n"
  "s_add_u32 m0, s60, 36864\n"
  "s_add_u32 s64, s57, 32768\n"
  "v_mul_f32 v237, 0x3e0293ee, v237\n"
  "v_mfma_f32_16x16x32_bf16 a[152:155], a[104:107], a[84:87], a[152:155]\n"
  "ds_read_b64_tr_b16 a[60:61], v41 offset:16384\n"
  "buffer_load_dwordx4 v60, s[88:91], s64 offen offset:0 sc1 lds\n"
  "v_cvt_pk_bf16_f32 v118, v236, v237\n"
  "v_mfma_f32_16x16x32_bf16 a[168:171], a[104:107], a[88:91], a[168:171]\n"
  "ds_read_b64_tr_b16 a[62:63], v41 offset:17408\n"
  "v_mul_f32 v238, 0x3e0293ee, v238\n"
  "v_mfma_f32_16x16x32_bf16 a[184:187], a[104:107], a[92:95], a[184:187]\n"
  "ds_read_b128 a[44:47], v36 offset:6144\n"
  "buffer_load_dwordx4 v60, s[88:91], s64 offen offset:1024 sc1 lds\n"
  "v_mul_f32 v239, 0x3e0293ee, v239\n"
  "v_mfma_f32_16x16x32_bf16 a[140:143], a[108:111], a[80:83], a[140:143]\n"
  "v_cvt_pk_bf16_f32 v119, v238, v239\n"
  "v_mfma_f32_16x16x32_bf16 a[156:159], a[108:111], a[84:87], a[156:159]\n"
  "buffer_load_dwordx4 v60, s[88:91], s64 offen offset:2048 sc1 lds\n"
  "v_mfma_f32_16x16x32_bf16 a[172:175], a[108:111], a[88:91], a[172:175]\n"
  "v_mul_f32 v240, 0x3e0293ee, v240\n"
  "v_mfma_f32_16x16x32_bf16 a[188:191], a[108:111], a[92:95], a[188:191]\n"
  "buffer_load_dwordx4 v60, s[88:91], s64 offen offset:3072 sc1 lds\n"
  "v_mul_f32 v241, 0x3e0293ee, v241\n"
  "s_waitcnt lgkmcnt(0)\n"
  "v_mfma_f32_16x16x32_bf16 a[128:131], a[48:51], a[32:35], a[128:131]\n"
  "ds_read_b64_tr_b16 a[96:97], v38 offset:24576\n"
  "v_cvt_pk_bf16_f32 v120, v240, v241\n"
  "v_mfma_f32_16x16x32_bf16 a[144:147], a[48:51], a[36:39], a[144:147]\n"
  "ds_read_b64_tr_b16 a[98:99], v38 offset:25600\n"
  "v_mul_f32 v242, 0x3e0293ee, v242\n"
  "v_mfma_f32_16x16x32_bf16 a[160:163], a[48:51], a[40:43], a[160:163]\n"
  "ds_read_b128 a[80:83], v37 offset:0\n"
  "v_mul_f32 v243, 0x3e0293ee, v243\n"
  "v_mfma_f32_16x16x32_bf16 a[176:179], a[48:51], a[44:47], a[176:179]\n"
  "ds_read_b64_tr_b16 a[100:101], v39 offset:24576\n"
  "v_mfma_f32_16x16x32_bf16 a[132:135], a[52:55], a[32:35], a[132:135]\n"
  "ds_read_b64_tr_b16 a[102:103], v39 offset:25600\n"
  "v_cvt_pk_bf16_f32 v121, v242, v243\n"
  "v_mfma_f32_16x16x32_bf16 a[148:151], a[52:55], a[36:39], a[148:151]\n"
  "ds_read_b128 a[84:87], v37 offset:2048\n"
  "v_mul_f32 v244, 0x3e0293ee, v244\n"
  "v_mfma_f32_16x16x32_bf16 a[164:167], a[52:55], a[40:43], a[164:167]\n"
  "ds_read_b64_tr_b16 a[104:105], v40 offset:24576\n"
  "v_mul_f32 v245, 0x3e0293ee, v245\n"
  "v_mfma_f32_16x16x32_bf16 a[180:183], a[52:55], a[44:47], a[180:183]\n"
  "ds_read_b64_tr_b16 a[106:107], v40 offset:25600\n"
  "v_cvt_pk_bf16_f32 v122, v244, v245\n"
  "v_mfma_f32_16x16x32_bf16 a[136:139], a[56:59], a[32:35], a[136:139]\n"
  "ds_read_b128 a[88:91], v37 offset:4096\n"
  "v_mul_f32 v246, 0x3e0293ee, v246\n"
  "v_mfma_f32_16x16x32_bf16 a[152:155], a[56:59], a[36:39], a[152:155]\n"
  "ds_read_b64_tr_b16 a[108:109], v41 offset:24576\n"
  "v_mul_f32 v247, 0x3e0293ee, v247\n"
  "v_mfma_f32_16x16x32_bf16 a[168:171], a[56:59], a[40:43], a[168:171]\n"
  "ds_read_b64_tr_b16 a[110:111], v41 offset:25600\n"
  "v_mfma_f32_16x16x32_bf16 a[184:187], a[56:59], a[44:47], a[184:187]\n"
  "ds_read_b128 a[92:95], v37 offset:6144\n"
  "v_cvt_pk_bf16_f32 v123, v246, v247\n"
  "v_mfma_f32_16x16x32_bf16 a[140:143], a[60:63], a[32:35], a[140:143]\n"
  "v_add_u32 v36, 0x8000, v36\n"
  "v_mul_f32 v248, 0x3e0293ee, v248\n"
  "v_mfma_f32_16x16x32_bf16 a[156:159], a[60:63], a[36:39], a[156:159]\n"
  "v_add_u32 v37, 0x8000, v37\n"
  "v_add_u32 v38, 0x8000, v38\n"
  "v_mfma_f32_16x16x32_bf16 a[172:175], a[60:63], a[40:43], a[172:175]\n"
  "v_add_u32 v39, 0x8000, v39\n"
  "v_mul_f32 v249, 0x3e0293ee, v249\n"
  "v_mfma_f32_16x16x32_bf16 a[188:191], a[60:63], a[44:47], a[188:191]\n"
  "v_add_u32 v40, 0x8000, v40\n"
  "v_add_u32 v41, 0x8000, v41\n"
  "s_waitcnt vmcnt(25) lgkmcnt(0)\n"
  "s_barrier\n"
  "v_mfma_f32_16x16x32_bf16 a[128:131], a[96:99], a[80:83], a[128:131]\n"
  "ds_read_b64_tr_b16 a[48:49], v38 offset:16384\n"
  "s_add_u32 m0, s60, 65536\n"
  "s_add_u32 s64, s57, 65536\n"
  "v_cvt_pk_bf16_f32 v124, v248, v249\n"
  "v_mul_f32 v250, 0x3e0293ee, v250\n"
  "v_mfma_f32_16x16x32_bf16 a[144:147], a[96:99], a[84:87], a[144:147]\n"
  "ds_read_b64_tr_b16 a[50:51], v38 offset:17408\n"
  "buffer_load_dwordx4 v59, s[88:91], s64 offen offset:0 sc1 lds\n"
  "v_mul_f32 v251, 0x3e0293ee, v251\n"
  "v_mfma_f32_16x16x32_bf16 a[160:163], a[96:99], a[88:91], a[160:163]\n"
  "ds_read_b128 a[32:35], v36 offset:0\n"
  "v_cvt_pk_bf16_f32 v125, v250, v251\n"
  "v_mfma_f32_16x16x32_bf16 a[176:179], a[96:99], a[92:95], a[176:179]\n"
  "ds_read_b64_tr_b16 a[52:53], v39 offset:16384\n"
  "buffer_load_dwordx4 v59, s[88:91], s64 offen offset:1024 sc1 lds\n"
  "v_mul_f32 v252, 0x3e0293ee, v252\n"
  "v_mfma_f32_16x16x32_bf16 a[132:135], a[100:103], a[80:83], a[132:135]\n"
  "ds_read_b64_tr_b16 a[54:55], v39 offset:17408\n"
  "v_mul_f32 v253, 0x3e0293ee, v253\n"
  "v_mfma_f32_16x16x32_bf16 a[148:151], a[100:103], a[84:87], a[148:151]\n"
  "ds_read_b128 a[36:39], v36 offset:2048\n"
  "buffer_load_dwordx4 v59, s[88:91], s64 offen offset:2048 sc1 lds\n"
  "v_cvt_pk_bf16_f32 v126, v252, v253\n"
  "v_mfma_f32_16x16x32_bf16 a[164:167], a[100:103], a[88:91], a[164:167]\n"
  "ds_read_b64_tr_b16 a[56:57], v40 offset:16384\n"
  "v_mfma_f32_16x16x32_bf16 a[180:183], a[100:103], a[92:95], a[180:183]\n"
  "ds_read_b64_tr_b16 a[58:59], v40 offset:17408\n"
  "buffer_load_dwordx4 v59, s[88:91], s64 offen offset:3072 sc1 lds\n"
  "v_mul_f32 v254, 0x3e0293ee, v254\n"
  "v_mfma_f32_16x16x32_bf16 a[136:139], a[104:107], a[80:83], a[136:139]\n"
  "ds_read_b128 a[40:43], v36 offset:4096\n"
  "s_add_u32 m0, s60, 69632\n"
  "s_add_u32 s64, s57, 65536\n"
  "v_mul_f32 v255, 0x3e0293ee, v255\n"
  "v_mfma_f32_16x16x32_bf16 a[152:155], a[104:107], a[84:87], a[152:155]\n"
  "ds_read_b64_tr_b16 a[60:61], v41 offset:16384\n"
  "buffer_load_dwordx4 v60, s[88:91], s64 offen offset:0 sc1 lds\n"
  "v_cvt_pk_bf16_f32 v127, v254, v255\n"
  "v_mfma_f32_16x16x32_bf16 a[168:171], a[104:107], a[88:91], a[168:171]\n"
  "ds_read_b64_tr_b16 a[62:63], v41 offset:17408\n"
  "v_mov_b32 v32, 0\n"
  "v_mfma_f32_16x16x32_bf16 a[184:187], a[104:107], a[92:95], a[184:187]\n"
  "ds_read_b128 a[44:47], v36 offset:6144\n"
  "buffer_load_dwordx4 v60, s[88:91], s64 offen offset:1024 sc1 lds\n"
  "v_mov_b32 v33, 0\n"
  "v_mfma_f32_16x16x32_bf16 a[140:143], a[108:111], a[80:83], a[140:143]\n"
  "v_mfma_f32_16x16x32_bf16 a[156:159], a[108:111], a[84:87], a[156:159]\n"
  "buffer_load_dwordx4 v60, s[88:91], s64 offen offset:2048 sc1 lds\n"
  "v_mov_b32 v34, 0\n"
  "v_mfma_f32_16x16x32_bf16 a[172:175], a[108:111], a[88:91], a[172:175]\n"
  "v_mov_b32 v35, 0\n"
  "v_mfma_f32_16x16x32_bf16 a[188:191], a[108:111], a[92:95], a[188:191]\n"
  "buffer_load_dwordx4 v60, s[88:91], s64 offen offset:3072 sc1 lds\n"
  "v_cvt_f32_u32 v56, s79\n"
  "s_waitcnt lgkmcnt(0)\n"
  "v_mfma_f32_16x16x32_bf16 a[128:131], a[48:51], a[32:35], a[128:131]\n"
  "ds_read_b64_tr_b16 a[96:97], v38 offset:24576\n"
  "v_fmamk_f32 v56, v56, 0x75000000, v63\n"
  "v_mfma_f32_16x16x32_bf16 a[144:147], a[48:51], a[36:39], a[144:147]\n"
  "ds_read_b64_tr_b16 a[98:99], v38 offset:25600\n"
  "v_add_f32 v192, 0x0, v56\n"
  "v_mfma_f32_16x16x32_bf16 a[160:163], a[48:51], a[40:43], a[160:163]\n"
  "ds_read_b128 a[80:83], v37 offset:0\n"
  "v_mfma_f32_16x16x32_bf16 a[176:179], a[48:51], a[44:47], a[176:179]\n"
  "ds_read_b64_tr_b16 a[100:101], v39 offset:24576\n"
  "v_min_f32 v192, 0, v192\n"
  "v_mfma_f32_16x16x32_bf16 a[132:135], a[52:55], a[32:35], a[132:135]\n"
  "ds_read_b64_tr_b16 a[102:103], v39 offset:25600\n"
  "v_add_f32 v193, 0xf1800000, v56\n"
  "v_mfma_f32_16x16x32_bf16 a[148:151], a[52:55], a[36:39], a[148:151]\n"
  "ds_read_b128 a[84:87], v37 offset:2048\n"
  "v_min_f32 v193, 0, v193\n"
  "v_mfma_f32_16x16x32_bf16 a[164:167], a[52:55], a[40:43], a[164:167]\n"
  "ds_read_b64_tr_b16 a[104:105], v40 offset:24576\n"
  "v_add_f32 v194, 0xf2000000, v56\n"
  "v_mfma_f32_16x16x32_bf16 a[180:183], a[52:55], a[44:47], a[180:183]\n"
  "ds_read_b64_tr_b16 a[106:107], v40 offset:25600\n"
  "v_min_f32 v194, 0, v194\n"
  "v_mfma_f32_16x16x32_bf16 a[136:139], a[56:59], a[32:35], a[136:139]\n"
  "ds_read_b128 a[88:91], v37 offset:4096\n"
  "v_mfma_f32_16x16x32_bf16 a[152:155], a[56:59], a[36:39], a[152:155]\n"
  "ds_read_b64_tr_b16 a[108:109], v41 offset:24576\n"
  "v_add_f32 v195, 0xf2400000, v56\n"
  "v_mfma_f32_16x16x32_bf16 a[168:171], a[56:59], a[40:43], a[168:171]\n"
  "ds_read_b64_tr_b16 a[110:111], v41 offset:25600\n"
  "v_min_f32 v195, 0, v195\n"
  "v_mfma_f32_16x16x32_bf16 a[184:187], a[56:59], a[44:47], a[184:187]\n"
  "ds_read_b128 a[92:95], v37 offset:6144\n"
  "v_add_f32 v196, 0xf3800000, v56\n"
  "v_mfma_f32_16x16x32_bf16 a[140:143], a[60:63], a[32:35], a[140:143]\n"
  "v_add_u32 v36, 0x8000, v36\n"
  "v_min_f32 v196, 0, v196\n"
  "v_mfma_f32_16x16x32_bf16 a[156:159], a[60:63], a[36:39], a[156:159]\n"
  "v_add_u32 v37, 0x8000, v37\n"
  "v_add_u32 v38, 0x8000, v38\n"
  "v_mfma_f32_16x16x32_bf16 a[172:175], a[60:63], a[40:43], a[172:175]\n"
  "v_add_u32 v39, 0x8000, v39\n"
  "v_add_f32 v197, 0xf3880000, v56\n"
  "v_mfma_f32_16x16x32_bf16 a[188:191], a[60:63], a[44:47], a[188:191]\n"
  "v_add_u32 v40, 0x8000, v40\n"
  "v_add_u32 v41, 0x8000, v41\n"
  "s_waitcnt vmcnt(24) lgkmcnt(0)\n"
  "s_barrier\n"
  "v_mfma_f32_16x16x32_bf16 a[128:131], a[96:99], a[80:83], a[128:131]\n"
  "ds_read_b64_tr_b16 a[48:49], v38 offset:16384\n"
  "v_min_f32 v197, 0, v197\n"
  "v_add_f32 v198, 0xf3900000, v56\n"
  "v_mfma_f32_16x16x32_bf16 a[144:147], a[96:99], a[84:87], a[144:147]\n"
  "ds_read_b64_tr_b16 a[50:51], v38 offset:17408\n"
  "v_min_f32 v198, 0, v198\n"
  "v_mfma_f32_16x16x32_bf16 a[160:163], a[96:99], a[88:91], a[160:163]\n"
  "ds_read_b128 a[32:35], v36 offset:0\n"
  "v_add_f32 v199, 0xf3980000, v56\n"
  "v_mfma_f32_16x16x32_bf16 a[176:179], a[96:99], a[92:95], a[176:179]\n"
  "ds_read_b64_tr_b16 a[52:53], v39 offset:16384\n"
  "v_min_f32 v199, 0, v199\n"
  "v_mfma_f32_16x16x32_bf16 a[132:135], a[100:103], a[80:83], a[132:135]\n"
  "ds_read_b64_tr_b16 a[54:55], v39 offset:17408\n"
  "v_mfma_f32_16x16x32_bf16 a[148:151], a[100:103], a[84:87], a[148:151]\n"
  "ds_read_b128 a[36:39], v36 offset:2048\n"
  "v_add_f32 v200, 0x73800000, v56\n"
  "v_mfma_f32_16x16x32_bf16 a[164:167], a[100:103], a[88:91], a[164:167]\n"
  "ds_read_b64_tr_b16 a[56:57], v40 offset:16384\n"
  "v_min_f32 v200, 0, v200\n"
  "v_mfma_f32_16x16x32_bf16 a[180:183], a[100:103], a[92:95], a[180:183]\n"
  "ds_read_b64_tr_b16 a[58:59], v40 offset:17408\n"
  "v_add_f32 v201, 0x73700000, v56\n"
  "v_mfma_f32_16x16x32_bf16 a[136:139], a[104:107], a[80:83], a[136:139]\n"
  "ds_read_b128 a[40:43], v36 offset:4096\n"
  "v_min_f32 v201, 0, v201\n"
  "v_mfma_f32_16x16x32_bf16 a[152:155], a[104:107], a[84:87], a[152:155]\n"
  "ds_read_b64_tr_b16 a[60:61], v41 offset:16384\n"
  "v_add_f32 v202, 0x73600000, v56\n"
  "v_mfma_f32_16x16x32_bf16 a[168:171], a[104:107], a[88:91], a[168:171]\n"
  "ds_read_b64_tr_b16 a[62:63], v41 offset:17408\n"
  "v_mfma_f32_16x16x32_bf16 a[184:187], a[104:107], a[92:95], a[184:187]\n"
  "ds_read_b128 a[44:47], v36 offset:6144\n"
  "v_min_f32 v202, 0, v202\n"
  "v_mfma_f32_16x16x32_bf16 a[140:143], a[108:111], a[80:83], a[140:143]\n"
  "v_add_f32 v203, 0x73500000, v56\n"
  "v_mfma_f32_16x16x32_bf16 a[156:159], a[108:111], a[84:87], a[156:159]\n"
  "v_min_f32 v203, 0, v203\n"
  "v_mfma_f32_16x16x32_bf16 a[172:175], a[108:111], a[88:91], a[172:175]\n"
  "v_add_f32 v204, 0x0, v56\n"
  "v_mfma_f32_16x16x32_bf16 a[188:191], a[108:111], a[92:95], a[188:191]\n"
  "v_min_f32 v204, 0, v204\n"
  "s_waitcnt lgkmcnt(0)\n"
  "v_mfma_f32_16x16x32_bf16 a[128:131], a[48:51], a[32:35], a[128:131]\n"
  "ds_read_b64_tr_b16 a[96:97], v38 offset:24576\n"
  "v_mfma_f32_16x16x32_bf16 a[144:147], a[48:51], a[36:39], a[144:147]\n"
  "ds_read_b64_tr_b16 a[98:99], v38 offset:25600\n"
  "v_add_f32 v205, 0xf1800000, v56\n"
  "v_mfma_f32_16x16x32_bf16 a[160:163], a[48:51], a[40:43], a[160:163]\n"
  "ds_read_b128 a[80:83], v37 offset:0\n"
  "v_min_f32 v205, 0, v205\n"
  "v_mfma_f32_16x16x32_bf16 a[176:179], a[48:51], a[44:47], a[176:179]\n"
  "ds_read_b64_tr_b16 a[100:101], v39 offset:24576\n"
  "v_add_f32 v206, 0xf2000000, v56\n"
  "v_mfma_f32_16x16x32_bf16 a[132:135], a[52:55], a[32:35], a[132:135]\n"
  "ds_read_b64_tr_b16 a[102:103], v39 offset:25600\n"
  "v_min_f32 v206, 0, v206\n"
  "v_mfma_f32_16x16x32_bf16 a[148:151], a[52:55], a[36:39], a[148:151]\n"
  "ds_read_b128 a[84:87], v37 offset:2048\n"
  "v_add_f32 v207, 0xf2400000, v56\n"
  "v_mfma_f32_16x16x32_bf16 a[164:167], a[52:55], a[40:43], a[164:167]\n"
  "ds_read_b64_tr_b16 a[104:105], v40 offset:24576\n"
  "v_mfma_f32_16x16x32_bf16 a[180:183], a[52:55], a[44:47], a[180:183]\n"
  "ds_read_b64_tr_b16 a[106:107], v40 offset:25600\n"
  "v_min_f32 v207, 0, v207\n"
  "v_mfma_f32_16x16x32_bf16 a[136:139], a[56:59], a[32:35], a[136:139]\n"
  "ds_read_b128 a[88:91], v37 offset:4096\n"
  "v_lshrrev_b32 v55, 4, v54\n"
  "v_mfma_f32_16x16x32_bf16 a[152:155], a[56:59], a[36:39], a[152:155]\n"
  "ds_read_b64_tr_b16 a[108:109], v41 offset:24576\n"
  "v_and_b32 v52, 15, v54\n"
  "v_mfma_f32_16x16x32_bf16 a[168:171], a[56:59], a[40:43], a[168:171]\n"
  "ds_read_b64_tr_b16 a[110:111], v41 offset:25600\n"
  "v_lshlrev_b32 v52, 4, v52\n"
  "v_mfma_f32_16x16x32_bf16 a[184:187], a[56:59], a[44:47], a[184:187]\n"
  "ds_read_b128 a[92:95], v37 offset:6144\n"
  "v_lshl_add_u32 v52, v55, 15, v52\n"
  "v_mfma_f32_16x16x32_bf16 a[140:143], a[60:63], a[32:35], a[140:143]\n"
  "v_mfma_f32_16x16x32_bf16 a[156:159], a[60:63], a[36:39], a[156:159]\n"
  "v_mfma_f32_16x16x32_bf16 a[172:175], a[60:63], a[40:43], a[172:175]\n"
  "v_mfma_f32_16x16x32_bf16 a[188:191], a[60:63], a[44:47], a[188:191]\n"
  "s_waitcnt vmcnt(16) lgkmcnt(0)\n"
  "s_barrier\n"
  "v_mfma_f32_16x16x32_bf16 a[128:131], a[96:99], a[80:83], a[128:131]\n"
  "ds_read_b128 a[0:3], v57 offset:0\n"
  "v_mfma_f32_16x16x32_bf16 a[144:147], a[96:99], a[84:87], a[144:147]\n"
  "ds_read_b128 a[4:7], v57 offset:1024\n"
  "v_mfma_f32_16x16x32_bf16 a[160:163], a[96:99], a[88:91], a[160:163]\n"
  "v_mfma_f32_16x16x32_bf16 a[176:179], a[96:99], a[92:95], a[176:179]\n"
  "ds_read_b128 a[8:11], v57 offset:2048\n"
  "v_mfma_f32_16x16x32_bf16 a[132:135], a[100:103], a[80:83], a[132:135]\n"
  "ds_read_b128 a[12:15], v57 offset:3072\n"
  "v_mfma_f32_16x16x32_bf16 a[148:151], a[100:103], a[84:87], a[148:151]\n"
  "v_mfma_f32_16x16x32_bf16 a[164:167], a[100:103], a[88:91], a[164:167]\n"
  "ds_read_b128 a[16:19], v57 offset:4096\n"
  "v_mfma_f32_16x16x32_bf16 a[180:183], a[100:103], a[92:95], a[180:183]\n"
  "ds_read_b128 a[20:23], v57 offset:5120\n"
  "v_mfma_f32_16x16x32_bf16 a[136:139], a[104:107], a[80:83], a[136:139]\n"
  "v_mfma_f32_16x16x32_bf16 a[152:155], a[104:107], a[84:87], a[152:155]\n"
  "ds_read_b128 a[24:27], v57 offset:6144\n"
  "v_mfma_f32_16x16x32_bf16 a[168:171], a[104:107], a[88:91], a[168:171]\n"
  "ds_read_b128 a[28:31], v57 offset:7168\n"
  "v_mfma_f32_16x16x32_bf16 a[184:187], a[104:107], a[92:95], a[184:187]\n"
  "v_mfma_f32_16x16x32_bf16 a[140:143], a[108:111], a[80:83], a[140:143]\n"
  "v_mfma_f32_16x16x32_bf16 a[156:159], a[108:111], a[84:87], a[156:159]\n"
  "v_mfma_f32_16x16x32_bf16 a[172:175], a[108:111], a[88:91], a[172:175]\n"
  "v_mfma_f32_16x16x32_bf16 a[188:191], a[108:111], a[92:95], a[188:191]\n"
  "s_lshl1_add_u32 s58, s71, s73\n"
  "s_lshl_b32 s58, s58, 18\n"
  "s_lshl1_add_u32 s59, s70, s72\n"
  "s_lshl_b32 s59, s59, 10\n"
  "s_add_u32 s58, s58, s59\n"
  "s_add_u32 s65, s58, 50331648\n"
  "s_cmp_eq_u32 s71, 15\n"
  "s_cselect_b32 s65, 2130706432, s65\n"
  "s_movk_i32 s97, 99\n"
  "s_lshl2_add_u32 s36, %2, s20\n"
  "s_lshr_b32 s37, s36, 3\n"
  "s_and_b32 s16, s36, 7\n"
  "s_mul_i32 s38, s37, 40960\n"
  "s_lshl_b32 s39, s16, 7\n"
  "s_add_u32 s38, s38, s39\n"
  "s_mov_b32 s44, s37\n"
  "s_mov_b32 s40, 62914560\n"
  "s_and_b32 s17, %2, 1\n"
  "s_add_u32 s17, s17, 8\n"
  "s_lshr_b32 s37, %2, 1\n"
  "s_cmp_eq_u32 s20, 1\n"
  "s_cselect_b32 s17, s52, s17\n"
  "s_cselect_b32 s37, s53, s37\n"
  "s_cmp_eq_u32 s20, 3\n"
  "s_cselect_b32 s37, 99, s37\n"
  "s_mul_i32 s39, s37, 40960\n"
  "s_lshl_b32 s36, s17, 7\n"
  "s_add_u32 s39, s39, s36\n"
  "s_mov_b32 s45, s37\n"
  "s_movk_i32 s42, 95\n"
  "s_mov_b32 s43, 69206016\n"
  "s_cmp_eq_u32 s20, 0\n"
  "s_cselect_b64 s[12:13], s[8:9], s[12:13]\n"
  "s_cselect_b32 s14, s14, 819200\n"
  "s_cselect_b32 s42, 635, s42\n"
  "s_cselect_b32 s43, 62914560, s43\n"
  "s_cselect_b32 s37, 0, s37\n"
  "s_cselect_b32 s46, 2, 0\n"
  "s_cmp_lt_u32 s37, 20\n"
  "s_cselect_b32 s41, s43, 2130706432\n"
  "s_add_u32 s43, s60, 0x20000\n"
  "s_and_b32 s58, %2, 0x3c\n"
  "s_mul_i32 s58, s58, 81920\n"
  "s_mul_i32 s59, s20, 73728\n"
  "s_add_u32 s58, s58, s59\n"
  "s_lshr_b32 s59, s53, 3\n"
  "s_mul_i32 s59, s59, 0\n"
  "s_add_u32 s58, s58, s59\n"
  "s_add_u32 s96, s58, 62914560\n"
  "s_waitcnt lgkmcnt(0)\n"
  "v_mfma_f32_16x16x32_bf16 v[192:195], a[0:3], v[64:67], v[192:195]\n"
  "s_mul_i32 s29, s20, 7680\n"
  "s_add_u32 s29, s29, 0x20000\n"
  "s_mul_i32 s30, s55, 5\n"
  "s_mul_i32 s31, s20, 5\n"
  "s_lshl2_add_u32 s31, s30, s31\n"
  "s_lshr_b32 s30, s30, 1\n"
  "s_mul_i32 s58, s72, 20\n"
  "s_add_u32 s30, s30, s58\n"
  "v_accvgpr_read_b32 v128, a128\n"
  "v_accvgpr_read_b32 v129, a129\n"
  "v_mfma_f32_16x16x32_bf16 v[196:199], a[4:7], v[64:67], v[196:199]\n"
  "v_accvgpr_read_b32 v130, a130\n"
  "v_accvgpr_read_b32 v131, a131\n"
  "v_accvgpr_read_b32 v132, a132\n"
  "v_accvgpr_read_b32 v133, a133\n"
  "v_accvgpr_read_b32 v134, a134\n"
  "v_accvgpr_read_b32 v135, a135\n"
  "v_cvt_pk_bf16_f32 v128, v128, v129\n"
  "v_cvt_pk_bf16_f32 v129, v130, v131\n"
  "v_cvt_pk_bf16_f32 v130, v132, v133\n"
  "v_cvt_pk_bf16_f32 v131, v134, v135\n"
  "buffer_store_dwordx4 v[128:131], v52, s[88:91], s65 offen offset:0 sc1\n"
  "v_mfma_f32_16x16x32_bf16 v[192:195], a[8:11], v[68:71], v[192:195]\n"
  "s_mul_i32 s58, s73, 6\n"
  "s_lshl2_add_u32 s30, s30, s58\n"
  "s_lshl_b32 s30, s30, 15\n"
  "s_add_u32 s30, s30, 52953088\n"
  "s_lshr_b32 s58, s31, 3\n"
  "s_and_b32 s59, s31, 3\n"
  "s_lshl2_add_u32 s58, s58, s59\n"
  "s_lshl_b32 s58, s58, 15\n"
  "v_accvgpr_read_b32 v136, a144\n"
  "v_accvgpr_read_b32 v137, a145\n"
  "v_mfma_f32_16x16x32_bf16 v[196:199], a[12:15], v[68:71], v[196:199]\n"
  "v_accvgpr_read_b32 v138, a146\n"
  "v_accvgpr_read_b32 v139, a147\n"
  "v_accvgpr_read_b32 v140, a148\n"
  "v_accvgpr_read_b32 v141, a149\n"
  "v_accvgpr_read_b32 v142, a150\n"
  "v_accvgpr_read_b32 v143, a151\n"
  "v_cvt_pk_bf16_f32 v136, v136, v137\n"
  "v_cvt_pk_bf16_f32 v137, v138, v139\n"
  "v_cvt_pk_bf16_f32 v138, v140, v141\n"
  "v_cvt_pk_bf16_f32 v139, v142, v143\n"
  "buffer_store_dwordx4 v[136:139], v52, s[88:91], s65 offen offset:256 sc1\n"
  "v_mfma_f32_16x16x32_bf16 v[192:195], a[16:19], v[72:75], v[192:195]\n"
  "s_bfe_u32 s59, s31, 0x10002\n"
  "s_lshl3_add_u32 s58, s59, s58\n"
  "s_add_u32 s24, s58, 50331648\n"
  "s_add_u32 s31, s31, 1\n"
  "s_lshr_b32 s58, s31, 3\n"
  "s_and_b32 s59, s31, 3\n"
  "s_lshl2_add_u32 s58, s58, s59\n"
  "s_lshl_b32 s58, s58, 15\n"
  "v_accvgpr_read_b32 v144, a160\n"
  "v_accvgpr_read_b32 v145, a161\n"
  "v_mfma_f32_16x16x32_bf16 v[196:199], a[20:23], v[72:75], v[196:199]\n"
  "v_accvgpr_read_b32 v146, a162\n"
  "v_accvgpr_read_b32 v147, a163\n"
  "v_accvgpr_read_b32 v148, a164\n"
  "v_accvgpr_read_b32 v149, a165\n"
  "v_accvgpr_read_b32 v150, a166\n"
  "v_accvgpr_read_b32 v151, a167\n"
  "v_cvt_pk_bf16_f32 v144, v144, v145\n"
  "v_cvt_pk_bf16_f32 v145, v146, v147\n"
  "v_cvt_pk_bf16_f32 v146, v148, v149\n"
  "v_cvt_pk_bf16_f32 v147, v150, v151\n"
  "buffer_store_dwordx4 v[144:147], v52, s[88:91], s65 offen offset:512 sc1\n"
  "v_mfma_f32_16x16x32_bf16 v[192:195], a[24:27], v[76:79], v[192:195]\n"
  "s_bfe_u32 s59, s31, 0x10002\n"
  "s_lshl3_add_u32 s58, s59, s58\n"
  "s_add_u32 s25, s58, 50331648\n"
  "s_add_u32 s31, s31, 1\n"
  "s_lshr_b32 s58, s31, 3\n"
  "s_and_b32 s59, s31, 3\n"
  "s_lshl2_add_u32 s58, s58, s59\n"
  "s_lshl_b32 s58, s58, 15\n"
  "v_accvgpr_read_b32 v152, a176\n"
  "v_accvgpr_read_b32 v153, a177\n"
  "v_accvgpr_read_b32 v154, a178\n"
  "v_mfma_f32_16x16x32_bf16 v[196:199], a[28:31], v[76:79], v[196:199]\n"
  "v_accvgpr_read_b32 v155, a179\n"
  "v_accvgpr_read_b32 v156, a180\n"
  "v_accvgpr_read_b32 v157, a181\n"
  "v_accvgpr_read_b32 v158, a182\n"
  "v_accvgpr_read_b32 v159, a183\n"
  "v_cvt_pk_bf16_f32 v152, v152, v153\n"
  "v_cvt_pk_bf16_f32 v153, v154, v155\n"
  "v_cvt_pk_bf16_f32 v154, v156, v157\n"
  "v_cvt_pk_bf16_f32 v155, v158, v159\n"
  "buffer_store_dwordx4 v[152:155], v52, s[88:91], s65 offen offset:768 sc1\n"
  "s_add_u32 s65, s65, 0x20000\n"
  "v_mfma_f32_16x16x32_bf16 v[200:203], a[0:3], v[80:83], v[200:203]\n"
  "ds_read_b128 a[0:3], v57 offset:8192\n"
  "s_bfe_u32 s59, s31, 0x10002\n"
  "s_lshl3_add_u32 s58, s59, s58\n"
  "s_add_u32 s26, s58, 50331648\n"
  "s_add_u32 s31, s31, 1\n"
  "s_lshr_b32 s58, s31, 3\n"
  "s_and_b32 s59, s31, 3\n"
  "s_lshl2_add_u32 s58, s58, s59\n"
  "s_lshl_b32 s58, s58, 15\n"
  "v_accvgpr_read_b32 v160, a136\n"
  "v_accvgpr_read_b32 v161, a137\n"
  "v_mfma_f32_16x16x32_bf16 v[204:207], a[4:7], v[80:83], v[204:207]\n"
  "ds_read_b128 a[4:7], v57 offset:9216\n"
  "v_accvgpr_read_b32 v162, a138\n"
  "v_accvgpr_read_b32 v163, a139\n"
  "v_accvgpr_read_b32 v164, a140\n"
  "v_accvgpr_read_b32 v165, a141\n"
  "v_accvgpr_read_b32 v166, a142\n"
  "v_accvgpr_read_b32 v167, a143\n"
  "v_cvt_pk_bf16_f32 v160, v160, v161\n"
  "v_cvt_pk_bf16_f32 v161, v162, v163\n"
  "v_cvt_pk_bf16_f32 v162, v164, v165\n"
  "v_cvt_pk_bf16_f32 v163, v166, v167\n"
  "buffer_store_dwordx4 v[160:163], v52, s[88:91], s65 offen offset:0 sc1\n"
  "v_mfma_f32_16x16x32_bf16 v[200:203], a[8:11], v[84:87], v[200:203]\n"
  "ds_read_b128 a[8:11], v57 offset:10240\n"
  "s_bfe_u32 s59, s31, 0x10002\n"
  "s_lshl3_add_u32 s58, s59, s58\n"
  "s_add_u32 s27, s58, 50331648\n"
  "s_add_u32 s31, s31, 1\n"
  "s_lshr_b32 s58, s31, 3\n"
  "s_and_b32 s59, s31, 3\n"
  "s_lshl2_add_u32 s58, s58, s59\n"
  "s_lshl_b32 s58, s58, 15\n"
  "v_accvgpr_read_b32 v168, a152\n"
  "v_accvgpr_read_b32 v169, a153\n"
  "v_mfma_f32_16x16x32_bf16 v[204:207], a[12:15], v[84:87], v[204:207]\n"
  "ds_read_b128 a[12:15], v57 offset:11264\n"
  "v_accvgpr_read_b32 v170, a154\n"
  "v_accvgpr_read_b32 v171, a155\n"
  "v_accvgpr_read_b32 v172, a156\n"
  "v_accvgpr_read_b32 v173, a157\n"
  "v_accvgpr_read_b32 v174, a158\n"
  "v_accvgpr_read_b32 v175, a159\n"
  "v_cvt_pk_bf16_f32 v168, v168, v169\n"
  "v_cvt_pk_bf16_f32 v169, v170, v171\n"
  "v_cvt_pk_bf16_f32 v170, v172, v173\n"
  "v_cvt_pk_bf16_f32 v171, v174, v175\n"
  "buffer_store_dwordx4 v[168:171], v52, s[88:91], s65 offen offset:256 sc1\n"
  "v_mfma_f32_16x16x32_bf16 v[200:203], a[16:19], v[88:91], v[200:203]\n"
  "ds_read_b128 a[16:19], v57 offset:12288\n"
  "s_bfe_u32 s59, s31, 0x10002\n"
  "s_lshl3_add_u32 s58, s59, s58\n"
  "s_add_u32 s28, s58, 50331648\n"
  "s_add_u32 s31, s31, 1\n"
  "s_lshl_b32 s58, s54, 10\n"
  "v_lshl_add_u32 v49, v54, 4, s58\n"
  "v_subrev_u32 v48, 256, v49\n"
  "v_max_i32 v48, 0, v48\n"
  "v_accvgpr_read_b32 v176, a168\n"
  "v_accvgpr_read_b32 v177, a169\n"
  "v_mfma_f32_16x16x32_bf16 v[204:207], a[20:23], v[88:91], v[204:207]\n"
  "ds_read_b128 a[20:23], v57 offset:13312\n"
  "v_accvgpr_read_b32 v178, a170\n"
  "v_accvgpr_read_b32 v179, a171\n"
  "v_accvgpr_read_b32 v180, a172\n"
  "v_accvgpr_read_b32 v181, a173\n"
  "v_accvgpr_read_b32 v182, a174\n"
  "v_accvgpr_read_b32 v183, a175\n"
  "v_cvt_pk_bf16_f32 v176, v176, v177\n"
  "v_cvt_pk_bf16_f32 v177, v178, v179\n"
  "v_cvt_pk_bf16_f32 v178, v180, v181\n"
  "v_cvt_pk_bf16_f32 v179, v182, v183\n"
  "buffer_store_dwordx4 v[176:179], v52, s[88:91], s65 offen offset:512 sc1\n"
  "v_mfma_f32_16x16x32_bf16 v[200:203], a[24:27], v[92:95], v[200:203]\n"
  "ds_read_b128 a[24:27], v57 offset:14336\n"
  "v_mov_b32 v38, 0x3f803f80\n"
  "v_mov_b32 v39, 0x3f803f80\n"
  "v_mov_b32 v40, 0x3f803f80\n"
  "v_mov_b32 v41, 0x3f803f80\n"
  "v_accvgpr_read_b32 v184, a184\n"
  "v_accvgpr_read_b32 v185, a185\n"
  "v_accvgpr_read_b32 v186, a186\n"
  "v_accvgpr_read_b32 v187, a187\n"
  "v_mfma_f32_16x16x32_bf16 v[204:207], a[28:31], v[92:95], v[204:207]\n"
  "ds_read_b128 a[28:31], v57 offset:15360\n"
  "v_accvgpr_read_b32 v188, a188\n"
  "v_accvgpr_read_b32 v189, a189\n"
  "v_accvgpr_read_b32 v190, a190\n"
  "v_accvgpr_read_b32 v191, a191\n"
  "v_cvt_pk_bf16_f32 v184, v184, v185\n"
  "v_cvt_pk_bf16_f32 v185, v186, v187\n"
  "v_cvt_pk_bf16_f32 v186, v188, v189\n"
  "v_cvt_pk_bf16_f32 v187, v190, v191\n"
  "buffer_store_dwordx4 v[184:187], v52, s[88:91], s65 offen offset:768 sc1\n"
  "at_head%=:\n"
  "ds_read_b128 a[32:35], v58 offset:16384\n"
  "ds_read_b128 a[36:39], v58 offset:17408\n"
  "ds_read_b128 a[40:43], v58 offset:18432\n"
  "ds_read_b128 a[44:47], v58 offset:19456\n"
  "v_mfma_f32_32x32x16_bf16 v[128:143], v[32:35], v[64:67], 0\n"
  "v_max3_f32 v55, v192, v193, v194\n"
  "v_max3_f32 v56, v200, v201, v202\n"
  "v_max3_f32 v55, v55, v195, v196\n"
  "v_max3_f32 v56, v56, v203, v204\n"
  "v_max3_f32 v55, v55, v197, v198\n"
  "v_max3_f32 v56, v56, v205, v206\n"
  "v_max_f32 v55, v55, v199\n"
  "v_max_f32 v56, v56, v207\n"
  "v_mfma_f32_32x32x16_bf16 v[144:159], v[32:35], v[64:67], 0\n"
  "v_mov_b32 v0, 0\n"
  "v_mov_b32 v1, 0\n"
  "v_mov_b32 v2, 0\n"
  "v_mov_b32 v3, 0\n"
  "v_mov_b32 v50, 0\n"
  "v_mov_b32 v51, 0\n"
  "v_mov_b32 v52, 0\n"
  "v_mov_b32 v53, 0\n"
  "v_permlane32_swap_b32 v55, v56\n"
  "v_max_f32 v55, v55, v56\n"
  "v_mfma_f32_32x32x16_bf16 v[160:175], v[32:35], v[64:67], 0\n"
  "v_mov_b32 v56, v55\n"
  "s_nop 1\n"
  "v_permlane16_swap_b32 v55, v56\n"
  "v_max_f32 v55, v55, v56\n"
  "v_sub_f32 v45, 0, v55\n"
  "v_sub_f32 v62, 0, v55\n"
  "s_nop 1\n"
  "v_permlane32_swap_b32 v45, v62\n"
  "v_add_f32 v192, v45, v192\n"
  "v_mfma_f32_32x32x16_bf16 v[176:191], v[32:35], v[64:67], 0\n"
  "v_add_f32 v193, v45, v193\n"
  "v_add_f32 v194, v45, v194\n"
  "v_add_f32 v195, v45, v195\n"
  "v_add_f32 v196, v45, v196\n"
  "v_add_f32 v197, v45, v197\n"
  "v_add_f32 v198, v45, v198\n"
  "v_add_f32 v199, v45, v199\n"
  "v_add_f32 v200, v62, v200\n"
  "v_add_f32 v201, v62, v201\n"
  "v_add_f32 v202, v62, v202\n"
  "v_add_f32 v203, v62, v203\n"
  "v_add_f32 v204, v62, v204\n"
  "v_add_f32 v205, v62, v205\n"
  "v_add_f32 v206, v62, v206\n"
  "v_add_f32 v207, v62, v207\n"
  "v_exp_f32 v192, v192\n"
  "v_add_f32 v16, 0xf4000000, v63\n"
  "v_min_f32 v16, v45, v16\n"
  "v_mov_b32 v224, v45\n"
  "v_mov_b32 v240, v45\n"
  "v_exp_f32 v193, v193\n"
  "v_add_f32 v17, 0xf4040000, v63\n"
  "v_min_f32 v17, v45, v17\n"
  "v_mov_b32 v225, v45\n"
  "v_mov_b32 v241, v45\n"
  "v_cvt_pk_bf16_f32 v192, v192, v193\n"
  "v_exp_f32 v194, v194\n"
  "v_add_f32 v18, 0xf4080000, v63\n"
  "v_min_f32 v18, v45, v18\n"
  "v_mov_b32 v226, v45\n"
  "v_mov_b32 v242, v45\n"
  "v_exp_f32 v195, v195\n"
  "v_add_f32 v19, 0xf40c0000, v63\n"
  "v_min_f32 v19, v45, v19\n"
  "v_mov_b32 v227, v45\n"
  "v_mov_b32 v243, v45\n"
  "v_cvt_pk_bf16_f32 v193, v194, v195\n"
  "v_exp_f32 v196, v196\n"
  "v_add_f32 v20, 0xf4400000, v63\n"
  "v_min_f32 v20, v45, v20\n"
  "v_mov_b32 v228, v45\n"
  "v_mov_b32 v244, v45\n"
  "v_exp_f32 v197, v197\n"
  "v_add_f32 v21, 0xf4440000, v63\n"
  "v_min_f32 v21, v45, v21\n"
  "v_mov_b32 v229, v45\n"
  "v_mov_b32 v245, v45\n"
  "v_add_f32 v22, 0xf4480000, v63\n"
  "v_min_f32 v22, v45, v22\n"
  "v_mov_b32 v230, v45\n"
  "v_mov_b32 v246, v45\n"
  "v_add_f32 v23, 0xf44c0000, v63\n"
  "v_min_f32 v23, v45, v23\n"
  "v_mov_b32 v231, v45\n"
  "v_mov_b32 v247, v45\n"
  "v_add_f32 v24, 0xf3800000, v63\n"
  "v_min_f32 v24, v62, v24\n"
  "v_mov_b32 v232, v62\n"
  "v_mov_b32 v248, v62\n"
  "v_add_f32 v25, 0xf3880000, v63\n"
  "v_min_f32 v25, v62, v25\n"
  "v_mov_b32 v233, v62\n"
  "v_mov_b32 v249, v62\n"
  "v_add_f32 v26, 0xf3900000, v63\n"
  "v_min_f32 v26, v62, v26\n"
  "v_mov_b32 v234, v62\n"
  "v_mov_b32 v250, v62\n"
  "v_add_f32 v27, 0xf3980000, v63\n"
  "v_min_f32 v27, v62, v27\n"
  "v_mov_b32 v235, v62\n"
  "v_mov_b32 v251, v62\n"
  "v_add_f32 v28, 0xf4000000, v63\n"
  "v_min_f32 v28, v62, v28\n"
  "v_mov_b32 v236, v62\n"
  "v_mov_b32 v252, v62\n"
  "v_add_f32 v29, 0xf4040000, v63\n"
  "v_min_f32 v29, v62, v29\n"
  "v_mov_b32 v237, v62\n"
  "v_mov_b32 v253, v62\n"
  "v_add_f32 v30, 0xf4080000, v63\n"
  "v_min_f32 v30, v62, v30\n"
  "v_mov_b32 v238, v62\n"
  "v_mov_b32 v254, v62\n"
  "v_add_f32 v31, 0xf40c0000, v63\n"
  "v_min_f32 v31, v62, v31\n"
  "v_mov_b32 v239, v62\n"
  "v_mov_b32 v255, v62\n"
  "s_waitcnt lgkmcnt(4)\n"
  "s_cmp_gt_u32 s79, 1\n"
  "s_cbranch_scc0 at_chk%=\n"
  "at_loop%=:\n"
  "v_mfma_f32_16x16x32_bf16 v[208:211], a[0:3], v[64:67], v[224:227]\n"
  "ds_read_b128 a[48:51], v58 offset:20480\n"
  "v_cvt_pk_bf16_f32 v194, v196, v197\n"
  "v_mfma_f32_16x16x32_bf16 v[212:215], a[4:7], v[64:67], v[228:231]\n"
  "v_exp_f32 v198, v198\n"
  "v_mfma_f32_16x16x32_bf16 v[208:211], a[8:11], v[68:71], v[208:211]\n"
  "ds_read_b128 a[52:55], v58 offset:21504\n"
  "v_readlane_b32 s77, v61, s76\n"
  "v_mfma_f32_16x16x32_bf16 v[212:215], a[12:15], v[68:71], v[212:215]\n"
  "v_exp_f32 v199, v199\n"
  "v_mfma_f32_16x16x32_bf16 v[208:211], a[16:19], v[72:75], v[208:211]\n"
  "ds_read_b128 a[56:59], v58 offset:22528\n"
  "s_mov_b32 m0, s78\n"
  "v_cvt_pk_bf16_f32 v195, v198, v199\n"
  "v_mfma_f32_16x16x32_bf16 v[212:215], a[20:23], v[72:75], v[212:215]\n"
  "v_exp_f32 v200, v200\n"
  "v_mfma_f32_16x16x32_bf16 v[208:211], a[24:27], v[76:79], v[208:211]\n"
  "ds_read_b128 a[60:63], v58 offset:23552\n"
  "s_waitcnt vmcnt(8)\n"
  "s_barrier\n"
  "v_mfma_f32_16x16x32_bf16 v[212:215], a[28:31], v[76:79], v[212:215]\n"
  "v_exp_f32 v201, v201\n"
  "v_mfma_f32_16x16x32_bf16 v[216:219], a[0:3], v[80:83], v[232:235]\n"
  "ds_read_b128 a[0:3], v57 offset:32768\n"
  "v_cvt_pk_bf16_f32 v200, v200, v201\n"
  "v_mfma_f32_16x16x32_bf16 v[220:223], a[4:7], v[80:83], v[236:239]\n"
  "v_exp_f32 v202, v202\n"
  "v_mfma_f32_16x16x32_bf16 v[216:219], a[8:11], v[84:87], v[216:219]\n"
  "ds_read_b128 a[4:7], v57 offset:33792\n"
  "buffer_load_dwordx4 v59, s[88:91], s77 offen offset:0 sc1 lds\n"
  "v_mfma_f32_16x16x32_bf16 v[220:223], a[12:15], v[84:87], v[220:223]\n"
  "v_exp_f32 v203, v203\n"
  "v_mfma_f32_16x16x32_bf16 v[216:219], a[16:19], v[88:91], v[216:219]\n"
  "ds_read_b128 a[8:11], v57 offset:34816\n"
  "v_cvt_pk_bf16_f32 v201, v202, v203\n"
  "v_mfma_f32_16x16x32_bf16 v[220:223], a[20:23], v[88:91], v[220:223]\n"
  "v_exp_f32 v204, v204\n"
  "v_mfma_f32_16x16x32_bf16 v[216:219], a[24:27], v[92:95], v[216:219]\n"
  "ds_read_b128 a[12:15], v57 offset:35840\n"
  "s_waitcnt lgkmcnt(4)\n"
  "v_mfma_f32_16x16x32_bf16 v[220:223], a[28:31], v[92:95], v[220:223]\n"
  "v_exp_f32 v205, v205\n"
  "v_mfma_f32_16x16x32_bf16 v[128:131], a[32:35], v[192:195], v[128:131]\n"
  "ds_read_b128 a[16:19], v57 offset:36864\n"
  "v_cvt_pk_bf16_f32 v202, v204, v205\n"
  "v_mfma_f32_16x16x32_bf16 v[132:135], a[36:39], v[192:195], v[132:135]\n"
  "v_exp_f32 v206, v206\n"
  "v_mfma_f32_16x16x32_bf16 v[136:139], a[40:43], v[192:195], v[136:139]\n"
  "ds_read_b128 a[20:23], v57 offset:37888\n"
  "buffer_load_dwordx4 v59, s[88:91], s77 offen offset:1024 sc1 lds\n"
  "v_mfma_f32_16x16x32_bf16 v[140:143], a[44:47], v[192:195], v[140:143]\n"
  "v_exp_f32 v207, v207\n"
  "v_mfma_f32_16x16x32_bf16 v[144:147], a[48:51], v[192:195], v[144:147]\n"
  "ds_read_b128 a[24:27], v57 offset:38912\n"
  "v_cvt_pk_bf16_f32 v203, v206, v207\n"
  "v_mfma_f32_16x16x32_bf16 v[148:151], a[52:55], v[192:195], v[148:151]\n"
  "v_exp_f32 v208, v208\n"
  "v_mfma_f32_16x16x32_bf16 v[152:155], a[56:59], v[192:195], v[152:155]\n"
  "ds_read_b128 a[28:31], v57 offset:39936\n"
  "buffer_load_dwordx4 v59, s[88:91], s77 offen offset:2048 sc1 lds\n"
  "v_mfma_f32_16x16x32_bf16 v[156:159], a[60:63], v[192:195], v[156:159]\n"
  "v_exp_f32 v209, v209\n"
  "v_mfma_f32_16x16x32_bf16 v[0:3], v[38:41], v[192:195], v[0:3]\n"
  "s_add_u32 s76, s76, 1\n"
  "v_mfma_f32_16x16x32_bf16 v[160:163], a[32:35], v[200:203], v[160:163]\n"
  "ds_read_b128 a[32:35], v58 offset:24576\n"
  "v_cvt_pk_bf16_f32 v208, v208, v209\n"
  "v_mfma_f32_16x16x32_bf16 v[164:167], a[36:39], v[200:203], v[164:167]\n"
  "v_exp_f32 v210, v210\n"
  "v_mfma_f32_16x16x32_bf16 v[168:171], a[40:43], v[200:203], v[168:171]\n"
  "ds_read_b128 a[36:39], v58 offset:25600\n"
  "buffer_load_dwordx4 v59, s[88:91], s77 offen offset:3072 sc1 lds\n"
  "v_mfma_f32_16x16x32_bf16 v[172:175], a[44:47], v[200:203], v[172:175]\n"
  "v_exp_f32 v211, v211\n"
  "v_mfma_f32_16x16x32_bf16 v[176:179], a[48:51], v[200:203], v[176:179]\n"
  "ds_read_b128 a[40:43], v58 offset:26624\n"
  "v_cvt_pk_bf16_f32 v209, v210, v211\n"
  "v_mfma_f32_16x16x32_bf16 v[180:183], a[52:55], v[200:203], v[180:183]\n"
  "v_exp_f32 v212, v212\n"
  "v_mfma_f32_16x16x32_bf16 v[184:187], a[56:59], v[200:203], v[184:187]\n"
  "ds_read_b128 a[44:47], v58 offset:27648\n"
  "s_add_u32 m0, s78, 0x1000\n"
  "v_mfma_f32_16x16x32_bf16 v[188:191], a[60:63], v[200:203], v[188:191]\n"
  "v_exp_f32 v213, v213\n"
  "v_mfma_f32_16x16x32_bf16 v[50:53], v[38:41], v[200:203], v[50:53]\n"
  "s_waitcnt lgkmcnt(4)\n"
  "v_mfma_f32_16x16x32_bf16 v[192:195], a[0:3], v[64:67], v[224:227]\n"
  "ds_read_b128 a[48:51], v58 offset:28672\n"
  "v_cvt_pk_bf16_f32 v210, v212, v213\n"
  "v_mfma_f32_16x16x32_bf16 v[196:199], a[4:7], v[64:67], v[228:231]\n"
  "v_exp_f32 v214, v214\n"
  "v_mfma_f32_16x16x32_bf16 v[192:195], a[8:11], v[68:71], v[192:195]\n"
  "ds_read_b128 a[52:55], v58 offset:29696\n"
  "buffer_load_dwordx4 v60, s[88:91], s77 offen offset:0 sc1 lds\n"
  "v_mfma_f32_16x16x32_bf16 v[196:199], a[12:15], v[68:71], v[196:199]\n"
  "v_exp_f32 v215, v215\n"
  "v_mfma_f32_16x16x32_bf16 v[192:195], a[16:19], v[72:75], v[192:195]\n"
  "ds_read_b128 a[56:59], v58 offset:30720\n"
  "v_cvt_pk_bf16_f32 v211, v214, v215\n"
  "v_mfma_f32_16x16x32_bf16 v[196:199], a[20:23], v[72:75], v[196:199]\n"
  "v_exp_f32 v216, v216\n"
  "v_mfma_f32_16x16x32_bf16 v[192:195], a[24:27], v[76:79], v[192:195]\n"
  "ds_read_b128 a[60:63], v58 offset:31744\n"
  "v_mfma_f32_16x16x32_bf16 v[196:199], a[28:31], v[76:79], v[196:199]\n"
  "v_exp_f32 v217, v217\n"
  "v_mfma_f32_16x16x32_bf16 v[200:203], a[0:3], v[80:83], v[232:235]\n"
  "ds_read_b128 a[0:3], v57 offset:40960\n"
  "v_cvt_pk_bf16_f32 v216, v216, v217\n"
  "v_mfma_f32_16x16x32_bf16 v[204:207], a[4:7], v[80:83], v[236:239]\n"
  "v_exp_f32 v218, v218\n"
  "v_mfma_f32_16x16x32_bf16 v[200:203], a[8:11], v[84:87], v[200:203]\n"
  "ds_read_b128 a[4:7], v57 offset:41984\n"
  "buffer_load_dwordx4 v60, s[88:91], s77 offen offset:1024 sc1 lds\n"
  "v_mfma_f32_16x16x32_bf16 v[204:207], a[12:15], v[84:87], v[204:207]\n"
  "v_exp_f32 v219, v219\n"
  "v_mfma_f32_16x16x32_bf16 v[200:203], a[16:19], v[88:91], v[200:203]\n"
  "ds_read_b128 a[8:11], v57 offset:43008\n"
  "v_cvt_pk_bf16_f32 v217, v218, v219\n"
  "v_mfma_f32_16x16x32_bf16 v[204:207], a[20:23], v[88:91], v[204:207]\n"
  "v_exp_f32 v220, v220\n"
  "v_mfma_f32_16x16x32_bf16 v[200:203], a[24:27], v[92:95], v[200:203]\n"
  "ds_read_b128 a[12:15], v57 offset:44032\n"
  "s_waitcnt lgkmcnt(4)\n"
  "v_mfma_f32_16x16x32_bf16 v[204:207], a[28:31], v[92:95], v[204:207]\n"
  "v_exp_f32 v221, v221\n"
  "v_mfma_f32_16x16x32_bf16 v[128:131], a[32:35], v[208:211], v[128:131]\n"
  "ds_read_b128 a[16:19], v57 offset:45056\n"
  "v_cvt_pk_bf16_f32 v218, v220, v221\n"
  "v_mfma_f32_16x16x32_bf16 v[132:135], a[36:39], v[208:211], v[132:135]\n"
  "v_exp_f32 v222, v222\n"
  "v_mfma_f32_16x16x32_bf16 v[136:139], a[40:43], v[208:211], v[136:139]\n"
  "ds_read_b128 a[20:23], v57 offset:46080\n"
  "buffer_load_dwordx4 v60, s[88:91], s77 offen offset:2048 sc1 lds\n"
  "v_mfma_f32_16x16x32_bf16 v[140:143], a[44:47], v[208:211], v[140:143]\n"
  "v_exp_f32 v223, v223\n"
  "v_mfma_f32_16x16x32_bf16 v[144:147], a[48:51], v[208:211], v[144:147]\n"
  "ds_read_b128 a[24:27], v57 offset:47104\n"
  "v_cvt_pk_bf16_f32 v219, v222, v223\n"
  "v_mfma_f32_16x16x32_bf16 v[148:151], a[52:55], v[208:211], v[148:151]\n"
  "v_exp_f32 v192, v192\n"
  "v_mfma_f32_16x16x32_bf16 v[152:155], a[56:59], v[208:211], v[152:155]\n"
  "ds_read_b128 a[28:31], v57 offset:48128\n"
  "v_mfma_f32_16x16x32_bf16 v[156:159], a[60:63], v[208:211], v[156:159]\n"
  "v_exp_f32 v193, v193\n"
  "v_mfma_f32_16x16x32_bf16 v[0:3], v[38:41], v[208:211], v[0:3]\n"
  "s_add_u32 s78, s78, 0x8000\n"
  "s_and_b32 s78, s78, 0x1ffff\n"
  "v_mfma_f32_16x16x32_bf16 v[160:163], a[32:35], v[216:219], v[160:163]\n"
  "ds_read_b128 a[32:35], v58 offset:49152\n"
  "v_cvt_pk_bf16_f32 v192, v192, v193\n"
  "v_mfma_f32_16x16x32_bf16 v[164:167], a[36:39], v[216:219], v[164:167]\n"
  "v_exp_f32 v194, v194\n"
  "v_mfma_f32_16x16x32_bf16 v[168:171], a[40:43], v[216:219], v[168:171]\n"
  "ds_read_b128 a[36:39], v58 offset:50176\n"
  "buffer_load_dwordx4 v60, s[88:91], s77 offen offset:3072 sc1 lds\n"
  "v_mfma_f32_16x16x32_bf16 v[172:175], a[44:47], v[216:219], v[172:175]\n"
  "v_exp_f32 v195, v195\n"
  "v_mfma_f32_16x16x32_bf16 v[176:179], a[48:51], v[216:219], v[176:179]\n"
  "ds_read_b128 a[40:43], v58 offset:51200\n"
  "v_cvt_pk_bf16_f32 v193, v194, v195\n"
  "v_mfma_f32_16x16x32_bf16 v[180:183], a[52:55], v[216:219], v[180:183]\n"
  "v_exp_f32 v196, v196\n"
  "v_mfma_f32_16x16x32_bf16 v[184:187], a[56:59], v[216:219], v[184:187]\n"
  "ds_read_b128 a[44:47], v58 offset:52224\n"
  "v_xor_b32 v57, 0x10000, v57\n"
  "v_mfma_f32_16x16x32_bf16 v[188:191], a[60:63], v[216:219], v[188:191]\n"
  "v_exp_f32 v197, v197\n"
  "v_mfma_f32_16x16x32_bf16 v[50:53], v[38:41], v[216:219], v[50:53]\n"
  "s_waitcnt lgkmcnt(4)\n"
  "v_mfma_f32_16x16x32_bf16 v[208:211], a[0:3], v[64:67], v[224:227]\n"
  "ds_read_b128 a[48:51], v58 offset:53248\n"
  "v_cvt_pk_bf16_f32 v194, v196, v197\n"
  "v_mfma_f32_16x16x32_bf16 v[212:215], a[4:7], v[64:67], v[228:231]\n"
  "v_exp_f32 v198, v198\n"
  "v_mfma_f32_16x16x32_bf16 v[208:211], a[8:11], v[68:71], v[208:211]\n"
  "ds_read_b128 a[52:55], v58 offset:54272\n"
  "v_readlane_b32 s77, v61, s76\n"
  "v_mfma_f32_16x16x32_bf16 v[212:215], a[12:15], v[68:71], v[212:215]\n"
  "v_exp_f32 v199, v199\n"
  "v_mfma_f32_16x16x32_bf16 v[208:211], a[16:19], v[72:75], v[208:211]\n"
  "ds_read_b128 a[56:59], v58 offset:55296\n"
  "s_mov_b32 m0, s78\n"
  "v_cvt_pk_bf16_f32 v195, v198, v199\n"
  "v_mfma_f32_16x16x32_bf16 v[212:215], a[20:23], v[72:75], v[212:215]\n"
  "v_exp_f32 v200, v200\n"
  "v_mfma_f32_16x16x32_bf16 v[208:211], a[24:27], v[76:79], v[208:211]\n"
  "ds_read_b128 a[60:63], v58 offset:56320\n"
  "s_waitcnt vmcnt(8)\n"
  "s_barrier\n"
  "v_mfma_f32_16x16x32_bf16 v[212:215], a[28:31], v[76:79], v[212:215]\n"
  "v_exp_f32 v201, v201\n"
  "v_mfma_f32_16x16x32_bf16 v[216:219], a[0:3], v[80:83], v[232:235]\n"
  "ds_read_b128 a[0:3], v57 offset:0\n"
  "v_cvt_pk_bf16_f32 v200, v200, v201\n"
  "v_mfma_f32_16x16x32_bf16 v[220:223], a[4:7], v[80:83], v[236:239]\n"
  "v_exp_f32 v202, v202\n"
  "v_mfma_f32_16x16x32_bf16 v[216:219], a[8:11], v[84:87], v[216:219]\n"
  "ds_read_b128 a[4:7], v57 offset:1024\n"
  "buffer_load_dwordx4 v59, s[88:91], s77 offen offset:0 sc1 lds\n"
  "v_mfma_f32_16x16x32_bf16 v[220:223], a[12:15], v[84:87], v[220:223]\n"
  "v_exp_f32 v203, v203\n"
  "v_mfma_f32_16x16x32_bf16 v[216:219], a[16:19], v[88:91], v[216:219]\n"
  "ds_read_b128 a[8:11], v57 offset:2048\n"
  "v_cvt_pk_bf16_f32 v201, v202, v203\n"
  "v_mfma_f32_16x16x32_bf16 v[220:223], a[20:23], v[88:91], v[220:223]\n"
  "v_exp_f32 v204, v204\n"
  "v_mfma_f32_16x16x32_bf16 v[216:219], a[24:27], v[92:95], v[216:219]\n"
  "ds_read_b128 a[12:15], v57 offset:3072\n"
  "s_waitcnt lgkmcnt(4)\n"
  "v_mfma_f32_16x16x32_bf16 v[220:223], a[28:31], v[92:95], v[220:223]\n"
  "v_exp_f32 v205, v205\n"
  "v_mfma_f32_16x16x32_bf16 v[128:131], a[32:35], v[192:195], v[128:131]\n"
  "ds_read_b128 a[16:19], v57 offset:4096\n"
  "v_cvt_pk_bf16_f32 v202, v204, v205\n"
  "v_mfma_f32_16x16x32_bf16 v[132:135], a[36:39], v[192:195], v[132:135]\n"
  "v_exp_f32 v206, v206\n"
  "v_mfma_f32_16x16x32_bf16 v[136:139], a[40:43], v[192:195], v[136:139]\n"
  "ds_read_b128 a[20:23], v57 offset:5120\n"
  "buffer_load_dwordx4 v59, s[88:91], s77 offen offset:1024 sc1 lds\n"
  "v_mfma_f32_16x16x32_bf16 v[140:143], a[44:47], v[192:195], v[140:143]\n"
  "v_exp_f32 v207, v207\n"
  "v_mfma_f32_16x16x32_bf16 v[144:147], a[48:51], v[192:195], v[144:147]\n"
  "ds_read_b128 a[24:27], v57 offset:6144\n"
  "v_cvt_pk_bf16_f32 v203, v206, v207\n"
  "v_mfma_f32_16x16x32_bf16 v[148:151], a[52:55], v[192:195], v[148:151]\n"
  "v_exp_f32 v208, v208\n"
  "v_mfma_f32_16x16x32_bf16 v[152:155], a[56:59], v[192:195], v[152:155]\n"
  "ds_read_b128 a[28:31], v57 offset:7168\n"
  "buffer_load_dwordx4 v59, s[88:91], s77 offen offset:2048 sc1 lds\n"
  "v_mfma_f32_16x16x32_bf16 v[156:159], a[60:63], v[192:195], v[156:159]\n"
  "v_exp_f32 v209, v209\n"
  "v_mfma_f32_16x16x32_bf16 v[0:3], v[38:41], v[192:195], v[0:3]\n"
  "s_add_u32 s76, s76, 1\n"
  "v_mfma_f32_16x16x32_bf16 v[160:163], a[32:35], v[200:203], v[160:163]\n"
  "ds_read_b128 a[32:35], v58 offset:57344\n"
  "v_cvt_pk_bf16_f32 v208, v208, v209\n"
  "v_mfma_f32_16x16x32_bf16 v[164:167], a[36:39], v[200:203], v[164:167]\n"
  "v_exp_f32 v210, v210\n"
  "v_mfma_f32_16x16x32_bf16 v[168:171], a[40:43], v[200:203], v[168:171]\n"
  "ds_read_b128 a[36:39], v58 offset:58368\n"
  "buffer_load_dwordx4 v59, s[88:91], s77 offen offset:3072 sc1 lds\n"
  "v_mfma_f32_16x16x32_bf16 v[172:175], a[44:47], v[200:203], v[172:175]\n"
  "v_exp_f32 v211, v211\n"
  "v_mfma_f32_16x16x32_bf16 v[176:179], a[48:51], v[200:203], v[176:179]\n"
  "ds_read_b128 a[40:43], v58 offset:59392\n"
  "v_cvt_pk_bf16_f32 v209, v210, v211\n"
  "v_mfma_f32_16x16x32_bf16 v[180:183], a[52:55], v[200:203], v[180:183]\n"
  "v_exp_f32 v212, v212\n"
  "v_mfma_f32_16x16x32_bf16 v[184:187], a[56:59], v[200:203], v[184:187]\n"
  "ds_read_b128 a[44:47], v58 offset:60416\n"
  "s_add_u32 m0, s78, 0x1000\n"
  "v_mfma_f32_16x16x32_bf16 v[188:191], a[60:63], v[200:203], v[188:191]\n"
  "v_exp_f32 v213, v213\n"
  "v_mfma_f32_16x16x32_bf16 v[50:53], v[38:41], v[200:203], v[50:53]\n"
  "s_waitcnt lgkmcnt(4)\n"
  "v_mfma_f32_16x16x32_bf16 v[192:195], a[0:3], v[64:67], v[240:243]\n"
  "ds_read_b128 a[48:51], v58 offset:61440\n"
  "v_cvt_pk_bf16_f32 v210, v212, v213\n"
  "v_mfma_f32_16x16x32_bf16 v[196:199], a[4:7], v[64:67], v[244:247]\n"
  "v_exp_f32 v214, v214\n"
  "v_mfma_f32_16x16x32_bf16 v[192:195], a[8:11], v[68:71], v[192:195]\n"
  "ds_read_b128 a[52:55], v58 offset:62464\n"
  "buffer_load_dwordx4 v60, s[88:91], s77 offen offset:0 sc1 lds\n"
  "v_mfma_f32_16x16x32_bf16 v[196:199], a[12:15], v[68:71], v[196:199]\n"
  "v_exp_f32 v215, v215\n"
  "v_mfma_f32_16x16x32_bf16 v[192:195], a[16:19], v[72:75], v[192:195]\n"
  "ds_read_b128 a[56:59], v58 offset:63488\n"
  "v_cvt_pk_bf16_f32 v211, v214, v215\n"
  "v_mfma_f32_16x16x32_bf16 v[196:199], a[20:23], v[72:75], v[196:199]\n"
  "v_exp_f32 v216, v216\n"
  "v_mfma_f32_16x16x32_bf16 v[192:195], a[24:27], v[76:79], v[192:195]\n"
  "ds_read_b128 a[60:63], v58 offset:64512\n"
  "s_sub_u32 s79, s79, 1\n"
  "v_mfma_f32_16x16x32_bf16 v[196:199], a[28:31], v[76:79], v[196:199]\n"
  "v_exp_f32 v217, v217\n"
  "v_mfma_f32_16x16x32_bf16 v[200:203], a[0:3], v[80:83], v[248:251]\n"
  "ds_read_b128 a[0:3], v57 offset:8192\n"
  "v_cvt_pk_bf16_f32 v216, v216, v217\n"
  "v_mfma_f32_16x16x32_bf16 v[204:207], a[4:7], v[80:83], v[252:255]\n"
  "v_exp_f32 v218, v218\n"
  "v_mfma_f32_16x16x32_bf16 v[200:203], a[8:11], v[84:87], v[200:203]\n"
  "ds_read_b128 a[4:7], v57 offset:9216\n"
  "buffer_load_dwordx4 v60, s[88:91], s77 offen offset:1024 sc1 lds\n"
  "v_mfma_f32_16x16x32_bf16 v[204:207], a[12:15], v[84:87], v[204:207]\n"
  "v_exp_f32 v219, v219\n"
  "v_mfma_f32_16x16x32_bf16 v[200:203], a[16:19], v[88:91], v[200:203]\n"
  "ds_read_b128 a[8:11], v57 offset:10240\n"
  "v_cvt_pk_bf16_f32 v217, v218, v219\n"
  "v_mfma_f32_16x16x32_bf16 v[204:207], a[20:23], v[88:91], v[204:207]\n"
  "v_exp_f32 v220, v220\n"
  "v_mfma_f32_16x16x32_bf16 v[200:203], a[24:27], v[92:95], v[200:203]\n"
  "ds_read_b128 a[12:15], v57 offset:11264\n"
  "s_waitcnt lgkmcnt(4)\n"
  "v_mfma_f32_16x16x32_bf16 v[204:207], a[28:31], v[92:95], v[204:207]\n"
  "v_exp_f32 v221, v221\n"
  "v_mfma_f32_16x16x32_bf16 v[128:131], a[32:35], v[208:211], v[128:131]\n"
  "ds_read_b128 a[16:19], v57 offset:12288\n"
  "v_cvt_pk_bf16_f32 v218, v220, v221\n"
  "v_mfma_f32_16x16x32_bf16 v[132:135], a[36:39], v[208:211], v[132:135]\n"
  "v_exp_f32 v222, v222\n"
  "v_mfma_f32_16x16x32_bf16 v[136:139], a[40:43], v[208:211], v[136:139]\n"
  "ds_read_b128 a[20:23], v57 offset:13312\n"
  "buffer_load_dwordx4 v60, s[88:91], s77 offen offset:2048 sc1 lds\n"
  "v_mfma_f32_16x16x32_bf16 v[140:143], a[44:47], v[208:211], v[140:143]\n"
  "v_exp_f32 v223, v223\n"
  "v_mfma_f32_16x16x32_bf16 v[144:147], a[48:51], v[208:211], v[144:147]\n"
  "ds_read_b128 a[24:27], v57 offset:14336\n"
  "v_cvt_pk_bf16_f32 v219, v222, v223\n"
  "v_mfma_f32_16x16x32_bf16 v[148:151], a[52:55], v[208:211], v[148:151]\n"
  "v_exp_f32 v192, v192\n"
  "v_mfma_f32_16x16x32_bf16 v[152:155], a[56:59], v[208:211], v[152:155]\n"
  "ds_read_b128 a[28:31], v57 offset:15360\n"
  "v_xor_b32 v58, 0x10000, v58\n"
  "v_mfma_f32_16x16x32_bf16 v[156:159], a[60:63], v[208:211], v[156:159]\n"
  "v_exp_f32 v193, v193\n"
  "v_mfma_f32_16x16x32_bf16 v[0:3], v[38:41], v[208:211], v[0:3]\n"
  "s_add_u32 s78, s78, 0x8000\n"
  "s_and_b32 s78, s78, 0x1ffff\n"
  "v_mfma_f32_16x16x32_bf16 v[160:163], a[32:35], v[216:219], v[160:163]\n"
  "ds_read_b128 a[32:35], v58 offset:16384\n"
  "v_cvt_pk_bf16_f32 v192, v192, v193\n"
  "v_mfma_f32_16x16x32_bf16 v[164:167], a[36:39], v[216:219], v[164:167]\n"
  "v_exp_f32 v194, v194\n"
  "v_mfma_f32_16x16x32_bf16 v[168:171], a[40:43], v[216:219], v[168:171]\n"
  "ds_read_b128 a[36:39], v58 offset:17408\n"
  "buffer_load_dwordx4 v60, s[88:91], s77 offen offset:3072 sc1 lds\n"
  "v_mfma_f32_16x16x32_bf16 v[172:175], a[44:47], v[216:219], v[172:175]\n"
  "v_exp_f32 v195, v195\n"
  "v_mfma_f32_16x16x32_bf16 v[176:179], a[48:51], v[216:219], v[176:179]\n"
  "ds_read_b128 a[40:43], v58 offset:18432\n"
  "v_cvt_pk_bf16_f32 v193, v194, v195\n"
  "v_mfma_f32_16x16x32_bf16 v[180:183], a[52:55], v[216:219], v[180:183]\n"
  "v_exp_f32 v196, v196\n"
  "v_mfma_f32_16x16x32_bf16 v[184:187], a[56:59], v[216:219], v[184:187]\n"
  "ds_read_b128 a[44:47], v58 offset:19456\n"
  "s_cmp_gt_u32 s79, s97\n"
  "v_mfma_f32_16x16x32_bf16 v[188:191], a[60:63], v[216:219], v[188:191]\n"
  "v_exp_f32 v197, v197\n"
  "v_mfma_f32_16x16x32_bf16 v[50:53], v[38:41], v[216:219], v[50:53]\n"
  "s_waitcnt lgkmcnt(4)\n"
  "s_cbranch_scc1 at_loop%=\n"
  "at_chk%=:\n"
  "s_cmp_gt_u32 s97, 1\n"
  "s_cbranch_scc1 at_ev%=\n"
  "s_cmp_eq_u32 s79, 0\n"
  "s_cbranch_scc0 at_mk%=\n"
  "v_mfma_f32_16x16x32_bf16 v[208:211], a[0:3], v[64:67], v[16:19]\n"
  "ds_read_b128 a[48:51], v58 offset:20480\n"
  "buffer_load_dwordx2 v[8:9], v49, s[8:11], s24 offen sc1\n"
  "v_cvt_pk_bf16_f32 v194, v196, v197\n"
  "v_mfma_f32_16x16x32_bf16 v[212:215], a[4:7], v[64:67], v[20:23]\n"
  "v_exp_f32 v198, v198\n"
  "v_mfma_f32_16x16x32_bf16 v[208:211], a[8:11], v[68:71], v[208:211]\n"
  "ds_read_b128 a[52:55], v58 offset:21504\n"
  "buffer_load_dwordx2 v[10:11], v49, s[8:11], s25 offen sc1\n"
  "v_readlane_b32 s77, v61, s76\n"
  "v_mfma_f32_16x16x32_bf16 v[212:215], a[12:15], v[68:71], v[212:215]\n"
  "v_exp_f32 v199, v199\n"
  "v_mfma_f32_16x16x32_bf16 v[208:211], a[16:19], v[72:75], v[208:211]\n"
  "ds_read_b128 a[56:59], v58 offset:22528\n"
  "s_mov_b32 m0, s78\n"
  "buffer_load_dwordx2 v[12:13], v49, s[8:11], s26 offen sc1\n"
  "v_cvt_pk_bf16_f32 v195, v198, v199\n"
  "v_mfma_f32_16x16x32_bf16 v[212:215], a[20:23], v[72:75], v[212:215]\n"
  "v_exp_f32 v200, v200\n"
  "v_mfma_f32_16x16x32_bf16 v[208:211], a[24:27], v[76:79], v[208:211]\n"
  "ds_read_b128 a[60:63], v58 offset:23552\n"
  "s_waitcnt vmcnt(11)\n"
  "s_barrier\n"
  "v_mfma_f32_16x16x32_bf16 v[212:215], a[28:31], v[76:79], v[212:215]\n"
  "v_exp_f32 v201, v201\n"
  "v_mfma_f32_16x16x32_bf16 v[216:219], a[0:3], v[80:83], v[24:27]\n"
  "ds_read_b128 a[0:3], v57 offset:32768\n"
  "v_cvt_pk_bf16_f32 v200, v200, v201\n"
  "v_mfma_f32_16x16x32_bf16 v[220:223], a[4:7], v[80:83], v[28:31]\n"
  "v_exp_f32 v202, v202\n"
  "v_mfma_f32_16x16x32_bf16 v[216:219], a[8:11], v[84:87], v[216:219]\n"
  "ds_read_b128 a[4:7], v57 offset:33792\n"
  "buffer_load_dwordx4 v59, s[88:91], s77 offen offset:0 sc1 lds\n"
  "v_add_f32 v240, 0xf4800000, v63\n"
  "v_min_f32 v240, v45, v240\n"
  "v_add_f32 v241, 0xf4820000, v63\n"
  "v_min_f32 v241, v45, v241\n"
  "v_mfma_f32_16x16x32_bf16 v[220:223], a[12:15], v[84:87], v[220:223]\n"
  "v_exp_f32 v203, v203\n"
  "v_mfma_f32_16x16x32_bf16 v[216:219], a[16:19], v[88:91], v[216:219]\n"
  "ds_read_b128 a[8:11], v57 offset:34816\n"
  "s_add_u32 m0, s29, 0\n"
  "s_add_u32 s64, s30, 0\n"
  "buffer_load_dwordx4 v48, s[8:11], s64 offen sc1 lds\n"
  "s_add_u32 m0, m0, 256\n"
  "s_nop 0\n"
  "buffer_load_dwordx4 v49, s[8:11], s64 offen sc1 lds\n"
  "s_mov_b32 m0, s78\n"
  "v_cvt_pk_bf16_f32 v201, v202, v203\n"
  "v_add_f32 v242, 0xf4840000, v63\n"
  "v_min_f32 v242, v45, v242\n"
  "v_add_f32 v243, 0xf4860000, v63\n"
  "v_mfma_f32_16x16x32_bf16 v[220:223], a[20:23], v[88:91], v[220:223]\n"
  "v_exp_f32 v204, v204\n"
  "v_mfma_f32_16x16x32_bf16 v[216:219], a[24:27], v[92:95], v[216:219]\n"
  "ds_read_b128 a[12:15], v57 offset:35840\n"
  "s_waitcnt lgkmcnt(4)\n"
  "v_min_f32 v243, v45, v243\n"
  "v_add_f32 v244, 0xf4a00000, v63\n"
  "v_min_f32 v244, v45, v244\n"
  "v_mfma_f32_16x16x32_bf16 v[220:223], a[28:31], v[92:95], v[220:223]\n"
  "v_exp_f32 v205, v205\n"
  "v_mfma_f32_16x16x32_bf16 v[128:131], a[32:35], v[192:195], v[128:131]\n"
  "ds_read_b128 a[16:19], v57 offset:36864\n"
  "buffer_load_dwordx2 v[14:15], v49, s[8:11], s27 offen sc1\n"
  "v_cvt_pk_bf16_f32 v202, v204, v205\n"
  "v_add_f32 v245, 0xf4a20000, v63\n"
  "v_min_f32 v245, v45, v245\n"
  "v_add_f32 v246, 0xf4a40000, v63\n"
  "v_mfma_f32_16x16x32_bf16 v[132:135], a[36:39], v[192:195], v[132:135]\n"
  "v_exp_f32 v206, v206\n"
  "v_mfma_f32_16x16x32_bf16 v[136:139], a[40:43], v[192:195], v[136:139]\n"
  "ds_read_b128 a[20:23], v57 offset:37888\n"
  "buffer_load_dwordx4 v59, s[88:91], s77 offen offset:1024 sc1 lds\n"
  "v_min_f32 v246, v45, v246\n"
  "v_add_f32 v247, 0xf4a60000, v63\n"
  "v_min_f32 v247, v45, v247\n"
  "v_mfma_f32_16x16x32_bf16 v[140:143], a[44:47], v[192:195], v[140:143]\n"
  "v_exp_f32 v207, v207\n"
  "v_mfma_f32_16x16x32_bf16 v[144:147], a[48:51], v[192:195], v[144:147]\n"
  "ds_read_b128 a[24:27], v57 offset:38912\n"
  "s_add_u32 m0, s29, 1280\n"
  "s_add_u32 s64, s30, 32768\n"
  "buffer_load_dwordx4 v48, s[8:11], s64 offen sc1 lds\n"
  "s_add_u32 m0, m0, 256\n"
  "s_nop 0\n"
  "buffer_load_dwordx4 v49, s[8:11], s64 offen sc1 lds\n"
  "s_mov_b32 m0, s78\n"
  "v_cvt_pk_bf16_f32 v203, v206, v207\n"
  "v_add_f32 v248, 0xf4400000, v63\n"
  "v_min_f32 v248, v62, v248\n"
  "v_add_f32 v249, 0xf4440000, v63\n"
  "v_min_f32 v249, v62, v249\n"
  "v_mfma_f32_16x16x32_bf16 v[148:151], a[52:55], v[192:195], v[148:151]\n"
  "v_exp_f32 v208, v208\n"
  "v_mfma_f32_16x16x32_bf16 v[152:155], a[56:59], v[192:195], v[152:155]\n"
  "ds_read_b128 a[28:31], v57 offset:39936\n"
  "buffer_load_dwordx4 v59, s[88:91], s77 offen offset:2048 sc1 lds\n"
  "v_add_f32 v250, 0xf4480000, v63\n"
  "v_min_f32 v250, v62, v250\n"
  "v_add_f32 v251, 0xf44c0000, v63\n"
  "v_mfma_f32_16x16x32_bf16 v[156:159], a[60:63], v[192:195], v[156:159]\n"
  "v_exp_f32 v209, v209\n"
  "v_mfma_f32_16x16x32_bf16 v[0:3], v[38:41], v[192:195], v[0:3]\n"
  "s_add_u32 s76, s76, 1\n"
  "v_mfma_f32_16x16x32_bf16 v[160:163], a[32:35], v[200:203], v[160:163]\n"
  "ds_read_b128 a[32:35], v58 offset:24576\n"
  "buffer_load_dwordx2 v[36:37], v49, s[8:11], s28 offen sc1\n"
  "v_cvt_pk_bf16_f32 v208, v208, v209\n"
  "v_min_f32 v251, v62, v251\n"
  "v_add_f32 v252, 0xf4800000, v63\n"
  "v_min_f32 v252, v62, v252\n"
  "v_mfma_f32_16x16x32_bf16 v[164:167], a[36:39], v[200:203], v[164:167]\n"
  "v_exp_f32 v210, v210\n"
  "v_mfma_f32_16x16x32_bf16 v[168:171], a[40:43], v[200:203], v[168:171]\n"
  "ds_read_b128 a[36:39], v58 offset:25600\n"
  "buffer_load_dwordx4 v59, s[88:91], s77 offen offset:3072 sc1 lds\n"
  "v_add_f32 v253, 0xf4820000, v63\n"
  "v_min_f32 v253, v62, v253\n"
  "v_add_f32 v254, 0xf4840000, v63\n"
  "v_mfma_f32_16x16x32_bf16 v[172:175], a[44:47], v[200:203], v[172:175]\n"
  "v_exp_f32 v211, v211\n"
  "v_mfma_f32_16x16x32_bf16 v[176:179], a[48:51], v[200:203], v[176:179]\n"
  "ds_read_b128 a[40:43], v58 offset:26624\n"
  "s_add_u32 m0, s29, 2560\n"
  "s_add_u32 s64, s30, 65536\n"
  "buffer_load_dwordx4 v48, s[8:11], s64 offen sc1 lds\n"
  "s_add_u32 m0, m0, 256\n"
  "s_nop 0\n"
  "buffer_load_dwordx4 v49, s[8:11], s64 offen sc1 lds\n"
  "s_mov_b32 m0, s78\n"
  "v_cvt_pk_bf16_f32 v209, v210, v211\n"
  "v_min_f32 v254, v62, v254\n"
  "v_add_f32 v255, 0xf4860000, v63\n"
  "v_min_f32 v255, v62, v255\n"
  "v_mfma_f32_16x16x32_bf16 v[180:183], a[52:55], v[200:203], v[180:183]\n"
  "v_exp_f32 v212, v212\n"
  "v_mfma_f32_16x16x32_bf16 v[184:187], a[56:59], v[200:203], v[184:187]\n"
  "ds_read_b128 a[44:47], v58 offset:27648\n"
  "s_add_u32 m0, s78, 0x1000\n"
  "v_mfma_f32_16x16x32_bf16 v[188:191], a[60:63], v[200:203], v[188:191]\n"
  "v_exp_f32 v213, v213\n"
  "v_mfma_f32_16x16x32_bf16 v[50:53], v[38:41], v[200:203], v[50:53]\n"
  "s_waitcnt lgkmcnt(4)\n"
  "v_mfma_f32_16x16x32_bf16 v[192:195], a[0:3], v[64:67], v[240:243]\n"
  "ds_read_b128 a[48:51], v58 offset:28672\n"
  "v_cvt_pk_bf16_f32 v210, v212, v213\n"
  "v_mfma_f32_16x16x32_bf16 v[196:199], a[4:7], v[64:67], v[244:247]\n"
  "v_exp_f32 v214, v214\n"
  "v_mfma_f32_16x16x32_bf16 v[192:195], a[8:11], v[68:71], v[192:195]\n"
  "ds_read_b128 a[52:55], v58 offset:29696\n"
  "buffer_load_dwordx4 v60, s[88:91], s77 offen offset:0 sc1 lds\n"
  "v_mfma_f32_16x16x32_bf16 v[196:199], a[12:15], v[68:71], v[196:199]\n"
  "v_exp_f32 v215, v215\n"
  "v_mfma_f32_16x16x32_bf16 v[192:195], a[16:19], v[72:75], v[192:195]\n"
  "ds_read_b128 a[56:59], v58 offset:30720\n"
  "s_add_u32 m0, s29, 3840\n"
  "s_add_u32 s64, s30, 98304\n"
  "buffer_load_dwordx4 v48, s[8:11], s64 offen sc1 lds\n"
  "s_add_u32 m0, m0, 256\n"
  "s_nop 0\n"
  "buffer_load_dwordx4 v49, s[8:11], s64 offen sc1 lds\n"
  "s_add_u32 m0, s78, 0x1000\n"
  "v_cvt_pk_bf16_f32 v211, v214, v215\n"
  "v_mfma_f32_16x16x32_bf16 v[196:199], a[20:23], v[72:75], v[196:199]\n"
  "v_exp_f32 v216, v216\n"
  "v_mfma_f32_16x16x32_bf16 v[192:195], a[24:27], v[76:79], v[192:195]\n"
  "ds_read_b128 a[60:63], v58 offset:31744\n"
  "v_mfma_f32_16x16x32_bf16 v[196:199], a[28:31], v[76:79], v[196:199]\n"
  "v_exp_f32 v217, v217\n"
  "v_mfma_f32_16x16x32_bf16 v[200:203], a[0:3], v[80:83], v[248:251]\n"
  "ds_read_b128 a[0:3], v57 offset:40960\n"
  "v_cvt_pk_bf16_f32 v216, v216, v217\n"
  "v_mfma_f32_16x16x32_bf16 v[204:207], a[4:7], v[80:83], v[252:255]\n"
  "v_exp_f32 v218, v218\n"
  "v_mfma_f32_16x16x32_bf16 v[200:203], a[8:11], v[84:87], v[200:203]\n"
  "ds_read_b128 a[4:7], v57 offset:41984\n"
  "buffer_load_dwordx4 v60, s[88:91], s77 offen offset:1024 sc1 lds\n"
  "v_add_f32 v16, 0xf4c00000, v63\n"
  "v_min_f32 v16, v45, v16\n"
  "v_add_f32 v17, 0xf4c20000, v63\n"
  "v_min_f32 v17, v45, v17\n"
  "v_mfma_f32_16x16x32_bf16 v[204:207], a[12:15], v[84:87], v[204:207]\n"
  "v_exp_f32 v219, v219\n"
  "v_mfma_f32_16x16x32_bf16 v[200:203], a[16:19], v[88:91], v[200:203]\n"
  "ds_read_b128 a[8:11], v57 offset:43008\n"
  "s_add_u32 m0, s29, 5120\n"
  "s_add_u32 s64, s30, 131072\n"
  "buffer_load_dwordx4 v48, s[8:11], s64 offen sc1 lds\n"
  "s_add_u32 m0, m0, 256\n"
  "s_nop 0\n"
  "buffer_load_dwordx4 v49, s[8:11], s64 offen sc1 lds\n"
  "s_add_u32 m0, s78, 0x1000\n"
  "v_cvt_pk_bf16_f32 v217, v218, v219\n"
  "v_add_f32 v18, 0xf4c40000, v63\n"
  "v_min_f32 v18, v45, v18\n"
  "v_add_f32 v19, 0xf4c60000, v63\n"
  "v_mfma_f32_16x16x32_bf16 v[204:207], a[20:23], v[88:91], v[204:207]\n"
  "v_exp_f32 v220, v220\n"
  "v_mfma_f32_16x16x32_bf16 v[200:203], a[24:27], v[92:95], v[200:203]\n"
  "ds_read_b128 a[12:15], v57 offset:44032\n"
  "s_waitcnt lgkmcnt(4)\n"
  "v_min_f32 v19, v45, v19\n"
  "v_add_f32 v20, 0xf4e00000, v63\n"
  "v_min_f32 v20, v45, v20\n"
  "v_mfma_f32_16x16x32_bf16 v[204:207], a[28:31], v[92:95], v[204:207]\n"
  "v_exp_f32 v221, v221\n"
  "v_mfma_f32_16x16x32_bf16 v[128:131], a[32:35], v[208:211], v[128:131]\n"
  "ds_read_b128 a[16:19], v57 offset:45056\n"
  "v_cvt_pk_bf16_f32 v218, v220, v221\n"
  "v_add_f32 v21, 0xf4e20000, v63\n"
  "v_min_f32 v21, v45, v21\n"
  "v_add_f32 v22, 0xf4e40000, v63\n"
  "v_mfma_f32_16x16x32_bf16 v[132:135], a[36:39], v[208:211], v[132:135]\n"
  "v_exp_f32 v222, v222\n"
  "v_mfma_f32_16x16x32_bf16 v[136:139], a[40:43], v[208:211], v[136:139]\n"
  "ds_read_b128 a[20:23], v57 offset:46080\n"
  "buffer_load_dwordx4 v60, s[88:91], s77 offen offset:2048 sc1 lds\n"
  "v_min_f32 v22, v45, v22\n"
  "v_add_f32 v23, 0xf4e60000, v63\n"
  "v_min_f32 v23, v45, v23\n"
  "v_mfma_f32_16x16x32_bf16 v[140:143], a[44:47], v[208:211], v[140:143]\n"
  "v_exp_f32 v223, v223\n"
  "v_mfma_f32_16x16x32_bf16 v[144:147], a[48:51], v[208:211], v[144:147]\n"
  "ds_read_b128 a[24:27], v57 offset:47104\n"
  "s_add_u32 m0, s29, 6400\n"
  "s_add_u32 s64, s30, 163840\n"
  "buffer_load_dwordx4 v48, s[8:11], s64 offen sc1 lds\n"
  "s_add_u32 m0, m0, 256\n"
  "s_nop 0\n"
  "buffer_load_dwordx4 v49, s[8:11], s64 offen sc1 lds\n"
  "s_add_u32 m0, s78, 0x1000\n"
  "v_cvt_pk_bf16_f32 v219, v222, v223\n"
  "v_add_f32 v24, 0xf4a00000, v63\n"
  "v_min_f32 v24, v62, v24\n"
  "v_add_f32 v25, 0xf4a20000, v63\n"
  "v_min_f32 v25, v62, v25\n"
  "v_mfma_f32_16x16x32_bf16 v[148:151], a[52:55], v[208:211], v[148:151]\n"
  "v_exp_f32 v192, v192\n"
  "v_mfma_f32_16x16x32_bf16 v[152:155], a[56:59], v[208:211], v[152:155]\n"
  "ds_read_b128 a[28:31], v57 offset:48128\n"
  "s_add_u32 s64, s96, 0\n"
  "buffer_load_dwordx4 a[64:67], v59, s[8:11], s64 offen offset:0 sc1\n"
  "v_add_f32 v26, 0xf4a40000, v63\n"
  "v_min_f32 v26, v62, v26\n"
  "v_add_f32 v27, 0xf4a60000, v63\n"
  "v_mfma_f32_16x16x32_bf16 v[156:159], a[60:63], v[208:211], v[156:159]\n"
  "v_exp_f32 v193, v193\n"
  "v_mfma_f32_16x16x32_bf16 v[0:3], v[38:41], v[208:211], v[0:3]\n"
  "s_add_u32 s78, s78, 0x8000\n"
  "s_and_b32 s78, s78, 0x1ffff\n"
  "v_mfma_f32_16x16x32_bf16 v[160:163], a[32:35], v[216:219], v[160:163]\n"
  "ds_read_b128 a[32:35], v58 offset:49152\n"
  "v_cvt_pk_bf16_f32 v192, v192, v193\n"
  "v_min_f32 v27, v62, v27\n"
  "v_add_f32 v28, 0xf4c00000, v63\n"
  "v_min_f32 v28, v62, v28\n"
  "v_mfma_f32_16x16x32_bf16 v[164:167], a[36:39], v[216:219], v[164:167]\n"
  "v_exp_f32 v194, v194\n"
  "v_mfma_f32_16x16x32_bf16 v[168:171], a[40:43], v[216:219], v[168:171]\n"
  "ds_read_b128 a[36:39], v58 offset:50176\n"
  "buffer_load_dwordx4 v60, s[88:91], s77 offen offset:3072 sc1 lds\n"
  "buffer_load_dwordx4 a[68:71], v59, s[8:11], s64 offen offset:1024 sc1\n"
  "v_add_f32 v29, 0xf4c20000, v63\n"
  "v_min_f32 v29, v62, v29\n"
  "v_add_f32 v30, 0xf4c40000, v63\n"
  "v_mfma_f32_16x16x32_bf16 v[172:175], a[44:47], v[216:219], v[172:175]\n"
  "v_exp_f32 v195, v195\n"
  "v_mfma_f32_16x16x32_bf16 v[176:179], a[48:51], v[216:219], v[176:179]\n"
  "ds_read_b128 a[40:43], v58 offset:51200\n"
  "v_cvt_pk_bf16_f32 v193, v194, v195\n"
  "v_min_f32 v30, v62, v30\n"
  "v_add_f32 v31, 0xf4c60000, v63\n"
  "v_min_f32 v31, v62, v31\n"
  "v_mfma_f32_16x16x32_bf16 v[180:183], a[52:55], v[216:219], v[180:183]\n"
  "v_exp_f32 v196, v196\n"
  "v_mfma_f32_16x16x32_bf16 v[184:187], a[56:59], v[216:219], v[184:187]\n"
  "ds_read_b128 a[44:47], v58 offset:52224\n"
  "buffer_load_dwordx4 a[72:75], v59, s[8:11], s64 offen offset:2048 sc1\n"
  "v_xor_b32 v57, 0x10000, v57\n"
  "v_mfma_f32_16x16x32_bf16 v[188:191], a[60:63], v[216:219], v[188:191]\n"
  "v_exp_f32 v197, v197\n"
  "v_mfma_f32_16x16x32_bf16 v[50:53], v[38:41], v[216:219], v[50:53]\n"
  "s_waitcnt lgkmcnt(4)\n"
  "v_mfma_f32_16x16x32_bf16 v[208:211], a[0:3], v[64:67], v[16:19]\n"
  "ds_read_b128 a[48:51], v58 offset:53248\n"
  "v_cvt_pk_bf16_f32 v194, v196, v197\n"
  "v_mfma_f32_16x16x32_bf16 v[212:215], a[4:7], v[64:67], v[20:23]\n"
  "v_exp_f32 v198, v198\n"
  "v_mfma_f32_16x16x32_bf16 v[208:211], a[8:11], v[68:71], v[208:211]\n"
  "ds_read_b128 a[52:55], v58 offset:54272\n"
  "buffer_load_dwordx4 a[76:79], v59, s[8:11], s64 offen offset:3072 sc1\n"
  "v_readlane_b32 s77, v61, s76\n"
  "v_mfma_f32_16x16x32_bf16 v[212:215], a[12:15], v[68:71], v[212:215]\n"
  "v_exp_f32 v199, v199\n"
  "v_mfma_f32_16x16x32_bf16 v[208:211], a[16:19], v[72:75], v[208:211]\n"
  "ds_read_b128 a[56:59], v58 offset:55296\n"
  "s_mov_b32 m0, s78\n"
  "v_cvt_pk_bf16_f32 v195, v198, v199\n"
  "v_mfma_f32_16x16x32_bf16 v[212:215], a[20:23], v[72:75], v[212:215]\n"
  "v_exp_f32 v200, v200\n"
  "v_mfma_f32_16x16x32_bf16 v[208:211], a[24:27], v[76:79], v[208:211]\n"
  "ds_read_b128 a[60:63], v58 offset:56320\n"
  "s_waitcnt vmcnt(29)\n"
  "s_barrier\n"
  "v_mfma_f32_16x16x32_bf16 v[212:215], a[28:31], v[76:79], v[212:215]\n"
  "v_exp_f32 v201, v201\n"
  "v_mfma_f32_16x16x32_bf16 v[216:219], a[0:3], v[80:83], v[24:27]\n"
  "ds_read_b128 a[0:3], v57 offset:0\n"
  "s_add_u32 s64, s96, 4096\n"
  "buffer_load_dwordx4 a[80:83], v59, s[8:11], s64 offen offset:0 sc1\n"
  "v_cvt_pk_bf16_f32 v200, v200, v201\n"
  "v_mfma_f32_16x16x32_bf16 v[220:223], a[4:7], v[80:83], v[28:31]\n"
  "v_exp_f32 v202, v202\n"
  "v_mfma_f32_16x16x32_bf16 v[216:219], a[8:11], v[84:87], v[216:219]\n"
  "ds_read_b128 a[4:7], v57 offset:1024\n"
  "buffer_load_dwordx4 v59, s[88:91], s77 offen offset:0 sc1 lds\n"
  "v_mfma_f32_16x16x32_bf16 v[220:223], a[12:15], v[84:87], v[220:223]\n"
  "v_exp_f32 v203, v203\n"
  "v_mfma_f32_16x16x32_bf16 v[216:219], a[16:19], v[88:91], v[216:219]\n"
  "ds_read_b128 a[8:11], v57 offset:2048\n"
  "buffer_load_dwordx4 a[84:87], v59, s[8:11], s64 offen offset:1024 sc1\n"
  "v_cvt_pk_bf16_f32 v201, v202, v203\n"
  "v_mfma_f32_16x16x32_bf16 v[220:223], a[20:23], v[88:91], v[220:223]\n"
  "v_exp_f32 v204, v204\n"
  "v_mfma_f32_16x16x32_bf16 v[216:219], a[24:27], v[92:95], v[216:219]\n"
  "ds_read_b128 a[12:15], v57 offset:3072\n"
  "s_waitcnt lgkmcnt(4)\n"
  "v_mfma_f32_16x16x32_bf16 v[220:223], a[28:31], v[92:95], v[220:223]\n"
  "v_exp_f32 v205, v205\n"
  "v_mfma_f32_16x16x32_bf16 v[128:131], a[32:35], v[192:195], v[128:131]\n"
  "ds_read_b128 a[16:19], v57 offset:4096\n"
  "buffer_load_dwordx4 a[88:91], v59, s[8:11], s64 offen offset:2048 sc1\n"
  "v_cvt_pk_bf16_f32 v202, v204, v205\n"
  "v_mfma_f32_16x16x32_bf16 v[132:135], a[36:39], v[192:195], v[132:135]\n"
  "v_exp_f32 v206, v206\n"
  "v_mfma_f32_16x16x32_bf16 v[136:139], a[40:43], v[192:195], v[136:139]\n"
  "ds_read_b128 a[20:23], v57 offset:5120\n"
  "buffer_load_dwordx4 v59, s[88:91], s77 offen offset:1024 sc1 lds\n"
  "v_mfma_f32_16x16x32_bf16 v[140:143], a[44:47], v[192:195], v[140:143]\n"
  "v_exp_f32 v207, v207\n"
  "v_mfma_f32_16x16x32_bf16 v[144:147], a[48:51], v[192:195], v[144:147]\n"
  "ds_read_b128 a[24:27], v57 offset:6144\n"
  "buffer_load_dwordx4 a[92:95], v59, s[8:11], s64 offen offset:3072 sc1\n"
  "v_cvt_pk_bf16_f32 v203, v206, v207\n"
  "v_mfma_f32_16x16x32_bf16 v[148:151], a[52:55], v[192:195], v[148:151]\n"
  "v_exp_f32 v208, v208\n"
  "v_mfma_f32_16x16x32_bf16 v[152:155], a[56:59], v[192:195], v[152:155]\n"
  "ds_read_b128 a[28:31], v57 offset:7168\n"
  "buffer_load_dwordx4 v59, s[88:91], s77 offen offset:2048 sc1 lds\n"
  "v_mfma_f32_16x16x32_bf16 v[156:159], a[60:63], v[192:195], v[156:159]\n"
  "v_exp_f32 v209, v209\n"
  "v_mfma_f32_16x16x32_bf16 v[0:3], v[38:41], v[192:195], v[0:3]\n"
  "s_add_u32 s76, s76, 1\n"
  "v_mfma_f32_16x16x32_bf16 v[160:163], a[32:35], v[200:203], v[160:163]\n"
  "ds_read_b128 a[32:35], v58 offset:57344\n"
  "v_cvt_pk_bf16_f32 v208, v208, v209\n"
  "v_mfma_f32_16x16x32_bf16 v[164:167], a[36:39], v[200:203], v[164:167]\n"
  "v_exp_f32 v210, v210\n"
  "v_mfma_f32_16x16x32_bf16 v[168:171], a[40:43], v[200:203], v[168:171]\n"
  "ds_read_b128 a[36:39], v58 offset:58368\n"
  "buffer_load_dwordx4 v59, s[88:91], s77 offen offset:3072 sc1 lds\n"
  "s_add_u32 s64, s96, 8192\n"
  "buffer_load_dwordx4 a[96:99], v59, s[8:11], s64 offen offset:0 sc1\n"
  "v_mfma_f32_16x16x32_bf16 v[172:175], a[44:47], v[200:203], v[172:175]\n"
  "v_exp_f32 v211, v211\n"
  "v_mfma_f32_16x16x32_bf16 v[176:179], a[48:51], v[200:203], v[176:179]\n"
  "ds_read_b128 a[40:43], v58 offset:59392\n"
  "v_cvt_pk_bf16_f32 v209, v210, v211\n"
  "v_mfma_f32_16x16x32_bf16 v[180:183], a[52:55], v[200:203], v[180:183]\n"
  "v_exp_f32 v212, v212\n"
  "v_mfma_f32_16x16x32_bf16 v[184:187], a[56:59], v[200:203], v[184:187]\n"
  "ds_read_b128 a[44:47], v58 offset:60416\n"
  "s_add_u32 m0, s78, 0x1000\n"
  "buffer_load_dwordx4 a[100:103], v59, s[8:11], s64 offen offset:1024 sc1\n"
  "v_mfma_f32_16x16x32_bf16 v[188:191], a[60:63], v[200:203], v[188:191]\n"
  "v_exp_f32 v213, v213\n"
  "v_mfma_f32_16x16x32_bf16 v[50:53], v[38:41], v[200:203], v[50:53]\n"
  "s_waitcnt lgkmcnt(4)\n"
  "s_add_u32 m0, s78, 0x1000\n"
  "ds_read_b128 a[48:51], v58 offset:61440\n"
  "v_cvt_pk_bf16_f32 v210, v212, v213\n"
  "v_exp_f32 v214, v214\n"
  "ds_read_b128 a[52:55], v58 offset:62464\n"
  "buffer_load_dwordx4 a[104:107], v59, s[8:11], s64 offen offset:2048 sc1\n"
  "v_exp_f32 v215, v215\n"
  "ds_read_b128 a[56:59], v58 offset:63488\n"
  "v_cvt_pk_bf16_f32 v211, v214, v215\n"
  "v_exp_f32 v216, v216\n"
  "ds_read_b128 a[60:63], v58 offset:64512\n"
  "buffer_load_dwordx4 a[108:111], v59, s[8:11], s64 offen offset:3072 sc1\n"
  "v_exp_f32 v217, v217\n"
  "buffer_load_dwordx4 v60, s[88:91], s77 offen offset:0 sc1 lds\n"
  "v_cvt_pk_bf16_f32 v216, v216, v217\n"
  "v_exp_f32 v218, v218\n"
  "buffer_load_dwordx4 v60, s[88:91], s77 offen offset:1024 sc1 lds\n"
  "v_exp_f32 v219, v219\n"
  "buffer_load_dwordx4 v60, s[88:91], s77 offen offset:2048 sc1 lds\n"
  "s_add_u32 s64, s96, 12288\n"
  "buffer_load_dwordx4 a[112:115], v59, s[8:11], s64 offen offset:0 sc1\n"
  "v_cvt_pk_bf16_f32 v217, v218, v219\n"
  "v_exp_f32 v220, v220\n"
  "buffer_load_dwordx4 v60, s[88:91], s77 offen offset:3072 sc1 lds\n"
  "v_exp_f32 v221, v221\n"
  "s_add_u32 s78, s78, 0x8000\n"
  "buffer_load_dwordx4 a[116:119], v59, s[8:11], s64 offen offset:1024 sc1\n"
  "v_cvt_pk_bf16_f32 v218, v220, v221\n"
  "v_exp_f32 v222, v222\n"
  "s_and_b32 s78, s78, 0x1ffff\n"
  "v_exp_f32 v223, v223\n"
  "v_xor_b32 v58, 0x10000, v58\n"
  "v_cvt_pk_bf16_f32 v219, v222, v223\n"
  "s_mov_b32 s79, s55\n"
  "s_waitcnt lgkmcnt(0)\n"
  "v_mfma_f32_16x16x32_bf16 v[0:3], v[38:41], v[208:211], v[0:3]\n"
  "v_mfma_f32_16x16x32_bf16 v[50:53], v[38:41], v[216:219], v[50:53]\n"
  "v_mfma_f32_16x16x32_bf16 v[128:131], a[32:35], v[208:211], v[128:131]\n"
  "buffer_load_dwordx4 a[120:123], v59, s[8:11], s64 offen offset:2048 sc1\n"
  "v_swap_b32 v64, v96\n"
  "v_swap_b32 v65, v97\n"
  "v_mfma_f32_16x16x32_bf16 v[132:135], a[36:39], v[208:211], v[132:135]\n"
  "v_swap_b32 v66, v98\n"
  "v_swap_b32 v67, v99\n"
  "v_and_b32 v4, 31, v54\n"
  "v_lshlrev_b32 v4, 7, v4\n"
  "v_mfma_f32_16x16x32_bf16 v[136:139], a[40:43], v[208:211], v[136:139]\n"
  "v_swap_b32 v68, v100\n"
  "v_swap_b32 v69, v101\n"
  "v_mfma_f32_16x16x32_bf16 v[140:143], a[44:47], v[208:211], v[140:143]\n"
  "v_swap_b32 v70, v102\n"
  "v_swap_b32 v71, v103\n"
  "v_mfma_f32_16x16x32_bf16 v[144:147], a[48:51], v[208:211], v[144:147]\n"
  "buffer_load_dwordx4 a[124:127], v59, s[8:11], s64 offen offset:3072 sc1\n"
  "v_swap_b32 v72, v104\n"
  "v_swap_b32 v73, v105\n"
  "v_swap_b32 v74, v106\n"
  "v_mfma_f32_16x16x32_bf16 v[148:151], a[52:55], v[208:211], v[148:151]\n"
  "v_swap_b32 v75, v107\n"
  "v_add_f32 v55, v0, v50\n"
  "v_rcp_f32 v0, v0\n"
  "v_rcp_f32 v50, v50\n"
  "v_mfma_f32_16x16x32_bf16 v[152:155], a[56:59], v[208:211], v[152:155]\n"
  "v_swap_b32 v76, v108\n"
  "v_swap_b32 v77, v109\n"
  "v_mfma_f32_16x16x32_bf16 v[156:159], a[60:63], v[208:211], v[156:159]\n"
  "v_swap_b32 v78, v110\n"
  "v_swap_b32 v79, v111\n"
  "v_cmp_ngt_f32 vcc, 0x5d800000, v55\n"
  "v_mfma_f32_16x16x32_bf16 v[160:163], a[32:35], v[216:219], v[160:163]\n"
  "v_swap_b32 v80, v112\n"
  "v_swap_b32 v81, v113\n"
  "v_mfma_f32_16x16x32_bf16 v[164:167], a[36:39], v[216:219], v[164:167]\n"
  "v_swap_b32 v82, v114\n"
  "v_swap_b32 v83, v115\n"
  "v_mfma_f32_16x16x32_bf16 v[168:171], a[40:43], v[216:219], v[168:171]\n"
  "s_cmp_lg_u64 vcc, 0\n"
  "s_cselect_b32 s58, 1, 0\n"
  "s_lshl_b32 s58, s58, s80\n"
  "s_or_b32 s82, s82, s58\n"
  "v_mfma_f32_16x16x32_bf16 v[172:175], a[44:47], v[216:219], v[172:175]\n"
  "v_swap_b32 v84, v116\n"
  "v_swap_b32 v85, v117\n"
  "v_swap_b32 v86, v118\n"
  "v_swap_b32 v87, v119\n"
  "v_mfma_f32_16x16x32_bf16 v[176:179], a[48:51], v[216:219], v[176:179]\n"
  "buffer_load_dword v5, v4, s[88:91], s75 offen offset:128 sc1\n"
  "v_swap_b32 v88, v120\n"
  "v_mfma_f32_16x16x32_bf16 v[180:183], a[52:55], v[216:219], v[180:183]\n"
  "v_swap_b32 v89, v121\n"
  "v_swap_b32 v90, v122\n"
  "v_swap_b32 v91, v123\n"
  "v_mfma_f32_16x16x32_bf16 v[184:187], a[56:59], v[216:219], v[184:187]\n"
  "v_swap_b32 v92, v124\n"
  "v_swap_b32 v93, v125\n"
  "v_swap_b32 v94, v126\n"
  "v_swap_b32 v95, v127\n"
  "v_cvt_f32_u32 v56, s79\n"
  "v_fmamk_f32 v56, v56, 0x75000000, v63\n"
  "v_add_f32 v192, 0x0, v56\n"
  "v_min_f32 v192, 0, v192\n"
  "v_add_f32 v193, 0xf1800000, v56\n"
  "v_min_f32 v193, 0, v193\n"
  "v_add_f32 v194, 0xf2000000, v56\n"
  "v_min_f32 v194, 0, v194\n"
  "v_add_f32 v195, 0xf2400000, v56\n"
  "v_min_f32 v195, 0, v195\n"
  "v_add_f32 v196, 0xf3800000, v56\n"
  "v_min_f32 v196, 0, v196\n"
  "v_add_f32 v197, 0xf3880000, v56\n"
  "v_min_f32 v197, 0, v197\n"
  "v_add_f32 v198, 0xf3900000, v56\n"
  "v_mfma_f32_16x16x32_bf16 v[188:191], a[60:63], v[216:219], v[188:191]\n"
  "v_min_f32 v198, 0, v198\n"
  "v_add_f32 v199, 0xf3980000, v56\n"
  "v_min_f32 v199, 0, v199\n"
  "v_add_f32 v200, 0x73800000, v56\n"
  "v_min_f32 v200, 0, v200\n"
  "v_add_f32 v201, 0x73700000, v56\n"
  "v_min_f32 v201, 0, v201\n"
  "v_add_f32 v202, 0x73600000, v56\n"
  "v_min_f32 v202, 0, v202\n"
  "v_add_f32 v203, 0x73500000, v56\n"
  "v_min_f32 v203, 0, v203\n"
  "v_add_f32 v204, 0x0, v56\n"
  "v_min_f32 v204, 0, v204\n"
  "v_add_f32 v205, 0xf1800000, v56\n"
  "v_min_f32 v205, 0, v205\n"
  "v_add_f32 v206, 0xf2000000, v56\n"
  "v_min_f32 v206, 0, v206\n"
  "v_add_f32 v207, 0xf2400000, v56\n"
  "v_min_f32 v207, 0, v207\n"
  "s_nop 1\n"
  "v_mfma_f32_16x16x32_bf16 v[192:195], a[0:3], v[64:67], v[192:195]\n"
  "v_mul_f32 v128, v0, v128\n"
  "v_mul_f32 v129, v0, v129\n"
  "v_cvt_pk_bf16_f32 v128, v128, v129\n"
  "v_mul_f32 v130, v0, v130\n"
  "v_mul_f32 v131, v0, v131\n"
  "v_cvt_pk_bf16_f32 v129, v130, v131\n"
  "v_mfma_f32_16x16x32_bf16 v[196:199], a[4:7], v[64:67], v[196:199]\n"
  "v_mul_f32 v132, v0, v132\n"
  "v_mul_f32 v133, v0, v133\n"
  "v_cvt_pk_bf16_f32 v130, v132, v133\n"
  "v_mul_f32 v134, v0, v134\n"
  "v_mul_f32 v135, v0, v135\n"
  "v_cvt_pk_bf16_f32 v131, v134, v135\n"
  "buffer_store_dwordx4 v[128:131], v46, s[88:91], s81 offen offset:0 sc1\n"
  "v_mfma_f32_16x16x32_bf16 v[192:195], a[8:11], v[68:71], v[192:195]\n"
  "v_mul_f32 v160, v50, v160\n"
  "v_mul_f32 v161, v50, v161\n"
  "v_cvt_pk_bf16_f32 v160, v160, v161\n"
  "v_mul_f32 v162, v50, v162\n"
  "v_mul_f32 v163, v50, v163\n"
  "v_cvt_pk_bf16_f32 v161, v162, v163\n"
  "v_mul_f32 v164, v50, v164\n"
  "v_mfma_f32_16x16x32_bf16 v[196:199], a[12:15], v[68:71], v[196:199]\n"
  "v_mul_f32 v165, v50, v165\n"
  "v_cvt_pk_bf16_f32 v162, v164, v165\n"
  "v_mul_f32 v166, v50, v166\n"
  "v_mul_f32 v167, v50, v167\n"
  "v_cvt_pk_bf16_f32 v163, v166, v167\n"
  "buffer_store_dwordx4 v[160:163], v46, s[88:91], s81 offen offset:1024 sc1\n"
  "s_add_u32 s81, s81, 0x1000\n"
  "v_mfma_f32_16x16x32_bf16 v[192:195], a[16:19], v[72:75], v[192:195]\n"
  "v_mul_f32 v136, v0, v136\n"
  "v_mul_f32 v137, v0, v137\n"
  "v_cvt_pk_bf16_f32 v136, v136, v137\n"
  "v_mul_f32 v138, v0, v138\n"
  "v_mul_f32 v139, v0, v139\n"
  "v_cvt_pk_bf16_f32 v137, v138, v139\n"
  "v_mfma_f32_16x16x32_bf16 v[196:199], a[20:23], v[72:75], v[196:199]\n"
  "v_mul_f32 v140, v0, v140\n"
  "v_mul_f32 v141, v0, v141\n"
  "v_cvt_pk_bf16_f32 v138, v140, v141\n"
  "v_mul_f32 v142, v0, v142\n"
  "v_mul_f32 v143, v0, v143\n"
  "v_cvt_pk_bf16_f32 v139, v142, v143\n"
  "buffer_store_dwordx4 v[136:139], v46, s[88:91], s81 offen offset:0 sc1\n"
  "v_mfma_f32_16x16x32_bf16 v[192:195], a[24:27], v[76:79], v[192:195]\n"
  "v_mul_f32 v168, v50, v168\n"
  "v_mul_f32 v169, v50, v169\n"
  "v_cvt_pk_bf16_f32 v168, v168, v169\n"
  "v_mul_f32 v170, v50, v170\n"
  "v_mul_f32 v171, v50, v171\n"
  "v_cvt_pk_bf16_f32 v169, v170, v171\n"
  "v_mul_f32 v172, v50, v172\n"
  "v_mfma_f32_16x16x32_bf16 v[196:199], a[28:31], v[76:79], v[196:199]\n"
  "v_mul_f32 v173, v50, v173\n"
  "v_cvt_pk_bf16_f32 v170, v172, v173\n"
  "v_mul_f32 v174, v50, v174\n"
  "v_mul_f32 v175, v50, v175\n"
  "v_cvt_pk_bf16_f32 v171, v174, v175\n"
  "buffer_store_dwordx4 v[168:171], v46, s[88:91], s81 offen offset:1024 sc1\n"
  "s_add_u32 s81, s81, 0x1000\n"
  "v_mfma_f32_16x16x32_bf16 v[200:203], a[0:3], v[80:83], v[200:203]\n"
  "ds_read_b128 a[0:3], v57 offset:8192\n"
  "v_mul_f32 v144, v0, v144\n"
  "v_mul_f32 v145, v0, v145\n"
  "v_cvt_pk_bf16_f32 v144, v144, v145\n"
  "v_mul_f32 v146, v0, v146\n"
  "v_mul_f32 v147, v0, v147\n"
  "v_cvt_pk_bf16_f32 v145, v146, v147\n"
  "v_mfma_f32_16x16x32_bf16 v[204:207], a[4:7], v[80:83], v[204:207]\n"
  "ds_read_b128 a[4:7], v57 offset:9216\n"
  "v_mul_f32 v148, v0, v148\n"
  "v_mul_f32 v149, v0, v149\n"
  "v_cvt_pk_bf16_f32 v146, v148, v149\n"
  "v_mul_f32 v150, v0, v150\n"
  "v_mul_f32 v151, v0, v151\n"
  "v_cvt_pk_bf16_f32 v147, v150, v151\n"
  "buffer_store_dwordx4 v[144:147], v46, s[88:91], s81 offen offset:0 sc1\n"
  "v_mfma_f32_16x16x32_bf16 v[200:203], a[8:11], v[84:87], v[200:203]\n"
  "ds_read_b128 a[8:11], v57 offset:10240\n"
  "v_mul_f32 v176, v50, v176\n"
  "v_mul_f32 v177, v50, v177\n"
  "v_cvt_pk_bf16_f32 v176, v176, v177\n"
  "v_mul_f32 v178, v50, v178\n"
  "v_mul_f32 v179, v50, v179\n"
  "v_cvt_pk_bf16_f32 v177, v178, v179\n"
  "v_mul_f32 v180, v50, v180\n"
  "v_mfma_f32_16x16x32_bf16 v[204:207], a[12:15], v[84:87], v[204:207]\n"
  "ds_read_b128 a[12:15], v57 offset:11264\n"
  "v_mul_f32 v181, v50, v181\n"
  "v_cvt_pk_bf16_f32 v178, v180, v181\n"
  "v_mul_f32 v182, v50, v182\n"
  "v_mul_f32 v183, v50, v183\n"
  "v_cvt_pk_bf16_f32 v179, v182, v183\n"
  "buffer_store_dwordx4 v[176:179], v46, s[88:91], s81 offen offset:1024 sc1\n"
  "s_add_u32 s81, s81, 0x1000\n"
  "v_mfma_f32_16x16x32_bf16 v[200:203], a[16:19], v[88:91], v[200:203]\n"
  "ds_read_b128 a[16:19], v57 offset:12288\n"
  "v_mul_f32 v152, v0, v152\n"
  "v_mul_f32 v153, v0, v153\n"
  "v_cvt_pk_bf16_f32 v152, v152, v153\n"
  "v_mul_f32 v154, v0, v154\n"
  "v_mul_f32 v155, v0, v155\n"
  "v_cvt_pk_bf16_f32 v153, v154, v155\n"
  "v_mfma_f32_16x16x32_bf16 v[204:207], a[20:23], v[88:91], v[204:207]\n"
  "ds_read_b128 a[20:23], v57 offset:13312\n"
  "v_mul_f32 v156, v0, v156\n"
  "v_mul_f32 v157, v0, v157\n"
  "v_cvt_pk_bf16_f32 v154, v156, v157\n"
  "v_mul_f32 v158, v0, v158\n"
  "v_mul_f32 v159, v0, v159\n"
  "v_cvt_pk_bf16_f32 v155, v158, v159\n"
  "buffer_store_dwordx4 v[152:155], v46, s[88:91], s81 offen offset:0 sc1\n"
  "v_mfma_f32_16x16x32_bf16 v[200:203], a[24:27], v[92:95], v[200:203]\n"
  "ds_read_b128 a[24:27], v57 offset:14336\n"
  "v_mul_f32 v184, v50, v184\n"
  "v_mul_f32 v185, v50, v185\n"
  "v_cvt_pk_bf16_f32 v184, v184, v185\n"
  "v_mul_f32 v186, v50, v186\n"
  "v_mul_f32 v187, v50, v187\n"
  "v_cvt_pk_bf16_f32 v185, v186, v187\n"
  "v_mul_f32 v188, v50, v188\n"
  "v_mfma_f32_16x16x32_bf16 v[204:207], a[28:31], v[92:95], v[204:207]\n"
  "ds_read_b128 a[28:31], v57 offset:15360\n"
  "v_mul_f32 v189, v50, v189\n"
  "v_cvt_pk_bf16_f32 v186, v188, v189\n"
  "v_mul_f32 v190, v50, v190\n"
  "v_mul_f32 v191, v50, v191\n"
  "v_cvt_pk_bf16_f32 v187, v190, v191\n"
  "buffer_store_dwordx4 v[184:187], v46, s[88:91], s81 offen offset:1024 sc1\n"
  "s_add_u32 s81, s81, 0x1000\n"
  "s_waitcnt vmcnt(8)\n"
  "v_cmp_ne_u32 vcc, 8, v5\n"
  "s_cmp_eq_u64 vcc, 0\n"
  "s_cbranch_scc0 e_spin%=\n"
  "e_ok%=:\n"
  "s_mov_b32 s10, s90\n"
  "s_lshl_b32 s81, s55, 20\n"
  "s_add_u32 s81, s81, s68\n"
  "s_add_u32 s80, s80, 1\n"
  "s_cmp_eq_u32 s80, 1\n"
  "s_cbranch_scc1 at_head%=\n"
  "s_waitcnt lgkmcnt(0)\n"
  "s_barrier\n"
  "s_cmp_eq_u32 s82, 0\n"
  "s_cbranch_scc1 at_end%=\n"
  "v_lshlrev_b32 v55, 4, v54\n"
  "s_lshl2_add_u32 s58, %2, s20\n"
  "s_lshl_b32 s58, s58, 14\n"
  "s_add_u32 s58, s58, 88080384\n"
  "v_add_u32 v55, s58, v55\n"
  "global_store_dwordx4 v55, v[64:67], s[88:89]\n"
  "v_add_u32 v55, 0x400, v55\n"
  "global_store_dwordx4 v55, v[68:71], s[88:89]\n"
  "v_add_u32 v55, 0x400, v55\n"
  "global_store_dwordx4 v55, v[72:75], s[88:89]\n"
  "v_add_u32 v55, 0x400, v55\n"
  "global_store_dwordx4 v55, v[76:79], s[88:89]\n"
  "v_add_u32 v55, 0x400, v55\n"
  "global_store_dwordx4 v55, v[80:83], s[88:89]\n"
  "v_add_u32 v55, 0x400, v55\n"
  "global_store_dwordx4 v55, v[84:87], s[88:89]\n"
  "v_add_u32 v55, 0x400, v55\n"
  "global_store_dwordx4 v55, v[88:91], s[88:89]\n"
  "v_add_u32 v55, 0x400, v55\n"
  "global_store_dwordx4 v55, v[92:95], s[88:89]\n"
  "v_add_u32 v55, 0x400, v55\n"
  "global_store_dwordx4 v55, v[96:99], s[88:89]\n"
  "v_add_u32 v55, 0x400, v55\n"
  "global_store_dwordx4 v55, v[100:103], s[88:89]\n"
  "v_add_u32 v55, 0x400, v55\n"
  "global_store_dwordx4 v55, v[104:107], s[88:89]\n"
  "v_add_u32 v55, 0x400, v55\n"
  "global_store_dwordx4 v55, v[108:111], s[88:89]\n"
  "v_add_u32 v55, 0x400, v55\n"
  "global_store_dwordx4 v55, v[112:115], s[88:89]\n"
  "v_add_u32 v55, 0x400, v55\n"
  "global_store_dwordx4 v55, v[116:119], s[88:89]\n"
  "v_add_u32 v55, 0x400, v55\n"
  "global_store_dwordx4 v55, v[120:123], s[88:89]\n"
  "v_add_u32 v55, 0x400, v55\n"
  "global_store_dwordx4 v55, v[124:127], s[88:89]\n"
  "v_add_u32 v55, 0x400, v55\n"
  "s_waitcnt vmcnt(0)\n"
  "s_branch at_end%=\n"
  "z_spin%=:\n"
  "s_add_u32 s83, s83, 1\n"
  "s_cmp_gt_u32 s83, 0x40000\n"
  "s_cbranch_scc1 z_ok%=\n"
  "buffer_atomic_add v7, v6, s[92:95], s69 offen sc1\n"
  "s_sleep 2\n"
  "buffer_load_dword v62, off, s[88:91], s69 sc1\n"
  "s_waitcnt vmcnt(0)\n"
  "v_readfirstlane_b32 s58, v62\n"
  "s_cmp_lg_u32 s58, s21\n"
  "s_cbranch_scc0 z_spin%=\n"
  "s_branch z_ok%=\n"
  "p_spin%=:\n"
  "s_add_u32 s83, s83, 1\n"
  "s_cmp_gt_u32 s83, 0x40000\n"
  "s_cbranch_scc1 p_ok%=\n"
  "buffer_atomic_add v7, v6, s[92:95], s69 offen sc1\n"
  "s_sleep 2\n"
  "buffer_load_dword v62, v4, s[88:91], s69 offen offset:128 sc1\n"
  "s_waitcnt vmcnt(0)\n"
  "v_cmp_ne_u32 vcc, v62, v5\n"
  "s_cmp_eq_u64 vcc, 0\n"
  "s_cbranch_scc0 p_spin%=\n"
  "s_branch p_ok%=\n"
  "at_ev%=:\n"
  "s_sub_u32 s97, s97, 1\n"
  "s_cmp_eq_u32 s97, 97\n"
  "s_cbranch_scc1 at_e2%=\n"
  "s_cmp_eq_u32 s97, 96\n"
  "s_cbranch_scc1 at_e3%=\n"
  "s_cmp_eq_u32 s97, 95\n"
  "s_cbranch_scc1 at_e4%=\n"
  "s_cmp_eq_u32 s97, 94\n"
  "s_cbranch_scc1 at_e5%=\n"
  "s_cmp_eq_u32 s97, 93\n"
  "s_cbranch_scc1 at_e6%=\n"
  "v_lshrrev_b32 v8, 3, v54\n"
  "v_and_b32 v9, 7, v54\n"
  "v_mul_u32_u24 v8, 1280, v8\n"
  "v_lshl_add_u32 v8, v9, 4, v8\n"
  "s_add_u32 m0, s43, 0\n"
  "s_add_u32 s64, s38, 0\n"
  "buffer_load_dwordx4 v8, s[8:11], s64 offen offset:0 lds\n"
  "s_add_u32 s64, s38, 9216\n"
  "buffer_load_dwordx4 v8, s[8:11], s64 offen offset:1024 lds\n"
  "s_add_u32 s64, s38, 18432\n"
  "buffer_load_dwordx4 v8, s[8:11], s64 offen offset:2048 lds\n"
  "s_add_u32 s64, s38, 27648\n"
  "buffer_load_dwordx4 v8, s[8:11], s64 offen offset:3072 lds\n"
  "s_add_u32 m0, s43, 4096\n"
  "s_add_u32 s64, s39, 0\n"
  "buffer_load_dwordx4 v8, s[12:15], s64 offen offset:0 lds\n"
  "s_add_u32 s64, s39, 9216\n"
  "buffer_load_dwordx4 v8, s[12:15], s64 offen offset:1024 lds\n"
  "s_add_u32 s64, s39, 18432\n"
  "buffer_load_dwordx4 v8, s[12:15], s64 offen offset:2048 lds\n"
  "s_add_u32 s64, s39, 27648\n"
  "buffer_load_dwordx4 v8, s[12:15], s64 offen offset:3072 lds\n"
  "v_lshrrev_b32 v9, 2, v54\n"
  "v_and_b32 v10, 3, v54\n"
  "v_lshl_add_u32 v10, v10, 3, s43\n"
  "v_lshl_add_u32 v9, v9, 7, v10\n"
  "s_cmp_eq_u32 s20, 0\n"
  "s_cselect_b32 s18, 0, 512\n"
  "s_cselect_b32 s19, 0x800, s18\n"
  "v_lshrrev_b32 v10, 4, v54\n"
  "v_mad_u32_u24 v11, v10, s18, v9\n"
  "v_add_u32 v12, s19, v11\n"
  "s_branch at_loop%=\n"
  "at_e2%=:\n"
  "ds_read_b64_tr_b16 a[64:65], v9 offset:0\n"
  "ds_read_b64_tr_b16 a[66:67], v9 offset:2048\n"
  "ds_read_b64_tr_b16 a[68:69], v9 offset:32\n"
  "ds_read_b64_tr_b16 a[70:71], v9 offset:2080\n"
  "ds_read_b64_tr_b16 a[72:73], v9 offset:64\n"
  "ds_read_b64_tr_b16 a[74:75], v9 offset:2112\n"
  "ds_read_b64_tr_b16 a[76:77], v9 offset:96\n"
  "ds_read_b64_tr_b16 a[78:79], v9 offset:2144\n"
  "ds_read_b64_tr_b16 a[80:81], v11 offset:4096\n"
  "ds_read_b64_tr_b16 a[82:83], v12 offset:4096\n"
  "ds_read_b64_tr_b16 a[84:85], v11 offset:4128\n"
  "ds_read_b64_tr_b16 a[86:87], v12 offset:4128\n"
  "ds_read_b64_tr_b16 a[88:89], v11 offset:4160\n"
  "ds_read_b64_tr_b16 a[90:91], v12 offset:4160\n"
  "ds_read_b64_tr_b16 a[92:93], v11 offset:4192\n"
  "ds_read_b64_tr_b16 a[94:95], v12 offset:4192\n"
  "s_waitcnt lgkmcnt(0)\n"
  "s_lshl2_add_u32 s18, s16, 0\n"
  "s_mul_i32 s19, s18, 205\n"
  "s_lshr_b32 s19, s19, 10\n"
  "s_mul_i32 s47, s19, 2\n"
  "s_sub_u32 s47, s44, s47\n"
  "s_and_b32 s47, s47, 15\n"
  "s_andn2_b32 s48, s44, 15\n"
  "s_or_b32 s47, s47, s48\n"
  "s_mul_i32 s47, s47, 5\n"
  "s_add_u32 s18, s18, s47\n"
  "s_mul_i32 s19, s19, 635\n"
  "s_add_u32 s18, s18, s19\n"
  "s_lshl_b32 s19, s18, 10\n"
  "s_add_u32 s19, s19, s40\n"
  "buffer_store_dwordx4 a[64:67], v46, s[88:91], s19 offen sc1\n"
  "s_lshl2_add_u32 s18, s17, 0\n"
  "s_mul_i32 s19, s18, 205\n"
  "s_lshr_b32 s19, s19, 10\n"
  "s_mul_i32 s47, s19, s46\n"
  "s_sub_u32 s47, s45, s47\n"
  "s_and_b32 s47, s47, 15\n"
  "s_andn2_b32 s48, s45, 15\n"
  "s_or_b32 s47, s47, s48\n"
  "s_mul_i32 s47, s47, 5\n"
  "s_add_u32 s18, s18, s47\n"
  "s_mul_i32 s19, s19, s42\n"
  "s_add_u32 s18, s18, s19\n"
  "s_lshl_b32 s19, s18, 10\n"
  "s_add_u32 s19, s19, s41\n"
  "buffer_store_dwordx4 a[80:83], v46, s[88:91], s19 offen sc1\n"
  "s_branch at_loop%=\n"
  "at_e3%=:\n"
  "s_lshl2_add_u32 s18, s16, 1\n"
  "s_mul_i32 s19, s18, 205\n"
  "s_lshr_b32 s19, s19, 10\n"
  "s_mul_i32 s47, s19, 2\n"
  "s_sub_u32 s47, s44, s47\n"
  "s_and_b32 s47, s47, 15\n"
  "s_andn2_b32 s48, s44, 15\n"
  "s_or_b32 s47, s47, s48\n"
  "s_mul_i32 s47, s47, 5\n"
  "s_add_u32 s18, s18, s47\n"
  "s_mul_i32 s19, s19, 635\n"
  "s_add_u32 s18, s18, s19\n"
  "s_lshl_b32 s19, s18, 10\n"
  "s_add_u32 s19, s19, s40\n"
  "buffer_store_dwordx4 a[68:71], v46, s[88:91], s19 offen sc1\n"
  "s_lshl2_add_u32 s18, s17, 1\n"
  "s_mul_i32 s19, s18, 205\n"
  "s_lshr_b32 s19, s19, 10\n"
  "s_mul_i32 s47, s19, s46\n"
  "s_sub_u32 s47, s45, s47\n"
  "s_and_b32 s47, s47, 15\n"
  "s_andn2_b32 s48, s45, 15\n"
  "s_or_b32 s47, s47, s48\n"
  "s_mul_i32 s47, s47, 5\n"
  "s_add_u32 s18, s18, s47\n"
  "s_mul_i32 s19, s19, s42\n"
  "s_add_u32 s18, s18, s19\n"
  "s_lshl_b32 s19, s18, 10\n"
  "s_add_u32 s19, s19, s41\n"
  "buffer_store_dwordx4 a[84:87], v46, s[88:91], s19 offen sc1\n"
  "s_branch at_loop%=\n"
  "at_e4%=:\n"
  "s_lshl2_add_u32 s18, s16, 2\n"
  "s_mul_i32 s19, s18, 205\n"
  "s_lshr_b32 s19, s19, 10\n"
  "s_mul_i32 s47, s19, 2\n"
  "s_sub_u32 s47, s44, s47\n"
  "s_and_b32 s47, s47, 15\n"
  "s_andn2_b32 s48, s44, 15\n"
  "s_or_b32 s47, s47, s48\n"
  "s_mul_i32 s47, s47, 5\n"
  "s_add_u32 s18, s18, s47\n"
  "s_mul_i32 s19, s19, 635\n"
  "s_add_u32 s18, s18, s19\n"
  "s_lshl_b32 s19, s18, 10\n"
  "s_add_u32 s19, s19, s40\n"
  "buffer_store_dwordx4 a[72:75], v46, s[88:91], s19 offen sc1\n"
  "s_lshl2_add_u32 s18, s17, 2\n"
  "s_mul_i32 s19, s18, 205\n"
  "s_lshr_b32 s19, s19, 10\n"
  "s_mul_i32 s47, s19, s46\n"
  "s_sub_u32 s47, s45, s47\n"
  "s_and_b32 s47, s47, 15\n"
  "s_andn2_b32 s48, s45, 15\n"
  "s_or_b32 s47, s47, s48\n"
  "s_mul_i32 s47, s47, 5\n"
  "s_add_u32 s18, s18, s47\n"
  "s_mul_i32 s19, s19, s42\n"
  "s_add_u32 s18, s18, s19\n"
  "s_lshl_b32 s19, s18, 10\n"
  "s_add_u32 s19, s19, s41\n"
  "buffer_store_dwordx4 a[88:91], v46, s[88:91], s19 offen sc1\n"
  "s_branch at_loop%=\n"
  "at_e5%=:\n"
  "s_lshl2_add_u32 s18, s16, 3\n"
  "s_mul_i32 s19, s18, 205\n"
  "s_lshr_b32 s19, s19, 10\n"
  "s_mul_i32 s47, s19, 2\n"
  "s_sub_u32 s47, s44, s47\n"
  "s_and_b32 s47, s47, 15\n"
  "s_andn2_b32 s48, s44, 15\n"
  "s_or_b32 s47, s47, s48\n"
  "s_mul_i32 s47, s47, 5\n"
  "s_add_u32 s18, s18, s47\n"
  "s_mul_i32 s19, s19, 635\n"
  "s_add_u32 s18, s18, s19\n"
  "s_lshl_b32 s19, s18, 10\n"
  "s_add_u32 s19, s19, s40\n"
  "buffer_store_dwordx4 a[76:79], v46, s[88:91], s19 offen sc1\n"
  "s_lshl2_add_u32 s18, s17, 3\n"
  "s_mul_i32 s19, s18, 205\n"
  "s_lshr_b32 s19, s19, 10\n"
  "s_mul_i32 s47, s19, s46\n"
  "s_sub_u32 s47, s45, s47\n"
  "s_and_b32 s47, s47, 15\n"
  "s_andn2_b32 s48, s45, 15\n"
  "s_or_b32 s47, s47, s48\n"
  "s_mul_i32 s47, s47, 5\n"
  "s_add_u32 s18, s18, s47\n"
  "s_mul_i32 s19, s19, s42\n"
  "s_add_u32 s18, s18, s19\n"
  "s_lshl_b32 s19, s18, 10\n"
  "s_add_u32 s19, s19, s41\n"
  "buffer_store_dwordx4 a[92:95], v46, s[88:91], s19 offen sc1\n"
  "s_branch at_loop%=\n"
  "at_e6%=:\n"
  "buffer_atomic_add v7, v6, s[88:91], s75 offen offset:128 sc1\n"
  "s_mov_b32 s97, 1\n"
  "s_mov_b64 s[8:9], s[88:89]\n"
  "s_mov_b32 s10, 0\n"
  "s_branch at_loop%=\n"
  "at_mk%=:\n"
  "v_add_f32 v240, 0x0, v63\n"
  "v_min_f32 v240, v45, v240\n"
  "v_add_f32 v241, 0xf1800000, v63\n"
  "v_min_f32 v241, v45, v241\n"
  "v_add_f32 v242, 0xf2000000, v63\n"
  "v_min_f32 v242, v45, v242\n"
  "v_add_f32 v243, 0xf2400000, v63\n"
  "v_min_f32 v243, v45, v243\n"
  "v_add_f32 v244, 0xf3800000, v63\n"
  "v_min_f32 v244, v45, v244\n"
  "v_add_f32 v245, 0xf3880000, v63\n"
  "v_min_f32 v245, v45, v245\n"
  "v_add_f32 v246, 0xf3900000, v63\n"
  "v_min_f32 v246, v45, v246\n"
  "v_add_f32 v247, 0xf3980000, v63\n"
  "v_min_f32 v247, v45, v247\n"
  "v_add_f32 v248, 0x73800000, v63\n"
  "v_min_f32 v248, v62, v248\n"
  "v_add_f32 v249, 0x73700000, v63\n"
  "v_min_f32 v249, v62, v249\n"
  "v_add_f32 v250, 0x73600000, v63\n"
  "v_min_f32 v250, v62, v250\n"
  "v_add_f32 v251, 0x73500000, v63\n"
  "v_min_f32 v251, v62, v251\n"
  "v_add_f32 v252, 0x0, v63\n"
  "v_min_f32 v252, v62, v252\n"
  "v_add_f32 v253, 0xf1800000, v63\n"
  "v_min_f32 v253, v62, v253\n"
  "v_add_f32 v254, 0xf2000000, v63\n"
  "v_min_f32 v254, v62, v254\n"
  "v_add_f32 v255, 0xf2400000, v63\n"
  "v_min_f32 v255, v62, v255\n"
  "s_branch at_loop%=\n"
  "e_spin%=:\n"
  "s_add_u32 s83, s83, 1\n"
  "s_cmp_gt_u32 s83, 0x40000\n"
  "s_cbranch_scc1 e_ok%=\n"
  "s_sleep 2\n"
  "buffer_load_dword v5, v4, s[88:91], s75 offen offset:128 sc1\n"
  "s_waitcnt vmcnt(0)\n"
  "v_cmp_ne_u32 vcc, 8, v5\n"
  "s_cmp_eq_u64 vcc, 0\n"
  "s_cbranch_scc0 e_spin%=\n"
  "s_branch e_ok%=\n"
  "at_end%=:\n"
  "s_lshl_b32 s58, s20, 8\n"
  "s_or_b32 %0, s82, s58\n"
    : "=s"(ret), "+v"(tid), "+s"(bid), "+s"(ka),
      "=&{v[8:9]}"(q[0]), "=&{v[10:11]}"(q[1]), "=&{v[12:13]}"(q[2]), "=&{v[14:15]}"(q[3]), "=&{v[36:37]}"(q[4]) :: "v1","v2","v3","v4","v5","v6","v7","v16","v17","v18","v19","v20","v21","v22","v23","v24","v25","v26","v27","v28","v29","v30","v31","v32","v33","v34","v35","v38","v39","v40","v41","v42","v43","v44","v45","v46","v47","v48","v49","v50","v51","v52","v53","v54","v55","v56","v57","v58","v59","v60","v61","v62","v63","v64","v65","v66","v67","v68","v69","v70","v71","v72","v73","v74","v75","v76","v77","v78","v79","v80","v81","v82","v83","v84","v85","v86","v87","v88","v89","v90","v91","v92","v93","v94","v95","v96","v97","v98","v99","v100","v101","v102","v103","v104","v105","v106","v107","v108","v109","v110","v111","v112","v113","v114","v115","v116","v117","v118","v119","v120","v121","v122","v123","v124","v125","v126","v127","v128","v129","v130","v131","v132","v133","v134","v135","v136","v137","v138","v139","v140","v141","v142","v143","v144","v145","v146","v147","v148","v149","v150","v151","v152","v153","v154","v155","v156","v157","v158","v159","v160","v161","v162","v163","v164","v165","v166","v167","v168","v169","v170","v171","v172","v173","v174","v175","v176","v177","v178","v179","v180","v181","v182","v183","v184","v185","v186","v187","v188","v189","v190","v191","v192","v193","v194","v195","v196","v197","v198","v199","v200","v201","v202","v203","v204","v205","v206","v207","v208","v209","v210","v211","v212","v213","v214","v215","v216","v217","v218","v219","v220","v221","v222","v223","v224","v225","v226","v227","v228","v229","v230","v231","v232","v233","v234","v235","v236","v237","v238","v239","v240","v241","v242","v243","v244","v245","v246","v247","v248","v249","v250","v251","v252","v253","v254","v255","a0","a1","a2","a3","a4","a5","a6","a7","a8","a9","a10","a11","a12","a13","a14","a15","a16","a17","a18","a19","a20","a21","a22","a23","a24","a25","a26","a27","a28","a29","a30","a31","a32","a33","a34","a35","a36","a37","a38","a39","a40","a41","a42","a43","a44","a45","a46","a47","a48","a49","a50","a51","a52","a53","a54","a55","a56","a57","a58","a59","a60","a61","a62","a63","a64","a65","a66","a67","a68","a69","a70","a71","a72","a73","a74","a75","a76","a77","a78","a79","a80","a81","a82","a83","a84","a85","a86","a87","a88","a89","a90","a91","a92","a93","a94","a95","a96","a97","a98","a99","a100","a101","a102","a103","a104","a105","a106","a107","a108","a109","a110","a111","a112","a113","a114","a115","a116","a117","a118","a119","a120","a121","a122","a123","a124","a125","a126","a127","a128","a129","a130","a131","a132","a133","a134","a135","a136","a137","a138","a139","a140","a141","a142","a143","a144","a145","a146","a147","a148","a149","a150","a151","a152","a153","a154","a155","a156","a157","a158","a159","a160","a161","a162","a163","a164","a165","a166","a167","a168","a169","a170","a171","a172","a173","a174","a175","a176","a177","a178","a179","a180","a181","a182","a183","a184","a185","a186","a187","a188","a189","a190","a191","a192","a193","a194","a195","a196","a197","a198","a199","a200","a201","a202","a203","a204","a205","a206","a207","a208","a209","a210","a211","a212","a213","a214","a215","a216","a217","a218","a219","a220","a221","a222","a223","a224","a225","a226","a227","a228","a229","a230","a231","a232","a233","a234","a235","a236","a237","a238","a239","a240","a241","a242","a243","a244","a245","a246","a247","a248","a249","a250","a251","a252","a253","a254","a255","s8","s9","s10","s11","s12","s13","s14","s15","s16","s17","s18","s19","s20","s21","s22","s23","s24","s25","s26","s27","s28","s29","s30","s31","s36","s37","s38","s39","s40","s41","s42","s43","s44","s45","s46","s47","s48","s49","s50","s51","s52","s53","s54","s55","s56","s57","s58","s59","s60","s61","s62","s64","s65","s66","s67","s68","s69","s70","s71","s72","s73","s74","s75","s76","s77","s78","s79","s80","s81","s82","s83","s84","s85","s86","s87","s88","s89","s90","s91","s92","s93","s94","s95","s96","s97","vcc","scc","memory");
  }
  ws = (char*)(__attribute__((address_space(1))) char*)ka[9]; out = (char*)(__attribute__((address_space(1))) char*)ka[10];
  int w = ret >> 8, lane = __builtin_amdgcn_mbcnt_hi(~0u, __builtin_amdgcn_mbcnt_lo(~0u, 0u));
  tid = w * 64 + lane;
  rsrc_t rs = __builtin_amdgcn_make_buffer_rsrc(ws, 0, 72 << 20, 0x20000);
  int mt = (bid & 7) * 4 + (bid >> 6), nt = (bid >> 3) & 7;
  if (ret & 3) {
    __builtin_amdgcn_fence(__ATOMIC_ACQUIRE, "agent");
#pragma unroll 1
    for (int b = 0; b < 2; ++b)
      if ((ret >> b) & 1) slow_rows(ws + WS_QS + (bid * 4 + w) * 16384 + b * 8192, ws, rs, mt, b ? nt : 15 - nt, w, lane);
    asm volatile(
  "s_mov_b64 s[8:9], %1\n"
  "s_mov_b32 s10, 83886080\n"
  "s_mov_b32 s11, 0x20000\n"
  "s_and_b32 s15, %2, 0x3c\n"
  "s_add_u32 s15, s15, %3\n"
  "s_mul_i32 s15, s15, 81920\n"
  "s_add_u32 s15, s15, 62914560\n"
  "s_lshr_b32 s12, %2, 9\n"
  "s_add_u32 s12, s12, 0\n"
  "s_and_b32 s12, s12, 3\n"
  "s_mul_i32 s12, s12, 20480\n"
  "s_add_u32 s12, s12, s15\n"
  "s_add_u32 s12, s12, 0\n"
  "buffer_load_dwordx4 a[64:67], %0, s[8:11], s12 offen offset:0 sc1\n"
  "buffer_load_dwordx4 a[68:71], %0, s[8:11], s12 offen offset:1024 sc1\n"
  "buffer_load_dwordx4 a[72:75], %0, s[8:11], s12 offen offset:2048 sc1\n"
  "buffer_load_dwordx4 a[76:79], %0, s[8:11], s12 offen offset:3072 sc1\n"
  "s_lshr_b32 s12, %2, 9\n"
  "s_add_u32 s12, s12, 0\n"
  "s_and_b32 s12, s12, 3\n"
  "s_mul_i32 s12, s12, 20480\n"
  "s_add_u32 s12, s12, s15\n"
  "s_add_u32 s12, s12, 4096\n"
  "buffer_load_dwordx4 a[80:83], %0, s[8:11], s12 offen offset:0 sc1\n"
  "buffer_load_dwordx4 a[84:87], %0, s[8:11], s12 offen offset:1024 sc1\n"
  "buffer_load_dwordx4 a[88:91], %0, s[8:11], s12 offen offset:2048 sc1\n"
  "buffer_load_dwordx4 a[92:95], %0, s[8:11], s12 offen offset:3072 sc1\n"
  "s_lshr_b32 s12, %2, 9\n"
  "s_add_u32 s12, s12, 0\n"
  "s_and_b32 s12, s12, 3\n"
  "s_mul_i32 s12, s12, 20480\n"
  "s_add_u32 s12, s12, s15\n"
  "s_add_u32 s12, s12, 8192\n"
  "buffer_load_dwordx4 a[96:99], %0, s[8:11], s12 offen offset:0 sc1\n"
  "buffer_load_dwordx4 a[100:103], %0, s[8:11], s12 offen offset:1024 sc1\n"
  "buffer_load_dwordx4 a[104:107], %0, s[8:11], s12 offen offset:2048 sc1\n"
  "buffer_load_dwordx4 a[108:111], %0, s[8:11], s12 offen offset:3072 sc1\n"
  "s_lshr_b32 s12, %2, 9\n"
  "s_add_u32 s12, s12, 0\n"
  "s_and_b32 s12, s12, 3\n"
  "s_mul_i32 s12, s12, 20480\n"
  "s_add_u32 s12, s12, s15\n"
  "s_add_u32 s12, s12, 12288\n"
  "buffer_load_dwordx4 a[112:115], %0, s[8:11], s12 offen offset:0 sc1\n"
  "buffer_load_dwordx4 a[116:119], %0, s[8:11], s12 offen offset:1024 sc1\n"
  "buffer_load_dwordx4 a[120:123], %0, s[8:11], s12 offen offset:2048 sc1\n"
  "buffer_load_dwordx4 a[124:127], %0, s[8:11], s12 offen offset:3072 sc1\n"
  "s_waitcnt vmcnt(0)\n"
    :: "v"(lane * 16), "s"(ws), "s"(bid), "s"(w) : "a0","a1","a2","a3","a4","a5","a6","a7","a8","a9","a10","a11","a12","a13","a14","a15","a16","a17","a18","a19","a20","a21","a22","a23","a24","a25","a26","a27","a28","a29","a30","a31","a32","a33","a34","a35","a36","a37","a38","a39","a40","a41","a42","a43","a44","a45","a46","a47","a48","a49","a50","a51","a52","a53","a54","a55","a56","a57","a58","a59","a60","a61","a62","a63","a64","a65","a66","a67","a68","a69","a70","a71","a72","a73","a74","a75","a76","a77","a78","a79","a80","a81","a82","a83","a84","a85","a86","a87","a88","a89","a90","a91","a92","a93","a94","a95","a96","a97","a98","a99","a100","a101","a102","a103","a104","a105","a106","a107","a108","a109","a110","a111","a112","a113","a114","a115","a116","a117","a118","a119","a120","a121","a122","a123","a124","a125","a126","a127","a128","a129","a130","a131","a132","a133","a134","a135","a136","a137","a138","a139","a140","a141","a142","a143","a144","a145","a146","a147","a148","a149","a150","a151","a152","a153","a154","a155","a156","a157","a158","a159","a160","a161","a162","a163","a164","a165","a166","a167","a168","a169","a170","a171","a172","a173","a174","a175","a176","a177","a178","a179","a180","a181","a182","a183","a184","a185","a186","a187","a188","a189","a190","a191","a192","a193","a194","a195","a196","a197","a198","a199","a200","a201","a202","a203","a204","a205","a206","a207","a208","a209","a210","a211","a212","a213","a214","a215","a216","a217","a218","a219","a220","a221","a222","a223","a224","a225","a226","a227","a228","a229","a230","a231","a232","a233","a234","a235","a236","a237","a238","a239","a240","a241","a242","a243","a244","a245","a246","a247","a248","a249","a250","a251","a252","a253","a254","a255","s8","s9","s10","s11","s12","s15","vcc","scc","memory");
  }
  int l16 = lane * 16;
  asm volatile("" :: "v"((__attribute__((address_space(3))) char*)lds) : "memory");
  local_attn(lds + 131072, q, rs, nt * 4 + w, mt * 64 + lane, bid);
  asm volatile(
  "s_mov_b64 s[8:9], %1\n"
  "s_mov_b32 s10, 83886080\n"
  "s_mov_b32 s11, 0x20000\n"
  "s_and_b32 s25, %3, 7\n"
  "s_lshr_b32 s26, %3, 6\n"
  "s_lshl2_add_u32 s25, s25, s26\n"
  "s_and_b32 s26, s25, 15\n"
  "s_lshr_b32 s30, s25, 4\n"
  "s_lshl1_add_u32 s29, s26, s30\n"
  "s_sub_u32 s27, 15, s26\n"
  "s_min_u32 s26, s26, s27\n"
  "s_lshl_b32 s26, s26, 7\n"
  "s_mul_i32 s27, s30, 12\n"
  "s_add_u32 s26, s26, s27\n"
  "s_add_u32 s23, s26, 73408640\n"
  "s_mov_b32 s83, 0\n"
  "buffer_load_dword v11, off, s[8:11], s23 sc1\n"
  "s_and_b32 s17, %3, 0x3c\n"
  "s_add_u32 s17, s17, %4\n"
  "s_mul_i32 s17, s17, 81920\n"
  "s_add_u32 s17, s17, 62914560\n"
  "s_lshl_b32 s18, %4, 15\n"
  "s_add_u32 m0, s18, 8192\n"
  "s_lshr_b32 s39, %3, 9\n"
  "s_add_u32 s39, s39, 3\n"
  "s_and_b32 s39, s39, 3\n"
  "s_mul_i32 s39, s39, 20480\n"
  "s_add_u32 s39, s39, s17\n"
  "s_add_u32 s39, s39, 12288\n"
  "buffer_load_dwordx4 %0, s[8:11], s39 offen offset:0 sc1 lds\n"
  "buffer_load_dwordx4 %0, s[8:11], s39 offen offset:1024 sc1 lds\n"
  "buffer_load_dwordx4 %0, s[8:11], s39 offen offset:2048 sc1 lds\n"
  "buffer_load_dwordx4 %0, s[8:11], s39 offen offset:3072 sc1 lds\n"
  "s_add_u32 m0, s18, 12288\n"
  "s_lshr_b32 s39, %3, 9\n"
  "s_add_u32 s39, s39, 3\n"
  "s_and_b32 s39, s39, 3\n"
  "s_mul_i32 s39, s39, 20480\n"
  "s_add_u32 s39, s39, s17\n"
  "s_add_u32 s39, s39, 16384\n"
  "buffer_load_dwordx4 %0, s[8:11], s39 offen offset:0 sc1 lds\n"
  "buffer_load_dwordx4 %0, s[8:11], s39 offen offset:1024 sc1 lds\n"
  "buffer_load_dwordx4 %0, s[8:11], s39 offen offset:2048 sc1 lds\n"
  "buffer_load_dwordx4 %0, s[8:11], s39 offen offset:3072 sc1 lds\n"
  "s_lshl2_add_u32 s27, s25, %4\n"
  "s_lshl_b32 s31, s29, 19\n"
  "s_lshl2_add_u32 s26, s30, %4\n"
  "s_lshl_b32 s26, s26, 16\n"
  "s_add_u32 s31, s31, s26\n"
  "s_add_u32 s21, s31, 33554432\n"
  "s_xor_b32 s16, s21, 524288\n"
  "s_and_b32 s26, %3, 127\n"
  "s_lshl_b32 s26, s26, 7\n"
  "s_lshr_b32 s31, %3, 7\n"
  "s_lshl2_add_u32 s26, s31, s26\n"
  "s_add_u32 s40, s26, 73400452\n"
  "s_xor_b32 s26, s26, 0x200\n"
  "s_add_u32 s41, s26, 73400452\n"
  "s_lshl2_add_u32 s26, %3, %4\n"
  "s_mul_i32 s31, s26, 5120\n"
  "s_add_u32 s42, s31, 75497472\n"
  "s_xor_b32 s26, s26, 16\n"
  "s_mul_i32 s31, s26, 5120\n"
  "s_add_u32 s43, s31, 75497472\n"
  "s_add_u32 s44, s42, 0x1000\n"
  "s_add_u32 s45, s43, 0x1000\n"
  "s_lshl_b32 s26, s25, 7\n"
  "s_add_u32 s24, s26, 73409664\n"
  "s_mul_i32 s26, s27, 20480\n"
  "s_add_u32 s19, s26, 58720256\n"
  "s_bfe_u32 s28, %3, 0x30003\n"
  "s_mul_i32 s46, s28, 8192\n"
  "s_lshr_b32 s26, %3, 6\n"
  "s_mul_i32 s26, s26, 0\n"
  "s_add_u32 s46, s46, s26\n"
  "s_lshl2_add_u32 s26, s28, %4\n"
  "s_mul_i32 s26, s26, 25600\n"
  "s_add_u32 s20, s26, 69206016\n"
  "s_mov_b64 s[12:13], %2\n"
  "s_mov_b32 s14, 5242880\n"
  "s_mov_b32 s15, 0x20000\n"
  "s_lshl_b32 s26, %4, 15\n"
  "v_add_u32 v7, s26, %0\n"
  "s_lshl2_add_u32 s26, s25, %4\n"
  "s_mul_i32 s26, s26, 40960\n"
  "s_mul_i32 s28, s28, 160\n"
  "s_add_u32 s26, s26, s28\n"
  "v_bfe_u32 v12, %0, 4, 4\n"
  "v_mul_u32_u24 v12, 2560, v12\n"
  "v_lshrrev_b32 v13, 8, %0\n"
  "v_lshl_add_u32 v12, v13, 3, v12\n"
  "v_add_u32 v14, s26, v12\n"
  "s_lshl2_add_u32 s26, s29, %4\n"
  "s_mul_i32 s26, s26, 40960\n"
  "s_add_u32 s26, s26, s28\n"
  "v_add_u32 v6, s26, v12\n"
  "s_add_u32 s26, %4, 0\n"
  "s_and_b32 s26, s26, 3\n"
  "s_lshl_b32 s27, s26, 10\n"
  "v_add_u32 v2, s27, %0\n"
  "s_add_u32 s26, %4, 1\n"
  "s_and_b32 s26, s26, 3\n"
  "s_lshl_b32 s27, s26, 10\n"
  "v_add_u32 v3, s27, %0\n"
  "s_sub_u32 s26, %4, 1\n"
  "s_and_b32 s26, s26, 3\n"
  "s_lshl_b32 s26, s26, 15\n"
  "s_add_u32 s26, s26, 0\n"
  "v_add_u32 v8, s26, %0\n"
  "s_add_u32 s26, %4, 2\n"
  "s_and_b32 s26, s26, 3\n"
  "s_lshl_b32 s27, s26, 10\n"
  "v_add_u32 v4, s27, %0\n"
  "s_sub_u32 s26, %4, 2\n"
  "s_and_b32 s26, s26, 3\n"
  "s_lshl_b32 s26, s26, 15\n"
  "s_add_u32 s26, s26, 5120\n"
  "v_add_u32 v9, s26, %0\n"
  "s_add_u32 s26, %4, 3\n"
  "s_and_b32 s26, s26, 3\n"
  "s_lshl_b32 s27, s26, 10\n"
  "v_add_u32 v5, s27, %0\n"
  "s_sub_u32 s26, %4, 3\n"
  "s_and_b32 s26, s26, 3\n"
  "s_lshl_b32 s26, s26, 15\n"
  "s_add_u32 s26, s26, 10240\n"
  "v_add_u32 v10, s26, %0\n"
  "v_mov_b32 v13, 1\n"
  "v_min_u32 v12, 16, %0\n"
  "v_lshlrev_b32 v12, 26, v12\n"
  "s_waitcnt vmcnt(8)\n"
  "buffer_atomic_add v13, v12, s[8:11], s24 offen sc1\n"
  "v_readfirstlane_b32 s26, v11\n"
  "s_cmp_eq_u32 s26, 64\n"
  "s_cbranch_scc0 c_spin%=\n"
  "c_ok%=:\n"
  "s_add_u32 s38, s46, 0\n"
  "s_and_b32 s38, s38, 0xf000\n"
  "s_add_u32 s38, s38, s16\n"
  "buffer_load_dwordx4 v[96:99], v2, s[8:11], s38 offen offset:0 sc1\n"
  "buffer_load_dwordx4 v[100:103], v3, s[8:11], s38 offen offset:0 sc1\n"
  "buffer_load_dwordx4 v[104:107], v4, s[8:11], s38 offen offset:0 sc1\n"
  "buffer_load_dwordx4 v[108:111], v5, s[8:11], s38 offen offset:0 sc1\n"
  "s_add_u32 s38, s46, 4096\n"
  "s_and_b32 s38, s38, 0xf000\n"
  "s_add_u32 s38, s38, s16\n"
  "buffer_load_dwordx4 v[112:115], v2, s[8:11], s38 offen offset:0 sc1\n"
  "buffer_load_dwordx4 v[116:119], v3, s[8:11], s38 offen offset:0 sc1\n"
  "buffer_load_dwordx4 v[120:123], v4, s[8:11], s38 offen offset:0 sc1\n"
  "buffer_load_dwordx4 v[124:127], v5, s[8:11], s38 offen offset:0 sc1\n"
  "s_waitcnt vmcnt(7)\n"
  "v_mfma_f32_16x16x32_bf16 v[16:19], a[64:67], v[96:99], 0\n"
  "v_mfma_f32_16x16x32_bf16 v[20:23], a[68:71], v[96:99], 0\n"
  "v_mfma_f32_16x16x32_bf16 v[24:27], a[72:75], v[96:99], 0\n"
  "v_mfma_f32_16x16x32_bf16 v[28:31], a[76:79], v[96:99], 0\n"
  "s_add_u32 s38, s46, 8192\n"
  "s_and_b32 s38, s38, 0xf000\n"
  "s_add_u32 s38, s38, s16\n"
  "buffer_load_dwordx4 v[128:131], v2, s[8:11], s38 offen offset:0 sc1\n"
  "v_mfma_f32_16x16x32_bf16 v[32:35], a[80:83], v[96:99], 0\n"
  "s_waitcnt vmcnt(7)\n"
  "v_mfma_f32_16x16x32_bf16 v[36:39], a[64:67], v[100:103], 0\n"
  "v_mfma_f32_16x16x32_bf16 v[40:43], a[68:71], v[100:103], 0\n"
  "v_mfma_f32_16x16x32_bf16 v[44:47], a[72:75], v[100:103], 0\n"
  "buffer_load_dwordx4 v[132:135], v3, s[8:11], s38 offen offset:0 sc1\n"
  "v_mfma_f32_16x16x32_bf16 v[48:51], a[76:79], v[100:103], 0\n"
  "v_mfma_f32_16x16x32_bf16 v[52:55], a[80:83], v[100:103], 0\n"
  "s_waitcnt vmcnt(7)\n"
  "v_mfma_f32_16x16x32_bf16 v[56:59], a[64:67], v[104:107], 0\n"
  "v_mfma_f32_16x16x32_bf16 v[60:63], a[68:71], v[104:107], 0\n"
  "buffer_load_dwordx4 v[136:139], v4, s[8:11], s38 offen offset:0 sc1\n"
  "v_mfma_f32_16x16x32_bf16 v[64:67], a[72:75], v[104:107], 0\n"
  "v_mfma_f32_16x16x32_bf16 v[68:71], a[76:79], v[104:107], 0\n"
  "v_mfma_f32_16x16x32_bf16 v[72:75], a[80:83], v[104:107], 0\n"
  "s_waitcnt vmcnt(7)\n"
  "v_mfma_f32_16x16x32_bf16 v[76:79], a[64:67], v[108:111], 0\n"
  "buffer_load_dwordx4 v[140:143], v5, s[8:11], s38 offen offset:0 sc1\n"
  "v_mfma_f32_16x16x32_bf16 v[80:83], a[68:71], v[108:111], 0\n"
  "v_mfma_f32_16x16x32_bf16 v[84:87], a[72:75], v[108:111], 0\n"
  "v_mfma_f32_16x16x32_bf16 v[88:91], a[76:79], v[108:111], 0\n"
  "s_add_u32 s38, s46, 12288\n"
  "s_and_b32 s38, s38, 0xf000\n"
  "s_add_u32 s38, s38, s16\n"
  "buffer_load_dwordx4 v[144:147], v2, s[8:11], s38 offen offset:0 sc1\n"
  "v_mfma_f32_16x16x32_bf16 v[92:95], a[80:83], v[108:111], 0\n"
  "s_waitcnt vmcnt(8)\n"
  "v_mfma_f32_16x16x32_bf16 v[16:19], a[84:87], v[112:115], v[16:19]\n"
  "v_mfma_f32_16x16x32_bf16 v[20:23], a[88:91], v[112:115], v[20:23]\n"
  "v_mfma_f32_16x16x32_bf16 v[24:27], a[92:95], v[112:115], v[24:27]\n"
  "buffer_load_dwordx4 v[148:151], v3, s[8:11], s38 offen offset:0 sc1\n"
  "v_mfma_f32_16x16x32_bf16 v[28:31], a[96:99], v[112:115], v[28:31]\n"
  "v_mfma_f32_16x16x32_bf16 v[32:35], a[100:103], v[112:115], v[32:35]\n"
  "s_waitcnt vmcnt(8)\n"
  "v_mfma_f32_16x16x32_bf16 v[36:39], a[84:87], v[116:119], v[36:39]\n"
  "v_mfma_f32_16x16x32_bf16 v[40:43], a[88:91], v[116:119], v[40:43]\n"
  "buffer_load_dwordx4 v[152:155], v4, s[8:11], s38 offen offset:0 sc1\n"
  "v_mfma_f32_16x16x32_bf16 v[44:47], a[92:95], v[116:119], v[44:47]\n"
  "v_mfma_f32_16x16x32_bf16 v[48:51], a[96:99], v[116:119], v[48:51]\n"
  "v_mfma_f32_16x16x32_bf16 v[52:55], a[100:103], v[116:119], v[52:55]\n"
  "s_waitcnt vmcnt(8)\n"
  "v_mfma_f32_16x16x32_bf16 v[56:59], a[84:87], v[120:123], v[56:59]\n"
  "buffer_load_dwordx4 v[156:159], v5, s[8:11], s38 offen offset:0 sc1\n"
  "v_mfma_f32_16x16x32_bf16 v[60:63], a[88:91], v[120:123], v[60:63]\n"
  "v_mfma_f32_16x16x32_bf16 v[64:67], a[92:95], v[120:123], v[64:67]\n"
  "v_mfma_f32_16x16x32_bf16 v[68:71], a[96:99], v[120:123], v[68:71]\n"
  "v_mfma_f32_16x16x32_bf16 v[72:75], a[100:103], v[120:123], v[72:75]\n"
  "s_add_u32 s38, s46, 16384\n"
  "s_and_b32 s38, s38, 0xf000\n"
  "s_add_u32 s38, s38, s16\n"
  "buffer_load_dwordx4 v[160:163], v2, s[8:11], s38 offen offset:0 sc1\n"
  "s_waitcnt vmcnt(9)\n"
  "v_mfma_f32_16x16x32_bf16 v[76:79], a[84:87], v[124:127], v[76:79]\n"
  "v_mfma_f32_16x16x32_bf16 v[80:83], a[88:91], v[124:127], v[80:83]\n"
  "v_mfma_f32_16x16x32_bf16 v[84:87], a[92:95], v[124:127], v[84:87]\n"
  "buffer_load_dwordx4 v[164:167], v3, s[8:11], s38 offen offset:0 sc1\n"
  "v_mfma_f32_16x16x32_bf16 v[88:91], a[96:99], v[124:127], v[88:91]\n"
  "v_mfma_f32_16x16x32_bf16 v[92:95], a[100:103], v[124:127], v[92:95]\n"
  "s_waitcnt vmcnt(9)\n"
  "v_mfma_f32_16x16x32_bf16 v[16:19], a[104:107], v[128:131], v[16:19]\n"
  "v_mfma_f32_16x16x32_bf16 v[20:23], a[108:111], v[128:131], v[20:23]\n"
  "buffer_load_dwordx4 v[168:171], v4, s[8:11], s38 offen offset:0 sc1\n"
  "v_mfma_f32_16x16x32_bf16 v[24:27], a[112:115], v[128:131], v[24:27]\n"
  "v_mfma_f32_16x16x32_bf16 v[28:31], a[116:119], v[128:131], v[28:31]\n"
  "v_mfma_f32_16x16x32_bf16 v[32:35], a[120:123], v[128:131], v[32:35]\n"
  "s_waitcnt vmcnt(9)\n"
  "v_mfma_f32_16x16x32_bf16 v[36:39], a[104:107], v[132:135], v[36:39]\n"
  "buffer_load_dwordx4 v[172:175], v5, s[8:11], s38 offen offset:0 sc1\n"
  "v_mfma_f32_16x16x32_bf16 v[40:43], a[108:111], v[132:135], v[40:43]\n"
  "v_mfma_f32_16x16x32_bf16 v[44:47], a[112:115], v[132:135], v[44:47]\n"
  "v_mfma_f32_16x16x32_bf16 v[48:51], a[116:119], v[132:135], v[48:51]\n"
  "v_mfma_f32_16x16x32_bf16 v[52:55], a[120:123], v[132:135], v[52:55]\n"
  "s_add_u32 s38, s46, 20480\n"
  "s_and_b32 s38, s38, 0xf000\n"
  "s_add_u32 s38, s38, s16\n"
  "buffer_load_dwordx4 v[96:99], v2, s[8:11], s38 offen offset:0 sc1\n"
  "s_waitcnt vmcnt(10)\n"
  "v_mfma_f32_16x16x32_bf16 v[56:59], a[104:107], v[136:139], v[56:59]\n"
  "v_mfma_f32_16x16x32_bf16 v[60:63], a[108:111], v[136:139], v[60:63]\n"
  "v_mfma_f32_16x16x32_bf16 v[64:67], a[112:115], v[136:139], v[64:67]\n"
  "v_mfma_f32_16x16x32_bf16 v[68:71], a[116:119], v[136:139], v[68:71]\n"
  "buffer_load_dwordx4 v[100:103], v3, s[8:11], s38 offen offset:0 sc1\n"
  "v_mfma_f32_16x16x32_bf16 v[72:75], a[120:123], v[136:139], v[72:75]\n"
  "s_waitcnt vmcnt(10)\n"
  "v_mfma_f32_16x16x32_bf16 v[76:79], a[104:107], v[140:143], v[76:79]\n"
  "v_mfma_f32_16x16x32_bf16 v[80:83], a[108:111], v[140:143], v[80:83]\n"
  "buffer_load_dwordx4 v[104:107], v4, s[8:11], s38 offen offset:0 sc1\n"
  "v_mfma_f32_16x16x32_bf16 v[84:87], a[112:115], v[140:143], v[84:87]\n"
  "v_mfma_f32_16x16x32_bf16 v[88:91], a[116:119], v[140:143], v[88:91]\n"
  "v_mfma_f32_16x16x32_bf16 v[92:95], a[120:123], v[140:143], v[92:95]\n"
  "s_waitcnt vmcnt(10)\n"
  "v_mfma_f32_16x16x32_bf16 v[16:19], a[124:127], v[144:147], v[16:19]\n"
  "buffer_load_dwordx4 v[108:111], v5, s[8:11], s38 offen offset:0 sc1\n"
  "v_mfma_f32_16x16x32_bf16 v[20:23], a[128:131], v[144:147], v[20:23]\n"
  "v_mfma_f32_16x16x32_bf16 v[24:27], a[132:135], v[144:147], v[24:27]\n"
  "v_mfma_f32_16x16x32_bf16 v[28:31], a[136:139], v[144:147], v[28:31]\n"
  "v_mfma_f32_16x16x32_bf16 v[32:35], a[140:143], v[144:147], v[32:35]\n"
  "s_add_u32 s38, s46, 24576\n"
  "s_and_b32 s38, s38, 0xf000\n"
  "s_add_u32 s38, s38, s16\n"
  "buffer_load_dwordx4 v[112:115], v2, s[8:11], s38 offen offset:0 sc1\n"
  "s_waitcnt vmcnt(11)\n"
  "v_mfma_f32_16x16x32_bf16 v[36:39], a[124:127], v[148:151], v[36:39]\n"
  "v_mfma_f32_16x16x32_bf16 v[40:43], a[128:131], v[148:151], v[40:43]\n"
  "v_mfma_f32_16x16x32_bf16 v[44:47], a[132:135], v[148:151], v[44:47]\n"
  "v_mfma_f32_16x16x32_bf16 v[48:51], a[136:139], v[148:151], v[48:51]\n"
  "buffer_load_dwordx4 v[116:119], v3, s[8:11], s38 offen offset:0 sc1\n"
  "v_mfma_f32_16x16x32_bf16 v[52:55], a[140:143], v[148:151], v[52:55]\n"
  "s_waitcnt vmcnt(11)\n"
  "v_mfma_f32_16x16x32_bf16 v[56:59], a[124:127], v[152:155], v[56:59]\n"
  "v_mfma_f32_16x16x32_bf16 v[60:63], a[128:131], v[152:155], v[60:63]\n"
  "v_mfma_f32_16x16x32_bf16 v[64:67], a[132:135], v[152:155], v[64:67]\n"
  "buffer_load_dwordx4 v[120:123], v4, s[8:11], s38 offen offset:0 sc1\n"
  "v_mfma_f32_16x16x32_bf16 v[68:71], a[136:139], v[152:155], v[68:71]\n"
  "v_mfma_f32_16x16x32_bf16 v[72:75], a[140:143], v[152:155], v[72:75]\n"
  "s_waitcnt vmcnt(11)\n"
  "v_mfma_f32_16x16x32_bf16 v[76:79], a[124:127], v[156:159], v[76:79]\n"
  "buffer_load_dwordx4 v[124:127], v5, s[8:11], s38 offen offset:0 sc1\n"
  "v_mfma_f32_16x16x32_bf16 v[80:83], a[128:131], v[156:159], v[80:83]\n"
  "v_mfma_f32_16x16x32_bf16 v[84:87], a[132:135], v[156:159], v[84:87]\n"
  "v_mfma_f32_16x16x32_bf16 v[88:91], a[136:139], v[156:159], v[88:91]\n"
  "v_mfma_f32_16x16x32_bf16 v[92:95], a[140:143], v[156:159], v[92:95]\n"
  "s_add_u32 s38, s46, 28672\n"
  "s_and_b32 s38, s38, 0xf000\n"
  "s_add_u32 s38, s38, s16\n"
  "buffer_load_dwordx4 v[128:131], v2, s[8:11], s38 offen offset:0 sc1\n"
  "s_waitcnt vmcnt(12)\n"
  "v_mfma_f32_16x16x32_bf16 v[16:19], a[144:147], v[160:163], v[16:19]\n"
  "v_mfma_f32_16x16x32_bf16 v[20:23], a[148:151], v[160:163], v[20:23]\n"
  "v_mfma_f32_16x16x32_bf16 v[24:27], a[152:155], v[160:163], v[24:27]\n"
  "v_mfma_f32_16x16x32_bf16 v[28:31], a[156:159], v[160:163], v[28:31]\n"
  "buffer_load_dwordx4 v[132:135], v3, s[8:11], s38 offen offset:0 sc1\n"
  "v_mfma_f32_16x16x32_bf16 v[32:35], a[160:163], v[160:163], v[32:35]\n"
  "s_waitcnt vmcnt(12)\n"
  "v_mfma_f32_16x16x32_bf16 v[36:39], a[144:147], v[164:167], v[36:39]\n"
  "v_mfma_f32_16x16x32_bf16 v[40:43], a[148:151], v[164:167], v[40:43]\n"
  "v_mfma_f32_16x16x32_bf16 v[44:47], a[152:155], v[164:167], v[44:47]\n"
  "buffer_load_dwordx4 v[136:139], v4, s[8:11], s38 offen offset:0 sc1\n"
  "v_mfma_f32_16x16x32_bf16 v[48:51], a[156:159], v[164:167], v[48:51]\n"
  "v_mfma_f32_16x16x32_bf16 v[52:55], a[160:163], v[164:167], v[52:55]\n"
  "s_waitcnt vmcnt(12)\n"
  "v_mfma_f32_16x16x32_bf16 v[56:59], a[144:147], v[168:171], v[56:59]\n"
  "v_mfma_f32_16x16x32_bf16 v[60:63], a[148:151], v[168:171], v[60:63]\n"
  "buffer_load_dwordx4 v[140:143], v5, s[8:11], s38 offen offset:0 sc1\n"
  "v_mfma_f32_16x16x32_bf16 v[64:67], a[152:155], v[168:171], v[64:67]\n"
  "v_mfma_f32_16x16x32_bf16 v[68:71], a[156:159], v[168:171], v[68:71]\n"
  "v_mfma_f32_16x16x32_bf16 v[72:75], a[160:163], v[168:171], v[72:75]\n"
  "s_add_u32 s38, s46, 32768\n"
  "s_and_b32 s38, s38, 0xf000\n"
  "s_add_u32 s38, s38, s16\n"
  "buffer_load_dwordx4 v[144:147], v2, s[8:11], s38 offen offset:0 sc1\n"
  "s_waitcnt vmcnt(13)\n"
  "v_mfma_f32_16x16x32_bf16 v[76:79], a[144:147], v[172:175], v[76:79]\n"
  "v_mfma_f32_16x16x32_bf16 v[80:83], a[148:151], v[172:175], v[80:83]\n"
  "v_mfma_f32_16x16x32_bf16 v[84:87], a[152:155], v[172:175], v[84:87]\n"
  "v_mfma_f32_16x16x32_bf16 v[88:91], a[156:159], v[172:175], v[88:91]\n"
  "buffer_load_dwordx4 v[148:151], v3, s[8:11], s38 offen offset:0 sc1\n"
  "v_mfma_f32_16x16x32_bf16 v[92:95], a[160:163], v[172:175], v[92:95]\n"
  "s_waitcnt vmcnt(13)\n"
  "v_mfma_f32_16x16x32_bf16 v[16:19], a[164:167], v[96:99], v[16:19]\n"
  "v_mfma_f32_16x16x32_bf16 v[20:23], a[168:171], v[96:99], v[20:23]\n"
  "v_mfma_f32_16x16x32_bf16 v[24:27], a[172:175], v[96:99], v[24:27]\n"
  "buffer_load_dwordx4 v[152:155], v4, s[8:11], s38 offen offset:0 sc1\n"
  "v_mfma_f32_16x16x32_bf16 v[28:31], a[176:179], v[96:99], v[28:31]\n"
  "v_mfma_f32_16x16x32_bf16 v[32:35], a[180:183], v[96:99], v[32:35]\n"
  "s_waitcnt vmcnt(13)\n"
  "v_mfma_f32_16x16x32_bf16 v[36:39], a[164:167], v[100:103], v[36:39]\n"
  "v_mfma_f32_16x16x32_bf16 v[40:43], a[168:171], v[100:103], v[40:43]\n"
  "buffer_load_dwordx4 v[156:159], v5, s[8:11], s38 offen offset:0 sc1\n"
  "v_mfma_f32_16x16x32_bf16 v[44:47], a[172:175], v[100:103], v[44:47]\n"
  "v_mfma_f32_16x16x32_bf16 v[48:51], a[176:179], v[100:103], v[48:51]\n"
  "v_mfma_f32_16x16x32_bf16 v[52:55], a[180:183], v[100:103], v[52:55]\n"
  "s_waitcnt vmcnt(13)\n"
  "v_mfma_f32_16x16x32_bf16 v[56:59], a[164:167], v[104:107], v[56:59]\n"
  "s_add_u32 s38, s46, 36864\n"
  "s_and_b32 s38, s38, 0xf000\n"
  "s_add_u32 s38, s38, s16\n"
  "buffer_load_dwordx4 v[160:163], v2, s[8:11], s38 offen offset:0 sc1\n"
  "v_mfma_f32_16x16x32_bf16 v[60:63], a[168:171], v[104:107], v[60:63]\n"
  "v_mfma_f32_16x16x32_bf16 v[64:67], a[172:175], v[104:107], v[64:67]\n"
  "v_mfma_f32_16x16x32_bf16 v[68:71], a[176:179], v[104:107], v[68:71]\n"
  "buffer_load_dwordx4 v[164:167], v3, s[8:11], s38 offen offset:0 sc1\n"
  "v_mfma_f32_16x16x32_bf16 v[72:75], a[180:183], v[104:107], v[72:75]\n"
  "s_waitcnt vmcnt(14)\n"
  "v_mfma_f32_16x16x32_bf16 v[76:79], a[164:167], v[108:111], v[76:79]\n"
  "v_mfma_f32_16x16x32_bf16 v[80:83], a[168:171], v[108:111], v[80:83]\n"
  "v_mfma_f32_16x16x32_bf16 v[84:87], a[172:175], v[108:111], v[84:87]\n"
  "buffer_load_dwordx4 v[168:171], v4, s[8:11], s38 offen offset:0 sc1\n"
  "v_mfma_f32_16x16x32_bf16 v[88:91], a[176:179], v[108:111], v[88:91]\n"
  "v_mfma_f32_16x16x32_bf16 v[92:95], a[180:183], v[108:111], v[92:95]\n"
  "s_waitcnt vmcnt(14)\n"
  "v_mfma_f32_16x16x32_bf16 v[16:19], a[184:187], v[112:115], v[16:19]\n"
  "v_mfma_f32_16x16x32_bf16 v[20:23], a[188:191], v[112:115], v[20:23]\n"
  "buffer_load_dwordx4 v[172:175], v5, s[8:11], s38 offen offset:0 sc1\n"
  "v_mfma_f32_16x16x32_bf16 v[24:27], a[192:195], v[112:115], v[24:27]\n"
  "v_mfma_f32_16x16x32_bf16 v[28:31], a[196:199], v[112:115], v[28:31]\n"
  "v_mfma_f32_16x16x32_bf16 v[32:35], a[200:203], v[112:115], v[32:35]\n"
  "s_waitcnt vmcnt(14)\n"
  "v_mfma_f32_16x16x32_bf16 v[36:39], a[184:187], v[116:119], v[36:39]\n"
  "s_add_u32 s38, s46, 40960\n"
  "s_and_b32 s38, s38, 0xf000\n"
  "s_add_u32 s38, s38, s16\n"
  "buffer_load_dwordx4 v[96:99], v2, s[8:11], s38 offen offset:0 sc1\n"
  "v_mfma_f32_16x16x32_bf16 v[40:43], a[188:191], v[116:119], v[40:43]\n"
  "v_mfma_f32_16x16x32_bf16 v[44:47], a[192:195], v[116:119], v[44:47]\n"
  "v_mfma_f32_16x16x32_bf16 v[48:51], a[196:199], v[116:119], v[48:51]\n"
  "v_mfma_f32_16x16x32_bf16 v[52:55], a[200:203], v[116:119], v[52:55]\n"
  "buffer_load_dwordx4 v[100:103], v3, s[8:11], s38 offen offset:0 sc1\n"
  "s_waitcnt vmcnt(15)\n"
  "v_mfma_f32_16x16x32_bf16 v[56:59], a[184:187], v[120:123], v[56:59]\n"
  "v_mfma_f32_16x16x32_bf16 v[60:63], a[188:191], v[120:123], v[60:63]\n"
  "v_mfma_f32_16x16x32_bf16 v[64:67], a[192:195], v[120:123], v[64:67]\n"
  "buffer_load_dwordx4 v[104:107], v4, s[8:11], s38 offen offset:0 sc1\n"
  "v_mfma_f32_16x16x32_bf16 v[68:71], a[196:199], v[120:123], v[68:71]\n"
  "v_mfma_f32_16x16x32_bf16 v[72:75], a[200:203], v[120:123], v[72:75]\n"
  "s_waitcnt vmcnt(15)\n"
  "v_mfma_f32_16x16x32_bf16 v[76:79], a[184:187], v[124:127], v[76:79]\n"
  "v_mfma_f32_16x16x32_bf16 v[80:83], a[188:191], v[124:127], v[80:83]\n"
  "buffer_load_dwordx4 v[108:111], v5, s[8:11], s38 offen offset:0 sc1\n"
  "v_mfma_f32_16x16x32_bf16 v[84:87], a[192:195], v[124:127], v[84:87]\n"
  "v_mfma_f32_16x16x32_bf16 v[88:91], a[196:199], v[124:127], v[88:91]\n"
  "v_mfma_f32_16x16x32_bf16 v[92:95], a[200:203], v[124:127], v[92:95]\n"
  "s_waitcnt vmcnt(15)\n"
  "v_mfma_f32_16x16x32_bf16 v[16:19], a[204:207], v[128:131], v[16:19]\n"
  "s_add_u32 s38, s46, 45056\n"
  "s_and_b32 s38, s38, 0xf000\n"
  "s_add_u32 s38, s38, s16\n"
  "buffer_load_dwordx4 v[112:115], v2, s[8:11], s38 offen offset:0 sc1\n"
  "v_mfma_f32_16x16x32_bf16 v[20:23], a[208:211], v[128:131], v[20:23]\n"
  "v_mfma_f32_16x16x32_bf16 v[24:27], a[212:215], v[128:131], v[24:27]\n"
  "v_mfma_f32_16x16x32_bf16 v[28:31], a[216:219], v[128:131], v[28:31]\n"
  "v_mfma_f32_16x16x32_bf16 v[32:35], a[220:223], v[128:131], v[32:35]\n"
  "buffer_load_dwordx4 v[116:119], v3, s[8:11], s38 offen offset:0 sc1\n"
  "s_waitcnt vmcnt(16)\n"
  "v_mfma_f32_16x16x32_bf16 v[36:39], a[204:207], v[132:135], v[36:39]\n"
  "v_mfma_f32_16x16x32_bf16 v[40:43], a[208:211], v[132:135], v[40:43]\n"
  "v_mfma_f32_16x16x32_bf16 v[44:47], a[212:215], v[132:135], v[44:47]\n"
  "v_mfma_f32_16x16x32_bf16 v[48:51], a[216:219], v[132:135], v[48:51]\n"
  "buffer_load_dwordx4 v[120:123], v4, s[8:11], s38 offen offset:0 sc1\n"
  "v_mfma_f32_16x16x32_bf16 v[52:55], a[220:223], v[132:135], v[52:55]\n"
  "s_waitcnt vmcnt(16)\n"
  "v_mfma_f32_16x16x32_bf16 v[56:59], a[204:207], v[136:139], v[56:59]\n"
  "v_mfma_f32_16x16x32_bf16 v[60:63], a[208:211], v[136:139], v[60:63]\n"
  "buffer_load_dwordx4 v[124:127], v5, s[8:11], s38 offen offset:0 sc1\n"
  "v_mfma_f32_16x16x32_bf16 v[64:67], a[212:215], v[136:139], v[64:67]\n"
  "v_mfma_f32_16x16x32_bf16 v[68:71], a[216:219], v[136:139], v[68:71]\n"
  "v_mfma_f32_16x16x32_bf16 v[72:75], a[220:223], v[136:139], v[72:75]\n"
  "s_waitcnt vmcnt(16)\n"
  "v_mfma_f32_16x16x32_bf16 v[76:79], a[204:207], v[140:143], v[76:79]\n"
  "s_add_u32 s38, s46, 49152\n"
  "s_and_b32 s38, s38, 0xf000\n"
  "s_add_u32 s38, s38, s16\n"
  "buffer_load_dwordx4 v[128:131], v2, s[8:11], s38 offen offset:0 sc1\n"
  "v_mfma_f32_16x16x32_bf16 v[80:83], a[208:211], v[140:143], v[80:83]\n"
  "v_mfma_f32_16x16x32_bf16 v[84:87], a[212:215], v[140:143], v[84:87]\n"
  "v_mfma_f32_16x16x32_bf16 v[88:91], a[216:219], v[140:143], v[88:91]\n"
  "v_mfma_f32_16x16x32_bf16 v[92:95], a[220:223], v[140:143], v[92:95]\n"
  "ds_read_b128 v[132:135], v7 offset:0\n"
  "buffer_load_dwordx4 v[136:139], v3, s[8:11], s38 offen offset:0 sc1\n"
  "s_waitcnt vmcnt(17)\n"
  "v_mfma_f32_16x16x32_bf16 v[16:19], a[224:227], v[144:147], v[16:19]\n"
  "v_mfma_f32_16x16x32_bf16 v[20:23], a[228:231], v[144:147], v[20:23]\n"
  "v_mfma_f32_16x16x32_bf16 v[24:27], a[232:235], v[144:147], v[24:27]\n"
  "v_mfma_f32_16x16x32_bf16 v[28:31], a[236:239], v[144:147], v[28:31]\n"
  "buffer_load_dwordx4 v[140:143], v4, s[8:11], s38 offen offset:0 sc1\n"
  "v_mfma_f32_16x16x32_bf16 v[32:35], a[240:243], v[144:147], v[32:35]\n"
  "s_waitcnt vmcnt(17)\n"
  "v_mfma_f32_16x16x32_bf16 v[36:39], a[224:227], v[148:151], v[36:39]\n"
  "v_mfma_f32_16x16x32_bf16 v[40:43], a[228:231], v[148:151], v[40:43]\n"
  "v_mfma_f32_16x16x32_bf16 v[44:47], a[232:235], v[148:151], v[44:47]\n"
  "buffer_load_dwordx4 v[144:147], v5, s[8:11], s38 offen offset:0 sc1\n"
  "v_mfma_f32_16x16x32_bf16 v[48:51], a[236:239], v[148:151], v[48:51]\n"
  "v_mfma_f32_16x16x32_bf16 v[52:55], a[240:243], v[148:151], v[52:55]\n"
  "s_waitcnt vmcnt(17)\n"
  "v_mfma_f32_16x16x32_bf16 v[56:59], a[224:227], v[152:155], v[56:59]\n"
  "s_add_u32 s38, s46, 53248\n"
  "s_and_b32 s38, s38, 0xf000\n"
  "s_add_u32 s38, s38, s16\n"
  "buffer_load_dwordx4 v[148:151], v2, s[8:11], s38 offen offset:0 sc1\n"
  "v_mfma_f32_16x16x32_bf16 v[60:63], a[228:231], v[152:155], v[60:63]\n"
  "v_mfma_f32_16x16x32_bf16 v[64:67], a[232:235], v[152:155], v[64:67]\n"
  "v_mfma_f32_16x16x32_bf16 v[68:71], a[236:239], v[152:155], v[68:71]\n"
  "v_mfma_f32_16x16x32_bf16 v[72:75], a[240:243], v[152:155], v[72:75]\n"
  "ds_read_b128 v[176:179], v7 offset:1024\n"
  "ds_read_b128 v[180:183], v7 offset:2048\n"
  "ds_read_b128 v[184:187], v7 offset:3072\n"
  "ds_read_b128 v[188:191], v7 offset:4096\n"
  "ds_read_b128 v[192:195], v7 offset:5120\n"
  "buffer_load_dwordx4 v[196:199], v3, s[8:11], s38 offen offset:0 sc1\n"
  "s_waitcnt vmcnt(18)\n"
  "v_mfma_f32_16x16x32_bf16 v[76:79], a[224:227], v[156:159], v[76:79]\n"
  "v_mfma_f32_16x16x32_bf16 v[80:83], a[228:231], v[156:159], v[80:83]\n"
  "v_mfma_f32_16x16x32_bf16 v[84:87], a[232:235], v[156:159], v[84:87]\n"
  "v_mfma_f32_16x16x32_bf16 v[88:91], a[236:239], v[156:159], v[88:91]\n"
  "buffer_load_dwordx4 v[152:155], v4, s[8:11], s38 offen offset:0 sc1\n"
  "v_mfma_f32_16x16x32_bf16 v[92:95], a[240:243], v[156:159], v[92:95]\n"
  "s_waitcnt vmcnt(18)\n"
  "v_mfma_f32_16x16x32_bf16 v[16:19], a[244:247], v[160:163], v[16:19]\n"
  "v_mfma_f32_16x16x32_bf16 v[20:23], a[248:251], v[160:163], v[20:23]\n"
  "v_mfma_f32_16x16x32_bf16 v[24:27], a[252:255], v[160:163], v[24:27]\n"
  "buffer_load_dwordx4 v[156:159], v5, s[8:11], s38 offen offset:0 sc1\n"
  "v_mfma_f32_16x16x32_bf16 v[28:31], a[0:3], v[160:163], v[28:31]\n"
  "v_mfma_f32_16x16x32_bf16 v[32:35], a[4:7], v[160:163], v[32:35]\n"
  "s_waitcnt vmcnt(18)\n"
  "v_mfma_f32_16x16x32_bf16 v[36:39], a[244:247], v[164:167], v[36:39]\n"
  "v_mfma_f32_16x16x32_bf16 v[40:43], a[248:251], v[164:167], v[40:43]\n"
  "s_add_u32 s38, s46, 57344\n"
  "s_and_b32 s38, s38, 0xf000\n"
  "s_add_u32 s38, s38, s16\n"
  "buffer_load_dwordx4 v[160:163], v2, s[8:11], s38 offen offset:0 sc1\n"
  "v_mfma_f32_16x16x32_bf16 v[44:47], a[252:255], v[164:167], v[44:47]\n"
  "v_mfma_f32_16x16x32_bf16 v[48:51], a[0:3], v[164:167], v[48:51]\n"
  "v_mfma_f32_16x16x32_bf16 v[52:55], a[4:7], v[164:167], v[52:55]\n"
  "ds_read_b128 v[200:203], v7 offset:6144\n"
  "ds_read_b128 v[204:207], v7 offset:7168\n"
  "ds_read_b128 v[208:211], v7 offset:8192\n"
  "ds_read_b128 v[212:215], v7 offset:9216\n"
  "ds_read_b128 v[216:219], v7 offset:10240\n"
  "buffer_load_dwordx4 v[220:223], v3, s[8:11], s38 offen offset:0 sc1\n"
  "s_waitcnt vmcnt(19)\n"
  "v_mfma_f32_16x16x32_bf16 v[56:59], a[244:247], v[168:171], v[56:59]\n"
  "v_mfma_f32_16x16x32_bf16 v[60:63], a[248:251], v[168:171], v[60:63]\n"
  "v_mfma_f32_16x16x32_bf16 v[64:67], a[252:255], v[168:171], v[64:67]\n"
  "v_mfma_f32_16x16x32_bf16 v[68:71], a[0:3], v[168:171], v[68:71]\n"
  "buffer_load_dwordx4 v[164:167], v4, s[8:11], s38 offen offset:0 sc1\n"
  "v_mfma_f32_16x16x32_bf16 v[72:75], a[4:7], v[168:171], v[72:75]\n"
  "s_waitcnt vmcnt(19)\n"
  "v_mfma_f32_16x16x32_bf16 v[76:79], a[244:247], v[172:175], v[76:79]\n"
  "v_mfma_f32_16x16x32_bf16 v[80:83], a[248:251], v[172:175], v[80:83]\n"
  "v_mfma_f32_16x16x32_bf16 v[84:87], a[252:255], v[172:175], v[84:87]\n"
  "buffer_load_dwordx4 v[168:171], v5, s[8:11], s38 offen offset:0 sc1\n"
  "v_mfma_f32_16x16x32_bf16 v[88:91], a[0:3], v[172:175], v[88:91]\n"
  "v_mfma_f32_16x16x32_bf16 v[92:95], a[4:7], v[172:175], v[92:95]\n"
  "s_waitcnt vmcnt(19)\n"
  "v_mfma_f32_16x16x32_bf16 v[16:19], a[8:11], v[96:99], v[16:19]\n"
  "v_mfma_f32_16x16x32_bf16 v[20:23], a[12:15], v[96:99], v[20:23]\n"
  "s_add_u32 s38, s46, 61440\n"
  "s_and_b32 s38, s38, 0xf000\n"
  "s_add_u32 s38, s38, s16\n"
  "buffer_load_dwordx4 v[172:175], v2, s[8:11], s38 offen offset:0 sc1\n"
  "v_mfma_f32_16x16x32_bf16 v[24:27], a[16:19], v[96:99], v[24:27]\n"
  "v_mfma_f32_16x16x32_bf16 v[28:31], a[20:23], v[96:99], v[28:31]\n"
  "v_mfma_f32_16x16x32_bf16 v[32:35], a[24:27], v[96:99], v[32:35]\n"
  "s_waitcnt vmcnt(19)\n"
  "v_mfma_f32_16x16x32_bf16 v[36:39], a[8:11], v[100:103], v[36:39]\n"
  "ds_read_b128 v[96:99], v7 offset:11264\n"
  "ds_read_b128 v[224:227], v7 offset:12288\n"
  "ds_read_b128 v[228:231], v7 offset:13312\n"
  "ds_read_b128 v[232:235], v7 offset:14336\n"
  "ds_read_b128 v[236:239], v7 offset:15360\n"
  "buffer_load_dwordx4 v[240:243], v3, s[8:11], s38 offen offset:0 sc1\n"
  "v_mfma_f32_16x16x32_bf16 v[40:43], a[12:15], v[100:103], v[40:43]\n"
  "v_mfma_f32_16x16x32_bf16 v[44:47], a[16:19], v[100:103], v[44:47]\n"
  "v_mfma_f32_16x16x32_bf16 v[48:51], a[20:23], v[100:103], v[48:51]\n"
  "buffer_load_dwordx4 v[244:247], v4, s[8:11], s38 offen offset:0 sc1\n"
  "v_mfma_f32_16x16x32_bf16 v[52:55], a[24:27], v[100:103], v[52:55]\n"
  "s_waitcnt vmcnt(20)\n"
  "v_mfma_f32_16x16x32_bf16 v[56:59], a[8:11], v[104:107], v[56:59]\n"
  "v_mfma_f32_16x16x32_bf16 v[60:63], a[12:15], v[104:107], v[60:63]\n"
  "v_mfma_f32_16x16x32_bf16 v[64:67], a[16:19], v[104:107], v[64:67]\n"
  "buffer_load_dwordx4 v[100:103], v5, s[8:11], s38 offen offset:0 sc1\n"
  "v_mfma_f32_16x16x32_bf16 v[68:71], a[20:23], v[104:107], v[68:71]\n"
  "v_mfma_f32_16x16x32_bf16 v[72:75], a[24:27], v[104:107], v[72:75]\n"
  "s_waitcnt vmcnt(20)\n"
  "v_mfma_f32_16x16x32_bf16 v[76:79], a[8:11], v[108:111], v[76:79]\n"
  "v_mfma_f32_16x16x32_bf16 v[80:83], a[12:15], v[108:111], v[80:83]\n"
  "s_add_u32 s38, s46, 0\n"
  "s_and_b32 s38, s38, 0xf000\n"
  "s_add_u32 s38, s38, s21\n"
  "buffer_load_dwordx4 v[248:251], v2, s[8:11], s38 offen offset:0 sc1\n"
  "v_mfma_f32_16x16x32_bf16 v[84:87], a[16:19], v[108:111], v[84:87]\n"
  "v_mfma_f32_16x16x32_bf16 v[88:91], a[20:23], v[108:111], v[88:91]\n"
  "v_mfma_f32_16x16x32_bf16 v[92:95], a[24:27], v[108:111], v[92:95]\n"
  "s_waitcnt vmcnt(20)\n"
  "v_mfma_f32_16x16x32_bf16 v[16:19], a[28:31], v[112:115], v[16:19]\n"
  "buffer_load_dwordx4 v[252:255], v3, s[8:11], s38 offen offset:0 sc1\n"
  "v_mfma_f32_16x16x32_bf16 v[20:23], a[32:35], v[112:115], v[20:23]\n"
  "v_mfma_f32_16x16x32_bf16 v[24:27], a[36:39], v[112:115], v[24:27]\n"
  "v_mfma_f32_16x16x32_bf16 v[28:31], a[40:43], v[112:115], v[28:31]\n"
  "v_mfma_f32_16x16x32_bf16 v[32:35], a[44:47], v[112:115], v[32:35]\n"
  "s_waitcnt vmcnt(20)\n"
  "v_mfma_f32_16x16x32_bf16 v[36:39], a[28:31], v[116:119], v[36:39]\n"
  "v_mfma_f32_16x16x32_bf16 v[40:43], a[32:35], v[116:119], v[40:43]\n"
  "v_mfma_f32_16x16x32_bf16 v[44:47], a[36:39], v[116:119], v[44:47]\n"
  "v_mfma_f32_16x16x32_bf16 v[48:51], a[40:43], v[116:119], v[48:51]\n"
  "v_mfma_f32_16x16x32_bf16 v[52:55], a[44:47], v[116:119], v[52:55]\n"
  "s_waitcnt vmcnt(19)\n"
  "v_mfma_f32_16x16x32_bf16 v[56:59], a[28:31], v[120:123], v[56:59]\n"
  "v_mfma_f32_16x16x32_bf16 v[60:63], a[32:35], v[120:123], v[60:63]\n"
  "v_mfma_f32_16x16x32_bf16 v[64:67], a[36:39], v[120:123], v[64:67]\n"
  "v_mfma_f32_16x16x32_bf16 v[68:71], a[40:43], v[120:123], v[68:71]\n"
  "v_mfma_f32_16x16x32_bf16 v[72:75], a[44:47], v[120:123], v[72:75]\n"
  "s_waitcnt vmcnt(18)\n"
  "v_mfma_f32_16x16x32_bf16 v[76:79], a[28:31], v[124:127], v[76:79]\n"
  "v_mfma_f32_16x16x32_bf16 v[80:83], a[32:35], v[124:127], v[80:83]\n"
  "v_mfma_f32_16x16x32_bf16 v[84:87], a[36:39], v[124:127], v[84:87]\n"
  "v_mfma_f32_16x16x32_bf16 v[88:91], a[40:43], v[124:127], v[88:91]\n"
  "v_mfma_f32_16x16x32_bf16 v[92:95], a[44:47], v[124:127], v[92:95]\n"
  "s_waitcnt vmcnt(17)\n"
  "v_mfma_f32_16x16x32_bf16 v[16:19], a[48:51], v[128:131], v[16:19]\n"
  "v_mfma_f32_16x16x32_bf16 v[20:23], a[52:55], v[128:131], v[20:23]\n"
  "v_mfma_f32_16x16x32_bf16 v[24:27], a[56:59], v[128:131], v[24:27]\n"
  "v_mfma_f32_16x16x32_bf16 v[28:31], a[60:63], v[128:131], v[28:31]\n"
  "s_waitcnt lgkmcnt(15)\n"
  "v_mfma_f32_16x16x32_bf16 v[32:35], v[132:135], v[128:131], v[32:35]\n"
  "s_waitcnt vmcnt(16)\n"
  "v_mfma_f32_16x16x32_bf16 v[36:39], a[48:51], v[136:139], v[36:39]\n"
  "v_mfma_f32_16x16x32_bf16 v[40:43], a[52:55], v[136:139], v[40:43]\n"
  "v_mfma_f32_16x16x32_bf16 v[44:47], a[56:59], v[136:139], v[44:47]\n"
  "v_mfma_f32_16x16x32_bf16 v[48:51], a[60:63], v[136:139], v[48:51]\n"
  "v_mfma_f32_16x16x32_bf16 v[52:55], v[132:135], v[136:139], v[52:55]\n"
  "s_waitcnt vmcnt(15)\n"
  "v_mfma_f32_16x16x32_bf16 v[56:59], a[48:51], v[140:143], v[56:59]\n"
  "v_mfma_f32_16x16x32_bf16 v[60:63], a[52:55], v[140:143], v[60:63]\n"
  "v_mfma_f32_16x16x32_bf16 v[64:67], a[56:59], v[140:143], v[64:67]\n"
  "v_mfma_f32_16x16x32_bf16 v[68:71], a[60:63], v[140:143], v[68:71]\n"
  "v_mfma_f32_16x16x32_bf16 v[72:75], v[132:135], v[140:143], v[72:75]\n"
  "s_waitcnt vmcnt(14)\n"
  "v_mfma_f32_16x16x32_bf16 v[76:79], a[48:51], v[144:147], v[76:79]\n"
  "v_mfma_f32_16x16x32_bf16 v[80:83], a[52:55], v[144:147], v[80:83]\n"
  "v_mfma_f32_16x16x32_bf16 v[84:87], a[56:59], v[144:147], v[84:87]\n"
  "v_mfma_f32_16x16x32_bf16 v[88:91], a[60:63], v[144:147], v[88:91]\n"
  "v_mfma_f32_16x16x32_bf16 v[92:95], v[132:135], v[144:147], v[92:95]\n"
  "s_waitcnt vmcnt(13)\n"
  "s_waitcnt lgkmcnt(14)\n"
  "v_mfma_f32_16x16x32_bf16 v[16:19], v[176:179], v[148:151], v[16:19]\n"
  "s_waitcnt lgkmcnt(13)\n"
  "v_mfma_f32_16x16x32_bf16 v[20:23], v[180:183], v[148:151], v[20:23]\n"
  "s_waitcnt lgkmcnt(12)\n"
  "v_mfma_f32_16x16x32_bf16 v[24:27], v[184:187], v[148:151], v[24:27]\n"
  "s_waitcnt lgkmcnt(11)\n"
  "v_mfma_f32_16x16x32_bf16 v[28:31], v[188:191], v[148:151], v[28:31]\n"
  "s_waitcnt lgkmcnt(10)\n"
  "v_mfma_f32_16x16x32_bf16 v[32:35], v[192:195], v[148:151], v[32:35]\n"
  "s_waitcnt vmcnt(12)\n"
  "v_mfma_f32_16x16x32_bf16 v[36:39], v[176:179], v[196:199], v[36:39]\n"
  "v_mfma_f32_16x16x32_bf16 v[40:43], v[180:183], v[196:199], v[40:43]\n"
  "v_mfma_f32_16x16x32_bf16 v[44:47], v[184:187], v[196:199], v[44:47]\n"
  "v_mfma_f32_16x16x32_bf16 v[48:51], v[188:191], v[196:199], v[48:51]\n"
  "v_mfma_f32_16x16x32_bf16 v[52:55], v[192:195], v[196:199], v[52:55]\n"
  "s_waitcnt vmcnt(11)\n"
  "v_mfma_f32_16x16x32_bf16 v[56:59], v[176:179], v[152:155], v[56:59]\n"
  "buffer_load_dwordx4 v[196:199], v4, s[8:11], s38 offen offset:0 sc1\n"
  "v_mfma_f32_16x16x32_bf16 v[60:63], v[180:183], v[152:155], v[60:63]\n"
  "v_mfma_f32_16x16x32_bf16 v[64:67], v[184:187], v[152:155], v[64:67]\n"
  "v_mfma_f32_16x16x32_bf16 v[68:71], v[188:191], v[152:155], v[68:71]\n"
  "v_mfma_f32_16x16x32_bf16 v[72:75], v[192:195], v[152:155], v[72:75]\n"
  "s_waitcnt vmcnt(11)\n"
  "v_mfma_f32_16x16x32_bf16 v[76:79], v[176:179], v[156:159], v[76:79]\n"
  "v_mfma_f32_16x16x32_bf16 v[80:83], v[180:183], v[156:159], v[80:83]\n"
  "buffer_load_dwordx4 v[176:179], v5, s[8:11], s38 offen offset:0 sc1\n"
  "v_mfma_f32_16x16x32_bf16 v[84:87], v[184:187], v[156:159], v[84:87]\n"
  "s_add_u32 s38, s46, 4096\n"
  "s_and_b32 s38, s38, 0xf000\n"
  "s_add_u32 s38, s38, s21\n"
  "buffer_load_dwordx4 v[180:183], v2, s[8:11], s38 offen offset:0 sc1\n"
  "v_mfma_f32_16x16x32_bf16 v[88:91], v[188:191], v[156:159], v[88:91]\n"
  "v_mfma_f32_16x16x32_bf16 v[92:95], v[192:195], v[156:159], v[92:95]\n"
  "buffer_load_dwordx4 v[184:187], v3, s[8:11], s38 offen offset:0 sc1\n"
  "s_waitcnt vmcnt(13)\n"
  "s_waitcnt lgkmcnt(9)\n"
  "v_mfma_f32_16x16x32_bf16 v[16:19], v[200:203], v[160:163], v[16:19]\n"
  "s_waitcnt lgkmcnt(8)\n"
  "v_mfma_f32_16x16x32_bf16 v[20:23], v[204:207], v[160:163], v[20:23]\n"
  "s_waitcnt lgkmcnt(7)\n"
  "v_mfma_f32_16x16x32_bf16 v[24:27], v[208:211], v[160:163], v[24:27]\n"
  "s_waitcnt lgkmcnt(6)\n"
  "v_mfma_f32_16x16x32_bf16 v[28:31], v[212:215], v[160:163], v[28:31]\n"
  "buffer_load_dwordx4 v[188:191], v4, s[8:11], s38 offen offset:0 sc1\n"
  "s_waitcnt lgkmcnt(5)\n"
  "v_mfma_f32_16x16x32_bf16 v[32:35], v[216:219], v[160:163], v[32:35]\n"
  "s_waitcnt vmcnt(13)\n"
  "v_mfma_f32_16x16x32_bf16 v[36:39], v[200:203], v[220:223], v[36:39]\n"
  "v_mfma_f32_16x16x32_bf16 v[40:43], v[204:207], v[220:223], v[40:43]\n"
  "v_mfma_f32_16x16x32_bf16 v[44:47], v[208:211], v[220:223], v[44:47]\n"
  "buffer_load_dwordx4 v[192:195], v5, s[8:11], s38 offen offset:0 sc1\n"
  "v_mfma_f32_16x16x32_bf16 v[48:51], v[212:215], v[220:223], v[48:51]\n"
  "v_mfma_f32_16x16x32_bf16 v[52:55], v[216:219], v[220:223], v[52:55]\n"
  "s_waitcnt vmcnt(13)\n"
  "v_mfma_f32_16x16x32_bf16 v[56:59], v[200:203], v[164:167], v[56:59]\n"
  "v_mfma_f32_16x16x32_bf16 v[60:63], v[204:207], v[164:167], v[60:63]\n"
  "buffer_load_dword v11, off, s[8:11], s24 sc1\n"
  "v_mfma_f32_16x16x32_bf16 v[64:67], v[208:211], v[164:167], v[64:67]\n"
  "v_mfma_f32_16x16x32_bf16 v[68:71], v[212:215], v[164:167], v[68:71]\n"
  "v_mfma_f32_16x16x32_bf16 v[72:75], v[216:219], v[164:167], v[72:75]\n"
  "s_add_u32 s38, s46, 8192\n"
  "s_and_b32 s38, s38, 0xf000\n"
  "s_add_u32 s38, s38, s21\n"
  "buffer_load_dwordx4 v[220:223], v2, s[8:11], s38 offen offset:0 sc1\n"
  "s_waitcnt vmcnt(14)\n"
  "v_mfma_f32_16x16x32_bf16 v[76:79], v[200:203], v[168:171], v[76:79]\n"
  "v_mfma_f32_16x16x32_bf16 v[80:83], v[204:207], v[168:171], v[80:83]\n"
  "v_mfma_f32_16x16x32_bf16 v[84:87], v[208:211], v[168:171], v[84:87]\n"
  "v_mfma_f32_16x16x32_bf16 v[88:91], v[212:215], v[168:171], v[88:91]\n"
  "buffer_load_dwordx4 v[200:203], v3, s[8:11], s38 offen offset:0 sc1\n"
  "v_mfma_f32_16x16x32_bf16 v[92:95], v[216:219], v[168:171], v[92:95]\n"
  "s_waitcnt vmcnt(14)\n"
  "s_waitcnt lgkmcnt(4)\n"
  "v_mfma_f32_16x16x32_bf16 v[16:19], v[96:99], v[172:175], v[16:19]\n"
  "s_waitcnt lgkmcnt(3)\n"
  "v_mfma_f32_16x16x32_bf16 v[20:23], v[224:227], v[172:175], v[20:23]\n"
  "s_waitcnt lgkmcnt(2)\n"
  "v_mfma_f32_16x16x32_bf16 v[24:27], v[228:231], v[172:175], v[24:27]\n"
  "buffer_load_dwordx4 v[204:207], v4, s[8:11], s38 offen offset:0 sc1\n"
  "s_waitcnt lgkmcnt(1)\n"
  "v_mfma_f32_16x16x32_bf16 v[28:31], v[232:235], v[172:175], v[28:31]\n"
  "s_waitcnt lgkmcnt(0)\n"
  "v_mfma_f32_16x16x32_bf16 v[32:35], v[236:239], v[172:175], v[32:35]\n"
  "s_waitcnt vmcnt(14)\n"
  "v_mfma_f32_16x16x32_bf16 v[36:39], v[96:99], v[240:243], v[36:39]\n"
  "v_mfma_f32_16x16x32_bf16 v[40:43], v[224:227], v[240:243], v[40:43]\n"
  "buffer_load_dwordx4 v[208:211], v5, s[8:11], s38 offen offset:0 sc1\n"
  "v_mfma_f32_16x16x32_bf16 v[44:47], v[228:231], v[240:243], v[44:47]\n"
  "v_mfma_f32_16x16x32_bf16 v[48:51], v[232:235], v[240:243], v[48:51]\n"
  "v_mfma_f32_16x16x32_bf16 v[52:55], v[236:239], v[240:243], v[52:55]\n"
  "s_waitcnt vmcnt(14)\n"
  "v_mfma_f32_16x16x32_bf16 v[56:59], v[96:99], v[244:247], v[56:59]\n"
  "s_add_u32 s38, s46, 12288\n"
  "s_and_b32 s38, s38, 0xf000\n"
  "s_add_u32 s38, s38, s21\n"
  "buffer_load_dwordx4 v[212:215], v2, s[8:11], s38 offen offset:0 sc1\n"
  "v_mfma_f32_16x16x32_bf16 v[60:63], v[224:227], v[244:247], v[60:63]\n"
  "v_mfma_f32_16x16x32_bf16 v[64:67], v[228:231], v[244:247], v[64:67]\n"
  "v_mfma_f32_16x16x32_bf16 v[68:71], v[232:235], v[244:247], v[68:71]\n"
  "buffer_load_dwordx4 v[216:219], v3, s[8:11], s38 offen offset:0 sc1\n"
  "v_mfma_f32_16x16x32_bf16 v[72:75], v[236:239], v[244:247], v[72:75]\n"
  "s_waitcnt vmcnt(15)\n"
  "v_mfma_f32_16x16x32_bf16 v[76:79], v[96:99], v[100:103], v[76:79]\n"
  "v_mfma_f32_16x16x32_bf16 v[80:83], v[224:227], v[100:103], v[80:83]\n"
  "v_mfma_f32_16x16x32_bf16 v[84:87], v[228:231], v[100:103], v[84:87]\n"
  "buffer_load_dwordx4 v[240:243], v4, s[8:11], s38 offen offset:0 sc1\n"
  "v_mfma_f32_16x16x32_bf16 v[88:91], v[232:235], v[100:103], v[88:91]\n"
  "v_mfma_f32_16x16x32_bf16 v[92:95], v[236:239], v[100:103], v[92:95]\n"
  "s_waitcnt vmcnt(15)\n"
  "v_mfma_f32_16x16x32_bf16 v[96:99], a[64:67], v[248:251], 0\n"
  "v_mfma_f32_16x16x32_bf16 v[100:103], a[68:71], v[248:251], 0\n"
  "buffer_load_dwordx4 v[244:247], v5, s[8:11], s38 offen offset:0 sc1\n"
  "v_mfma_f32_16x16x32_bf16 v[104:107], a[72:75], v[248:251], 0\n"
  "ds_write_b128 v7, v[36:39] offset:16384\n"
  "v_mfma_f32_16x16x32_bf16 v[108:111], a[76:79], v[248:251], 0\n"
  "ds_write_b128 v7, v[40:43] offset:17408\n"
  "v_mfma_f32_16x16x32_bf16 v[112:115], a[80:83], v[248:251], 0\n"
  "ds_write_b128 v7, v[44:47] offset:18432\n"
  "s_waitcnt vmcnt(15)\n"
  "v_mfma_f32_16x16x32_bf16 v[116:119], a[64:67], v[252:255], 0\n"
  "s_add_u32 s38, s46, 16384\n"
  "s_and_b32 s38, s38, 0xf000\n"
  "s_add_u32 s38, s38, s21\n"
  "buffer_load_dwordx4 v[224:227], v2, s[8:11], s38 offen offset:0 sc1\n"
  "ds_write_b128 v7, v[48:51] offset:19456\n"
  "v_mfma_f32_16x16x32_bf16 v[120:123], a[68:71], v[252:255], 0\n"
  "ds_write_b128 v7, v[52:55] offset:20480\n"
  "v_mfma_f32_16x16x32_bf16 v[124:127], a[72:75], v[252:255], 0\n"
  "ds_write_b128 v7, v[56:59] offset:21504\n"
  "v_mfma_f32_16x16x32_bf16 v[128:131], a[76:79], v[252:255], 0\n"
  "ds_write_b128 v7, v[60:63] offset:22528\n"
  "v_mfma_f32_16x16x32_bf16 v[132:135], a[80:83], v[252:255], 0\n"
  "buffer_load_dwordx4 v[228:231], v3, s[8:11], s38 offen offset:0 sc1\n"
  "ds_write_b128 v7, v[64:67] offset:23552\n"
  "s_waitcnt vmcnt(16)\n"
  "v_mfma_f32_16x16x32_bf16 v[136:139], a[64:67], v[196:199], 0\n"
  "ds_write_b128 v7, v[68:71] offset:24576\n"
  "v_mfma_f32_16x16x32_bf16 v[140:143], a[68:71], v[196:199], 0\n"
  "ds_write_b128 v7, v[72:75] offset:25600\n"
  "v_mfma_f32_16x16x32_bf16 v[144:147], a[72:75], v[196:199], 0\n"
  "buffer_load_dwordx4 v[232:235], v4, s[8:11], s38 offen offset:0 sc1\n"
  "ds_write_b128 v7, v[76:79] offset:26624\n"
  "v_mfma_f32_16x16x32_bf16 v[148:151], a[76:79], v[196:199], 0\n"
  "ds_write_b128 v7, v[80:83] offset:27648\n"
  "v_mfma_f32_16x16x32_bf16 v[152:155], a[80:83], v[196:199], 0\n"
  "ds_write_b128 v7, v[84:87] offset:28672\n"
  "s_waitcnt vmcnt(16)\n"
  "v_mfma_f32_16x16x32_bf16 v[156:159], a[64:67], v[176:179], 0\n"
  "ds_write_b128 v7, v[88:91] offset:29696\n"
  "v_mfma_f32_16x16x32_bf16 v[160:163], a[68:71], v[176:179], 0\n"
  "buffer_load_dwordx4 a[64:67], v5, s[8:11], s38 offen offset:0 sc1\n"
  "ds_write_b128 v7, v[92:95] offset:30720\n"
  "v_mfma_f32_16x16x32_bf16 v[164:167], a[72:75], v[176:179], 0\n"
  "v_mfma_f32_16x16x32_bf16 v[168:171], a[76:79], v[176:179], 0\n"
  "v_mfma_f32_16x16x32_bf16 v[172:175], a[80:83], v[176:179], 0\n"
  "s_waitcnt vmcnt(16)\n"
  "v_mfma_f32_16x16x32_bf16 v[96:99], a[84:87], v[180:183], v[96:99]\n"
  "s_add_u32 s38, s46, 20480\n"
  "s_and_b32 s38, s38, 0xf000\n"
  "s_add_u32 s38, s38, s21\n"
  "buffer_load_dwordx4 a[68:71], v2, s[8:11], s38 offen offset:0 sc1\n"
  "v_mfma_f32_16x16x32_bf16 v[100:103], a[88:91], v[180:183], v[100:103]\n"
  "v_mfma_f32_16x16x32_bf16 v[104:107], a[92:95], v[180:183], v[104:107]\n"
  "s_waitcnt lgkmcnt(0)\n"
  "s_barrier\n"
  "v_mfma_f32_16x16x32_bf16 v[108:111], a[96:99], v[180:183], v[108:111]\n"
  "ds_read_b128 v[36:39], v8 offset:16384\n"
  "v_mfma_f32_16x16x32_bf16 v[112:115], a[100:103], v[180:183], v[112:115]\n"
  "buffer_load_dwordx4 a[72:75], v3, s[8:11], s38 offen offset:0 sc1\n"
  "ds_read_b128 v[40:43], v8 offset:17408\n"
  "s_waitcnt vmcnt(17)\n"
  "v_mfma_f32_16x16x32_bf16 v[116:119], a[84:87], v[184:187], v[116:119]\n"
  "ds_read_b128 v[44:47], v8 offset:18432\n"
  "v_mfma_f32_16x16x32_bf16 v[120:123], a[88:91], v[184:187], v[120:123]\n"
  "ds_read_b128 v[48:51], v8 offset:19456\n"
  "v_mfma_f32_16x16x32_bf16 v[124:127], a[92:95], v[184:187], v[124:127]\n"
  "ds_read_b128 v[52:55], v8 offset:20480\n"
  "v_mfma_f32_16x16x32_bf16 v[128:131], a[96:99], v[184:187], v[128:131]\n"
  "buffer_load_dwordx4 a[76:79], v4, s[8:11], s38 offen offset:0 sc1\n"
  "ds_read_b128 v[56:59], v9 offset:16384\n"
  "v_mfma_f32_16x16x32_bf16 v[132:135], a[100:103], v[184:187], v[132:135]\n"
  "ds_read_b128 v[60:63], v9 offset:17408\n"
  "s_waitcnt vmcnt(17)\n"
  "v_mfma_f32_16x16x32_bf16 v[136:139], a[84:87], v[188:191], v[136:139]\n"
  "ds_read_b128 v[64:67], v9 offset:18432\n"
  "v_mfma_f32_16x16x32_bf16 v[140:143], a[88:91], v[188:191], v[140:143]\n"
  "buffer_load_dwordx4 a[80:83], v5, s[8:11], s38 offen offset:0 sc1\n"
  "ds_read_b128 v[68:71], v9 offset:19456\n"
  "v_mfma_f32_16x16x32_bf16 v[144:147], a[92:95], v[188:191], v[144:147]\n"
  "ds_read_b128 v[72:75], v9 offset:20480\n"
  "v_mfma_f32_16x16x32_bf16 v[148:151], a[96:99], v[188:191], v[148:151]\n"
  "ds_read_b128 v[76:79], v10 offset:16384\n"
  "v_mfma_f32_16x16x32_bf16 v[152:155], a[100:103], v[188:191], v[152:155]\n"
  "ds_read_b128 v[80:83], v10 offset:17408\n"
  "s_waitcnt vmcnt(17)\n"
  "v_mfma_f32_16x16x32_bf16 v[156:159], a[84:87], v[192:195], v[156:159]\n"
  "s_add_u32 s38, s46, 24576\n"
  "s_and_b32 s38, s38, 0xf000\n"
  "s_add_u32 s38, s38, s21\n"
  "buffer_load_dwordx4 v[236:239], v2, s[8:11], s38 offen offset:0 sc1\n"
  "ds_read_b128 v[84:87], v10 offset:18432\n"
  "v_mfma_f32_16x16x32_bf16 v[160:163], a[88:91], v[192:195], v[160:163]\n"
  "ds_read_b128 v[88:91], v10 offset:19456\n"
  "v_mfma_f32_16x16x32_bf16 v[164:167], a[92:95], v[192:195], v[164:167]\n"
  "ds_read_b128 v[92:95], v10 offset:20480\n"
  "v_mfma_f32_16x16x32_bf16 v[168:171], a[96:99], v[192:195], v[168:171]\n"
  "v_mfma_f32_16x16x32_bf16 v[172:175], a[100:103], v[192:195], v[172:175]\n"
  "buffer_load_dwordx4 a[84:87], v3, s[8:11], s38 offen offset:0 sc1\n"
  "s_waitcnt vmcnt(17)\n"
  "v_mfma_f32_16x16x32_bf16 v[96:99], a[104:107], v[220:223], v[96:99]\n"
  "v_mfma_f32_16x16x32_bf16 v[100:103], a[108:111], v[220:223], v[100:103]\n"
  "v_mfma_f32_16x16x32_bf16 v[104:107], a[112:115], v[220:223], v[104:107]\n"
  "v_mfma_f32_16x16x32_bf16 v[108:111], a[116:119], v[220:223], v[108:111]\n"
  "buffer_load_dwordx4 a[88:91], v4, s[8:11], s38 offen offset:0 sc1\n"
  "s_waitcnt lgkmcnt(0)\n"
  "v_mfma_f32_16x16x32_bf16 v[112:115], a[120:123], v[220:223], v[112:115]\n"
  "v_add_f32 v16, v16, v36\n"
  "v_add_f32 v17, v17, v37\n"
  "s_waitcnt vmcnt(17)\n"
  "v_mfma_f32_16x16x32_bf16 v[116:119], a[104:107], v[200:203], v[116:119]\n"
  "v_add_f32 v18, v18, v38\n"
  "v_add_f32 v19, v19, v39\n"
  "v_mfma_f32_16x16x32_bf16 v[120:123], a[108:111], v[200:203], v[120:123]\n"
  "v_add_f32 v56, v56, v76\n"
  "v_add_f32 v57, v57, v77\n"
  "v_mfma_f32_16x16x32_bf16 v[124:127], a[112:115], v[200:203], v[124:127]\n"
  "buffer_load_dwordx4 a[92:95], v5, s[8:11], s38 offen offset:0 sc1\n"
  "v_add_f32 v58, v58, v78\n"
  "v_add_f32 v59, v59, v79\n"
  "v_mfma_f32_16x16x32_bf16 v[128:131], a[116:119], v[200:203], v[128:131]\n"
  "v_add_f32 v16, v16, v56\n"
  "v_add_f32 v17, v17, v57\n"
  "v_mfma_f32_16x16x32_bf16 v[132:135], a[120:123], v[200:203], v[132:135]\n"
  "v_add_f32 v18, v18, v58\n"
  "v_add_f32 v19, v19, v59\n"
  "s_waitcnt vmcnt(17)\n"
  "v_mfma_f32_16x16x32_bf16 v[136:139], a[104:107], v[204:207], v[136:139]\n"
  "s_add_u32 s38, s46, 28672\n"
  "s_and_b32 s38, s38, 0xf000\n"
  "s_add_u32 s38, s38, s21\n"
  "buffer_load_dwordx4 a[96:99], v2, s[8:11], s38 offen offset:0 sc1\n"
  "buffer_store_dwordx4 v[16:19], %0, s[8:11], s43 offen offset:0 sc1\n"
  "v_add_f32 v20, v20, v40\n"
  "v_mfma_f32_16x16x32_bf16 v[140:143], a[108:111], v[204:207], v[140:143]\n"
  "v_add_f32 v21, v21, v41\n"
  "v_add_f32 v22, v22, v42\n"
  "v_mfma_f32_16x16x32_bf16 v[144:147], a[112:115], v[204:207], v[144:147]\n"
  "v_add_f32 v23, v23, v43\n"
  "v_add_f32 v60, v60, v80\n"
  "v_mfma_f32_16x16x32_bf16 v[148:151], a[116:119], v[204:207], v[148:151]\n"
  "v_add_f32 v61, v61, v81\n"
  "v_add_f32 v62, v62, v82\n"
  "v_mfma_f32_16x16x32_bf16 v[152:155], a[120:123], v[204:207], v[152:155]\n"
  "buffer_load_dwordx4 a[100:103], v3, s[8:11], s38 offen offset:0 sc1\n"
  "v_add_f32 v63, v63, v83\n"
  "v_add_f32 v20, v20, v60\n"
  "s_waitcnt vmcnt(19)\n"
  "v_mfma_f32_16x16x32_bf16 v[156:159], a[104:107], v[208:211], v[156:159]\n"
  "v_add_f32 v21, v21, v61\n"
  "v_add_f32 v22, v22, v62\n"
  "v_mfma_f32_16x16x32_bf16 v[160:163], a[108:111], v[208:211], v[160:163]\n"
  "v_add_f32 v23, v23, v63\n"
  "buffer_store_dwordx4 v[20:23], %0, s[8:11], s43 offen offset:1024 sc1\n"
  "v_mfma_f32_16x16x32_bf16 v[164:167], a[112:115], v[208:211], v[164:167]\n"
  "v_add_f32 v24, v24, v44\n"
  "v_add_f32 v25, v25, v45\n"
  "v_mfma_f32_16x16x32_bf16 v[168:171], a[116:119], v[208:211], v[168:171]\n"
  "buffer_load_dwordx4 a[104:107], v4, s[8:11], s38 offen offset:0 sc1\n"
  "v_add_f32 v26, v26, v46\n"
  "v_add_f32 v27, v27, v47\n"
  "v_mfma_f32_16x16x32_bf16 v[172:175], a[120:123], v[208:211], v[172:175]\n"
  "v_add_f32 v64, v64, v84\n"
  "v_add_f32 v65, v65, v85\n"
  "s_waitcnt vmcnt(20)\n"
  "v_mfma_f32_16x16x32_bf16 v[96:99], a[124:127], v[212:215], v[96:99]\n"
  "v_add_f32 v66, v66, v86\n"
  "v_add_f32 v67, v67, v87\n"
  "v_mfma_f32_16x16x32_bf16 v[100:103], a[128:131], v[212:215], v[100:103]\n"
  "v_add_f32 v24, v24, v64\n"
  "v_add_f32 v25, v25, v65\n"
  "v_mfma_f32_16x16x32_bf16 v[104:107], a[132:135], v[212:215], v[104:107]\n"
  "buffer_load_dwordx4 a[108:111], v5, s[8:11], s38 offen offset:0 sc1\n"
  "v_add_f32 v26, v26, v66\n"
  "v_add_f32 v27, v27, v67\n"
  "v_mfma_f32_16x16x32_bf16 v[108:111], a[136:139], v[212:215], v[108:111]\n"
  "buffer_store_dwordx4 v[24:27], %0, s[8:11], s43 offen offset:2048 sc1\n"
  "v_add_f32 v28, v28, v48\n"
  "v_mfma_f32_16x16x32_bf16 v[112:115], a[140:143], v[212:215], v[112:115]\n"
  "v_add_f32 v29, v29, v49\n"
  "v_add_f32 v30, v30, v50\n"
  "s_waitcnt vmcnt(21)\n"
  "v_mfma_f32_16x16x32_bf16 v[116:119], a[124:127], v[216:219], v[116:119]\n"
  "v_add_f32 v31, v31, v51\n"
  "v_add_f32 v68, v68, v88\n"
  "v_mfma_f32_16x16x32_bf16 v[120:123], a[128:131], v[216:219], v[120:123]\n"
  "s_add_u32 s38, s46, 32768\n"
  "s_and_b32 s38, s38, 0xf000\n"
  "s_add_u32 s38, s38, s21\n"
  "buffer_load_dwordx4 a[112:115], v2, s[8:11], s38 offen offset:0 sc1\n"
  "v_add_f32 v69, v69, v89\n"
  "v_add_f32 v70, v70, v90\n"
  "v_mfma_f32_16x16x32_bf16 v[124:127], a[132:135], v[216:219], v[124:127]\n"
  "v_add_f32 v71, v71, v91\n"
  "v_add_f32 v28, v28, v68\n"
  "v_mfma_f32_16x16x32_bf16 v[128:131], a[136:139], v[216:219], v[128:131]\n"
  "v_add_f32 v29, v29, v69\n"
  "v_add_f32 v30, v30, v70\n"
  "v_mfma_f32_16x16x32_bf16 v[132:135], a[140:143], v[216:219], v[132:135]\n"
  "buffer_load_dwordx4 a[116:119], v3, s[8:11], s38 offen offset:0 sc1\n"
  "v_add_f32 v31, v31, v71\n"
  "buffer_store_dwordx4 v[28:31], %0, s[8:11], s43 offen offset:3072 sc1\n"
  "s_waitcnt vmcnt(23)\n"
  "v_mfma_f32_16x16x32_bf16 v[136:139], a[124:127], v[240:243], v[136:139]\n"
  "v_add_f32 v32, v32, v52\n"
  "v_add_f32 v33, v33, v53\n"
  "v_mfma_f32_16x16x32_bf16 v[140:143], a[128:131], v[240:243], v[140:143]\n"
  "v_add_f32 v34, v34, v54\n"
  "v_add_f32 v35, v35, v55\n"
  "v_mfma_f32_16x16x32_bf16 v[144:147], a[132:135], v[240:243], v[144:147]\n"
  "v_add_f32 v72, v72, v92\n"
  "v_add_f32 v73, v73, v93\n"
  "v_mfma_f32_16x16x32_bf16 v[148:151], a[136:139], v[240:243], v[148:151]\n"
  "buffer_load_dwordx4 a[120:123], v4, s[8:11], s38 offen offset:0 sc1\n"
  "v_add_f32 v74, v74, v94\n"
  "v_add_f32 v75, v75, v95\n"
  "v_mfma_f32_16x16x32_bf16 v[152:155], a[140:143], v[240:243], v[152:155]\n"
  "v_add_f32 v32, v32, v72\n"
  "v_add_f32 v33, v33, v73\n"
  "s_waitcnt vmcnt(23)\n"
  "v_mfma_f32_16x16x32_bf16 v[156:159], a[124:127], v[244:247], v[156:159]\n"
  "v_add_f32 v34, v34, v74\n"
  "v_add_f32 v35, v35, v75\n"
  "v_mfma_f32_16x16x32_bf16 v[160:163], a[128:131], v[244:247], v[160:163]\n"
  "buffer_store_dwordx4 v[32:35], %0, s[8:11], s45 offen offset:0 sc1\n"
  "v_mfma_f32_16x16x32_bf16 v[164:167], a[132:135], v[244:247], v[164:167]\n"
  "buffer_load_dwordx4 a[124:127], v5, s[8:11], s38 offen offset:0 sc1\n"
  "v_mfma_f32_16x16x32_bf16 v[168:171], a[136:139], v[244:247], v[168:171]\n"
  "v_mfma_f32_16x16x32_bf16 v[172:175], a[140:143], v[244:247], v[172:175]\n"
  "s_waitcnt vmcnt(24)\n"
  "v_mfma_f32_16x16x32_bf16 v[96:99], a[144:147], v[224:227], v[96:99]\n"
  "v_mfma_f32_16x16x32_bf16 v[100:103], a[148:151], v[224:227], v[100:103]\n"
  "s_add_u32 s38, s46, 36864\n"
  "s_and_b32 s38, s38, 0xf000\n"
  "s_add_u32 s38, s38, s21\n"
  "buffer_load_dwordx4 a[128:131], v2, s[8:11], s38 offen offset:0 sc1\n"
  "v_mfma_f32_16x16x32_bf16 v[104:107], a[152:155], v[224:227], v[104:107]\n"
  "v_mfma_f32_16x16x32_bf16 v[108:111], a[156:159], v[224:227], v[108:111]\n"
  "v_mfma_f32_16x16x32_bf16 v[112:115], a[160:163], v[224:227], v[112:115]\n"
  "s_waitcnt vmcnt(24)\n"
  "v_mfma_f32_16x16x32_bf16 v[116:119], a[144:147], v[228:231], v[116:119]\n"
  "buffer_load_dwordx4 a[132:135], v3, s[8:11], s38 offen offset:0 sc1\n"
  "v_mfma_f32_16x16x32_bf16 v[120:123], a[148:151], v[228:231], v[120:123]\n"
  "v_mfma_f32_16x16x32_bf16 v[124:127], a[152:155], v[228:231], v[124:127]\n"
  "v_mfma_f32_16x16x32_bf16 v[128:131], a[156:159], v[228:231], v[128:131]\n"
  "buffer_load_dwordx4 a[136:139], v4, s[8:11], s38 offen offset:0 sc1\n"
  "v_mfma_f32_16x16x32_bf16 v[132:135], a[160:163], v[228:231], v[132:135]\n"
  "s_waitcnt vmcnt(25)\n"
  "v_mfma_f32_16x16x32_bf16 v[136:139], a[144:147], v[232:235], v[136:139]\n"
  "v_mfma_f32_16x16x32_bf16 v[140:143], a[148:151], v[232:235], v[140:143]\n"
  "v_mfma_f32_16x16x32_bf16 v[144:147], a[152:155], v[232:235], v[144:147]\n"
  "buffer_load_dwordx4 a[140:143], v5, s[8:11], s38 offen offset:0 sc1\n"
  "v_mfma_f32_16x16x32_bf16 v[148:151], a[156:159], v[232:235], v[148:151]\n"
  "v_mfma_f32_16x16x32_bf16 v[152:155], a[160:163], v[232:235], v[152:155]\n"
  "s_waitcnt vmcnt(25)\n"
  "v_mfma_f32_16x16x32_bf16 v[156:159], a[144:147], a[64:67], v[156:159]\n"
  "v_mfma_f32_16x16x32_bf16 v[160:163], a[148:151], a[64:67], v[160:163]\n"
  "s_add_u32 s38, s46, 40960\n"
  "s_and_b32 s38, s38, 0xf000\n"
  "s_add_u32 s38, s38, s21\n"
  "buffer_load_dwordx4 a[144:147], v2, s[8:11], s38 offen offset:0 sc1\n"
  "v_mfma_f32_16x16x32_bf16 v[164:167], a[152:155], a[64:67], v[164:167]\n"
  "v_mfma_f32_16x16x32_bf16 v[168:171], a[156:159], a[64:67], v[168:171]\n"
  "v_mfma_f32_16x16x32_bf16 v[172:175], a[160:163], a[64:67], v[172:175]\n"
  "s_waitcnt vmcnt(25)\n"
  "v_mfma_f32_16x16x32_bf16 v[96:99], a[164:167], a[68:71], v[96:99]\n"
  "buffer_load_dwordx4 a[148:151], v3, s[8:11], s38 offen offset:0 sc1\n"
  "v_mfma_f32_16x16x32_bf16 v[100:103], a[168:171], a[68:71], v[100:103]\n"
  "v_mfma_f32_16x16x32_bf16 v[104:107], a[172:175], a[68:71], v[104:107]\n"
  "v_mfma_f32_16x16x32_bf16 v[108:111], a[176:179], a[68:71], v[108:111]\n"
  "v_mfma_f32_16x16x32_bf16 v[112:115], a[180:183], a[68:71], v[112:115]\n"
  "buffer_load_dwordx4 a[152:155], v4, s[8:11], s38 offen offset:0 sc1\n"
  "s_waitcnt vmcnt(26)\n"
  "v_mfma_f32_16x16x32_bf16 v[116:119], a[164:167], a[72:75], v[116:119]\n"
  "v_mfma_f32_16x16x32_bf16 v[120:123], a[168:171], a[72:75], v[120:123]\n"
  "v_mfma_f32_16x16x32_bf16 v[124:127], a[172:175], a[72:75], v[124:127]\n"
  "buffer_load_dwordx4 a[156:159], v5, s[8:11], s38 offen offset:0 sc1\n"
  "v_mfma_f32_16x16x32_bf16 v[128:131], a[176:179], a[72:75], v[128:131]\n"
  "v_mfma_f32_16x16x32_bf16 v[132:135], a[180:183], a[72:75], v[132:135]\n"
  "s_waitcnt vmcnt(26)\n"
  "v_mfma_f32_16x16x32_bf16 v[136:139], a[164:167], a[76:79], v[136:139]\n"
  "v_mfma_f32_16x16x32_bf16 v[140:143], a[168:171], a[76:79], v[140:143]\n"
  "s_add_u32 s38, s46, 45056\n"
  "s_and_b32 s38, s38, 0xf000\n"
  "s_add_u32 s38, s38, s21\n"
  "buffer_load_dwordx4 a[64:67], v2, s[8:11], s38 offen offset:0 sc1\n"
  "v_mfma_f32_16x16x32_bf16 v[144:147], a[172:175], a[76:79], v[144:147]\n"
  "v_mfma_f32_16x16x32_bf16 v[148:151], a[176:179], a[76:79], v[148:151]\n"
  "v_mfma_f32_16x16x32_bf16 v[152:155], a[180:183], a[76:79], v[152:155]\n"
  "s_waitcnt vmcnt(26)\n"
  "v_mfma_f32_16x16x32_bf16 v[156:159], a[164:167], a[80:83], v[156:159]\n"
  "buffer_load_dwordx4 a[160:163], v3, s[8:11], s38 offen offset:0 sc1\n"
  "v_mfma_f32_16x16x32_bf16 v[160:163], a[168:171], a[80:83], v[160:163]\n"
  "v_mfma_f32_16x16x32_bf16 v[164:167], a[172:175], a[80:83], v[164:167]\n"
  "v_mfma_f32_16x16x32_bf16 v[168:171], a[176:179], a[80:83], v[168:171]\n"
  "v_mfma_f32_16x16x32_bf16 v[172:175], a[180:183], a[80:83], v[172:175]\n"
  "buffer_load_dwordx4 a[68:71], v4, s[8:11], s38 offen offset:0 sc1\n"
  "s_waitcnt vmcnt(27)\n"
  "v_mfma_f32_16x16x32_bf16 v[96:99], a[184:187], v[236:239], v[96:99]\n"
  "s_waitcnt vmcnt(12)\n"
  "buffer_atomic_add v13, v12, s[8:11], s41 offen sc1\n"
  "v_mfma_f32_16x16x32_bf16 v[100:103], a[188:191], v[236:239], v[100:103]\n"
  "v_mfma_f32_16x16x32_bf16 v[104:107], a[192:195], v[236:239], v[104:107]\n"
  "v_mfma_f32_16x16x32_bf16 v[108:111], a[196:199], v[236:239], v[108:111]\n"
  "buffer_load_dwordx4 a[72:75], v5, s[8:11], s38 offen offset:0 sc1\n"
  "v_mfma_f32_16x16x32_bf16 v[112:115], a[200:203], v[236:239], v[112:115]\n"
  "v_mfma_f32_16x16x32_bf16 v[116:119], a[184:187], a[84:87], v[116:119]\n"
  "v_mfma_f32_16x16x32_bf16 v[120:123], a[188:191], a[84:87], v[120:123]\n"
  "s_add_u32 s38, s46, 49152\n"
  "s_and_b32 s38, s38, 0xf000\n"
  "s_add_u32 s38, s38, s21\n"
  "buffer_load_dwordx4 a[76:79], v2, s[8:11], s38 offen offset:0 sc1\n"
  "v_mfma_f32_16x16x32_bf16 v[124:127], a[192:195], a[84:87], v[124:127]\n"
  "v_mfma_f32_16x16x32_bf16 v[128:131], a[196:199], a[84:87], v[128:131]\n"
  "v_mfma_f32_16x16x32_bf16 v[132:135], a[200:203], a[84:87], v[132:135]\n"
  "v_mfma_f32_16x16x32_bf16 v[136:139], a[184:187], a[88:91], v[136:139]\n"
  "ds_read_b128 a[164:167], v7 offset:0\n"
  "buffer_load_dwordx4 a[168:171], v3, s[8:11], s38 offen offset:0 sc1\n"
  "v_mfma_f32_16x16x32_bf16 v[140:143], a[188:191], a[88:91], v[140:143]\n"
  "v_mfma_f32_16x16x32_bf16 v[144:147], a[192:195], a[88:91], v[144:147]\n"
  "v_mfma_f32_16x16x32_bf16 v[148:151], a[196:199], a[88:91], v[148:151]\n"
  "v_mfma_f32_16x16x32_bf16 v[152:155], a[200:203], a[88:91], v[152:155]\n"
  "buffer_load_dwordx4 a[172:175], v4, s[8:11], s38 offen offset:0 sc1\n"
  "v_mfma_f32_16x16x32_bf16 v[156:159], a[184:187], a[92:95], v[156:159]\n"
  "v_mfma_f32_16x16x32_bf16 v[160:163], a[188:191], a[92:95], v[160:163]\n"
  "v_mfma_f32_16x16x32_bf16 v[164:167], a[192:195], a[92:95], v[164:167]\n"
  "v_mfma_f32_16x16x32_bf16 v[168:171], a[196:199], a[92:95], v[168:171]\n"
  "buffer_load_dwordx4 a[176:179], v5, s[8:11], s38 offen offset:0 sc1\n"
  "v_mfma_f32_16x16x32_bf16 v[172:175], a[200:203], a[92:95], v[172:175]\n"
  "v_mfma_f32_16x16x32_bf16 v[96:99], a[204:207], a[96:99], v[96:99]\n"
  "v_mfma_f32_16x16x32_bf16 v[100:103], a[208:211], a[96:99], v[100:103]\n"
  "v_mfma_f32_16x16x32_bf16 v[104:107], a[212:215], a[96:99], v[104:107]\n"
  "s_add_u32 s38, s46, 53248\n"
  "s_and_b32 s38, s38, 0xf000\n"
  "s_add_u32 s38, s38, s21\n"
  "buffer_load_dwordx4 a[80:83], v2, s[8:11], s38 offen offset:0 sc1\n"
  "v_mfma_f32_16x16x32_bf16 v[108:111], a[216:219], a[96:99], v[108:111]\n"
  "v_mfma_f32_16x16x32_bf16 v[112:115], a[220:223], a[96:99], v[112:115]\n"
  "v_mfma_f32_16x16x32_bf16 v[116:119], a[204:207], a[100:103], v[116:119]\n"
  "ds_read_b128 a[180:183], v7 offset:1024\n"
  "ds_read_b128 a[84:87], v7 offset:2048\n"
  "ds_read_b128 a[88:91], v7 offset:3072\n"
  "ds_read_b128 a[184:187], v7 offset:4096\n"
  "ds_read_b128 a[188:191], v7 offset:5120\n"
  "buffer_load_dwordx4 a[192:195], v3, s[8:11], s38 offen offset:0 sc1\n"
  "v_mfma_f32_16x16x32_bf16 v[120:123], a[208:211], a[100:103], v[120:123]\n"
  "v_mfma_f32_16x16x32_bf16 v[124:127], a[212:215], a[100:103], v[124:127]\n"
  "v_mfma_f32_16x16x32_bf16 v[128:131], a[216:219], a[100:103], v[128:131]\n"
  "v_mfma_f32_16x16x32_bf16 v[132:135], a[220:223], a[100:103], v[132:135]\n"
  "buffer_load_dwordx4 a[196:199], v4, s[8:11], s38 offen offset:0 sc1\n"
  "v_mfma_f32_16x16x32_bf16 v[136:139], a[204:207], a[104:107], v[136:139]\n"
  "v_mfma_f32_16x16x32_bf16 v[140:143], a[208:211], a[104:107], v[140:143]\n"
  "v_mfma_f32_16x16x32_bf16 v[144:147], a[212:215], a[104:107], v[144:147]\n"
  "v_mfma_f32_16x16x32_bf16 v[148:151], a[216:219], a[104:107], v[148:151]\n"
  "buffer_load_dwordx4 a[92:95], v5, s[8:11], s38 offen offset:0 sc1\n"
  "v_mfma_f32_16x16x32_bf16 v[152:155], a[220:223], a[104:107], v[152:155]\n"
  "v_mfma_f32_16x16x32_bf16 v[156:159], a[204:207], a[108:111], v[156:159]\n"
  "v_mfma_f32_16x16x32_bf16 v[160:163], a[208:211], a[108:111], v[160:163]\n"
  "v_mfma_f32_16x16x32_bf16 v[164:167], a[212:215], a[108:111], v[164:167]\n"
  "s_add_u32 s38, s46, 57344\n"
  "s_and_b32 s38, s38, 0xf000\n"
  "s_add_u32 s38, s38, s21\n"
  "buffer_load_dwordx4 a[200:203], v2, s[8:11], s38 offen offset:0 sc1\n"
  "v_mfma_f32_16x16x32_bf16 v[168:171], a[216:219], a[108:111], v[168:171]\n"
  "v_mfma_f32_16x16x32_bf16 v[172:175], a[220:223], a[108:111], v[172:175]\n"
  "v_mfma_f32_16x16x32_bf16 v[96:99], a[224:227], a[112:115], v[96:99]\n"
  "v_mfma_f32_16x16x32_bf16 v[100:103], a[228:231], a[112:115], v[100:103]\n"
  "ds_read_b128 a[96:99], v7 offset:6144\n"
  "ds_read_b128 a[100:103], v7 offset:7168\n"
  "ds_read_b128 a[104:107], v7 offset:8192\n"
  "ds_read_b128 a[204:207], v7 offset:9216\n"
  "ds_read_b128 a[208:211], v7 offset:10240\n"
  "buffer_load_dwordx4 a[212:215], v3, s[8:11], s38 offen offset:0 sc1\n"
  "v_mfma_f32_16x16x32_bf16 v[104:107], a[232:235], a[112:115], v[104:107]\n"
  "v_mfma_f32_16x16x32_bf16 v[108:111], a[236:239], a[112:115], v[108:111]\n"
  "v_mfma_f32_16x16x32_bf16 v[112:115], a[240:243], a[112:115], v[112:115]\n"
  "buffer_load_dwordx4 a[216:219], v4, s[8:11], s38 offen offset:0 sc1\n"
  "v_mfma_f32_16x16x32_bf16 v[116:119], a[224:227], a[116:119], v[116:119]\n"
  "v_mfma_f32_16x16x32_bf16 v[120:123], a[228:231], a[116:119], v[120:123]\n"
  "v_mfma_f32_16x16x32_bf16 v[124:127], a[232:235], a[116:119], v[124:127]\n"
  "v_mfma_f32_16x16x32_bf16 v[128:131], a[236:239], a[116:119], v[128:131]\n"
  "buffer_load_dwordx4 a[108:111], v5, s[8:11], s38 offen offset:0 sc1\n"
  "v_mfma_f32_16x16x32_bf16 v[132:135], a[240:243], a[116:119], v[132:135]\n"
  "v_mfma_f32_16x16x32_bf16 v[136:139], a[224:227], a[120:123], v[136:139]\n"
  "v_mfma_f32_16x16x32_bf16 v[140:143], a[228:231], a[120:123], v[140:143]\n"
  "v_mfma_f32_16x16x32_bf16 v[144:147], a[232:235], a[120:123], v[144:147]\n"
  "s_add_u32 s38, s46, 61440\n"
  "s_and_b32 s38, s38, 0xf000\n"
  "s_add_u32 s38, s38, s21\n"
  "buffer_load_dwordx4 a[220:223], v2, s[8:11], s38 offen offset:0 sc1\n"
  "v_mfma_f32_16x16x32_bf16 v[148:151], a[236:239], a[120:123], v[148:151]\n"
  "v_mfma_f32_16x16x32_bf16 v[152:155], a[240:243], a[120:123], v[152:155]\n"
  "s_waitcnt vmcnt(26)\n"
  "v_mfma_f32_16x16x32_bf16 v[156:159], a[224:227], a[124:127], v[156:159]\n"
  "v_mfma_f32_16x16x32_bf16 v[160:163], a[228:231], a[124:127], v[160:163]\n"
  "ds_read_b128 a[112:115], v7 offset:11264\n"
  "ds_read_b128 a[116:119], v7 offset:12288\n"
  "ds_read_b128 a[120:123], v7 offset:13312\n"
  "ds_read_b128 a[224:227], v7 offset:14336\n"
  "ds_read_b128 v[248:251], v7 offset:15360\n"
  "buffer_load_dwordx4 v[252:255], v3, s[8:11], s38 offen offset:0 sc1\n"
  "v_mfma_f32_16x16x32_bf16 v[164:167], a[232:235], a[124:127], v[164:167]\n"
  "v_mfma_f32_16x16x32_bf16 v[168:171], a[236:239], a[124:127], v[168:171]\n"
  "v_mfma_f32_16x16x32_bf16 v[172:175], a[240:243], a[124:127], v[172:175]\n"
  "s_waitcnt vmcnt(26)\n"
  "v_mfma_f32_16x16x32_bf16 v[96:99], a[244:247], a[128:131], v[96:99]\n"
  "buffer_load_dwordx4 a[228:231], v4, s[8:11], s38 offen offset:0 sc1\n"
  "v_mfma_f32_16x16x32_bf16 v[100:103], a[248:251], a[128:131], v[100:103]\n"
  "v_mfma_f32_16x16x32_bf16 v[104:107], a[252:255], a[128:131], v[104:107]\n"
  "v_mfma_f32_16x16x32_bf16 v[108:111], a[0:3], a[128:131], v[108:111]\n"
  "buffer_load_dwordx4 a[232:235], v5, s[8:11], s38 offen offset:0 sc1\n"
  "v_mfma_f32_16x16x32_bf16 v[112:115], a[4:7], a[128:131], v[112:115]\n"
  "s_waitcnt vmcnt(27)\n"
  "v_mfma_f32_16x16x32_bf16 v[116:119], a[244:247], a[132:135], v[116:119]\n"
  "v_mfma_f32_16x16x32_bf16 v[120:123], a[248:251], a[132:135], v[120:123]\n"
  "v_mfma_f32_16x16x32_bf16 v[124:127], a[252:255], a[132:135], v[124:127]\n"
  "v_readfirstlane_b32 s26, v11\n"
  "s_cmp_eq_u32 s26, 32\n"
  "s_cbranch_scc0 b_spin%=\n"
  "b_ok%=:\n"
  "v_mfma_f32_16x16x32_bf16 v[128:131], a[0:3], a[132:135], v[128:131]\n"
  "v_mfma_f32_16x16x32_bf16 v[132:135], a[4:7], a[132:135], v[132:135]\n"
  "s_waitcnt vmcnt(26)\n"
  "v_mfma_f32_16x16x32_bf16 v[136:139], a[244:247], a[136:139], v[136:139]\n"
  "v_mfma_f32_16x16x32_bf16 v[140:143], a[248:251], a[136:139], v[140:143]\n"
  "s_add_u32 s38, s19, 0\n"
  "buffer_load_dwordx4 a[236:239], v2, s[8:11], s38 offen offset:0 sc1\n"
  "v_mfma_f32_16x16x32_bf16 v[144:147], a[252:255], a[136:139], v[144:147]\n"
  "v_mfma_f32_16x16x32_bf16 v[148:151], a[0:3], a[136:139], v[148:151]\n"
  "v_mfma_f32_16x16x32_bf16 v[152:155], a[4:7], a[136:139], v[152:155]\n"
  "s_waitcnt vmcnt(26)\n"
  "v_mfma_f32_16x16x32_bf16 v[156:159], a[244:247], a[140:143], v[156:159]\n"
  "s_add_u32 s39, s20, 0\n"
  "buffer_load_dwordx4 a[124:127], %0, s[8:11], s39 offen offset:0 sc1\n"
  "v_mfma_f32_16x16x32_bf16 v[160:163], a[248:251], a[140:143], v[160:163]\n"
  "v_mfma_f32_16x16x32_bf16 v[164:167], a[252:255], a[140:143], v[164:167]\n"
  "v_mfma_f32_16x16x32_bf16 v[168:171], a[0:3], a[140:143], v[168:171]\n"
  "v_mfma_f32_16x16x32_bf16 v[172:175], a[4:7], a[140:143], v[172:175]\n"
  "buffer_load_dwordx4 a[240:243], %0, s[8:11], s39 offen offset:1024 sc1\n"
  "s_waitcnt vmcnt(27)\n"
  "v_mfma_f32_16x16x32_bf16 v[96:99], a[8:11], a[144:147], v[96:99]\n"
  "v_mfma_f32_16x16x32_bf16 v[100:103], a[12:15], a[144:147], v[100:103]\n"
  "v_mfma_f32_16x16x32_bf16 v[104:107], a[16:19], a[144:147], v[104:107]\n"
  "buffer_load_dwordx4 a[128:131], %0, s[8:11], s39 offen offset:2048 sc1\n"
  "v_mfma_f32_16x16x32_bf16 v[108:111], a[20:23], a[144:147], v[108:111]\n"
  "v_mfma_f32_16x16x32_bf16 v[112:115], a[24:27], a[144:147], v[112:115]\n"
  "s_waitcnt vmcnt(27)\n"
  "v_mfma_f32_16x16x32_bf16 v[116:119], a[8:11], a[148:151], v[116:119]\n"
  "v_mfma_f32_16x16x32_bf16 v[120:123], a[12:15], a[148:151], v[120:123]\n"
  "buffer_load_dwordx4 a[132:135], %0, s[8:11], s39 offen offset:3072 sc1\n"
  "v_mfma_f32_16x16x32_bf16 v[124:127], a[16:19], a[148:151], v[124:127]\n"
  "v_mfma_f32_16x16x32_bf16 v[128:131], a[20:23], a[148:151], v[128:131]\n"
  "v_mfma_f32_16x16x32_bf16 v[132:135], a[24:27], a[148:151], v[132:135]\n"
  "s_waitcnt vmcnt(27)\n"
  "v_mfma_f32_16x16x32_bf16 v[136:139], a[8:11], a[152:155], v[136:139]\n"
  "s_add_u32 s39, s20, 4096\n"
  "buffer_load_dwordx4 a[136:139], %0, s[8:11], s39 offen offset:0 sc1\n"
  "v_mfma_f32_16x16x32_bf16 v[140:143], a[12:15], a[152:155], v[140:143]\n"
  "v_mfma_f32_16x16x32_bf16 v[144:147], a[16:19], a[152:155], v[144:147]\n"
  "v_mfma_f32_16x16x32_bf16 v[148:151], a[20:23], a[152:155], v[148:151]\n"
  "v_mfma_f32_16x16x32_bf16 v[152:155], a[24:27], a[152:155], v[152:155]\n"
  "buffer_load_dwordx4 a[244:247], v3, s[8:11], s38 offen offset:0 sc1\n"
  "s_waitcnt vmcnt(28)\n"
  "v_mfma_f32_16x16x32_bf16 v[156:159], a[8:11], a[156:159], v[156:159]\n"
  "v_mfma_f32_16x16x32_bf16 v[160:163], a[12:15], a[156:159], v[160:163]\n"
  "v_mfma_f32_16x16x32_bf16 v[164:167], a[16:19], a[156:159], v[164:167]\n"
  "v_mfma_f32_16x16x32_bf16 v[168:171], a[20:23], a[156:159], v[168:171]\n"
  "buffer_load_dwordx4 a[248:251], v4, s[8:11], s38 offen offset:0 sc1\n"
  "v_mfma_f32_16x16x32_bf16 v[172:175], a[24:27], a[156:159], v[172:175]\n"
  "s_waitcnt vmcnt(28)\n"
  "v_mfma_f32_16x16x32_bf16 v[96:99], a[28:31], a[64:67], v[96:99]\n"
  "v_mfma_f32_16x16x32_bf16 v[100:103], a[32:35], a[64:67], v[100:103]\n"
  "buffer_load_dwordx4 a[252:255], v5, s[8:11], s38 offen offset:0 sc1\n"
  "v_mfma_f32_16x16x32_bf16 v[104:107], a[36:39], a[64:67], v[104:107]\n"
  "v_mfma_f32_16x16x32_bf16 v[108:111], a[40:43], a[64:67], v[108:111]\n"
  "v_mfma_f32_16x16x32_bf16 v[112:115], a[44:47], a[64:67], v[112:115]\n"
  "s_waitcnt vmcnt(28)\n"
  "v_mfma_f32_16x16x32_bf16 v[116:119], a[28:31], a[160:163], v[116:119]\n"
  "s_add_u32 s38, s19, 4096\n"
  "buffer_load_dwordx4 a[0:3], v2, s[8:11], s38 offen offset:0 sc1\n"
  "v_mfma_f32_16x16x32_bf16 v[120:123], a[32:35], a[160:163], v[120:123]\n"
  "v_mfma_f32_16x16x32_bf16 v[124:127], a[36:39], a[160:163], v[124:127]\n"
  "v_mfma_f32_16x16x32_bf16 v[128:131], a[40:43], a[160:163], v[128:131]\n"
  "v_mfma_f32_16x16x32_bf16 v[132:135], a[44:47], a[160:163], v[132:135]\n"
  "buffer_load_dwordx4 a[140:143], %0, s[8:11], s39 offen offset:1024 sc1\n"
  "s_waitcnt vmcnt(29)\n"
  "v_mfma_f32_16x16x32_bf16 v[136:139], a[28:31], a[68:71], v[136:139]\n"
  "v_mfma_f32_16x16x32_bf16 v[140:143], a[32:35], a[68:71], v[140:143]\n"
  "v_mfma_f32_16x16x32_bf16 v[144:147], a[36:39], a[68:71], v[144:147]\n"
  "v_mfma_f32_16x16x32_bf16 v[148:151], a[40:43], a[68:71], v[148:151]\n"
  "buffer_load_dwordx4 a[4:7], %0, s[8:11], s39 offen offset:2048 sc1\n"
  "v_mfma_f32_16x16x32_bf16 v[152:155], a[44:47], a[68:71], v[152:155]\n"
  "s_waitcnt vmcnt(28)\n"
  "v_mfma_f32_16x16x32_bf16 v[156:159], a[28:31], a[72:75], v[156:159]\n"
  "v_mfma_f32_16x16x32_bf16 v[160:163], a[32:35], a[72:75], v[160:163]\n"
  "v_mfma_f32_16x16x32_bf16 v[164:167], a[36:39], a[72:75], v[164:167]\n"
  "buffer_load_dwordx4 a[144:147], %0, s[8:11], s39 offen offset:3072 sc1\n"
  "v_mfma_f32_16x16x32_bf16 v[168:171], a[40:43], a[72:75], v[168:171]\n"
  "v_mfma_f32_16x16x32_bf16 v[172:175], a[44:47], a[72:75], v[172:175]\n"
  "s_waitcnt vmcnt(28)\n"
  "v_mfma_f32_16x16x32_bf16 v[96:99], a[48:51], a[76:79], v[96:99]\n"
  "s_add_u32 s39, s20, 8192\n"
  "buffer_load_dwordx4 a[148:151], %0, s[8:11], s39 offen offset:0 sc1\n"
  "v_mfma_f32_16x16x32_bf16 v[100:103], a[52:55], a[76:79], v[100:103]\n"
  "v_mfma_f32_16x16x32_bf16 v[104:107], a[56:59], a[76:79], v[104:107]\n"
  "v_mfma_f32_16x16x32_bf16 v[108:111], a[60:63], a[76:79], v[108:111]\n"
  "s_waitcnt lgkmcnt(15)\n"
  "v_mfma_f32_16x16x32_bf16 v[112:115], a[164:167], a[76:79], v[112:115]\n"
  "buffer_load_dwordx4 a[152:155], %0, s[8:11], s39 offen offset:1024 sc1\n"
  "s_waitcnt vmcnt(29)\n"
  "v_mfma_f32_16x16x32_bf16 v[116:119], a[48:51], a[168:171], v[116:119]\n"
  "v_mfma_f32_16x16x32_bf16 v[120:123], a[52:55], a[168:171], v[120:123]\n"
  "v_mfma_f32_16x16x32_bf16 v[124:127], a[56:59], a[168:171], v[124:127]\n"
  "v_mfma_f32_16x16x32_bf16 v[128:131], a[60:63], a[168:171], v[128:131]\n"
  "buffer_load_dwordx4 a[8:11], v3, s[8:11], s38 offen offset:0 sc1\n"
  "v_mfma_f32_16x16x32_bf16 v[132:135], a[164:167], a[168:171], v[132:135]\n"
  "s_waitcnt vmcnt(29)\n"
  "v_mfma_f32_16x16x32_bf16 v[136:139], a[48:51], a[172:175], v[136:139]\n"
  "v_mfma_f32_16x16x32_bf16 v[140:143], a[52:55], a[172:175], v[140:143]\n"
  "v_mfma_f32_16x16x32_bf16 v[144:147], a[56:59], a[172:175], v[144:147]\n"
  "buffer_load_dwordx4 a[12:15], v4, s[8:11], s38 offen offset:0 sc1\n"
  "v_mfma_f32_16x16x32_bf16 v[148:151], a[60:63], a[172:175], v[148:151]\n"
  "v_mfma_f32_16x16x32_bf16 v[152:155], a[164:167], a[172:175], v[152:155]\n"
  "s_waitcnt vmcnt(29)\n"
  "v_mfma_f32_16x16x32_bf16 v[156:159], a[48:51], a[176:179], v[156:159]\n"
  "v_mfma_f32_16x16x32_bf16 v[160:163], a[52:55], a[176:179], v[160:163]\n"
  "buffer_load_dwordx4 a[16:19], v5, s[8:11], s38 offen offset:0 sc1\n"
  "v_mfma_f32_16x16x32_bf16 v[164:167], a[56:59], a[176:179], v[164:167]\n"
  "v_mfma_f32_16x16x32_bf16 v[168:171], a[60:63], a[176:179], v[168:171]\n"
  "v_mfma_f32_16x16x32_bf16 v[172:175], a[164:167], a[176:179], v[172:175]\n"
  "s_add_u32 s38, s19, 8192\n"
  "buffer_load_dwordx4 a[20:23], v2, s[8:11], s38 offen offset:0 sc1\n"
  "s_waitcnt vmcnt(30)\n"
  "s_waitcnt lgkmcnt(14)\n"
  "v_mfma_f32_16x16x32_bf16 v[96:99], a[180:183], a[80:83], v[96:99]\n"
  "s_waitcnt lgkmcnt(13)\n"
  "v_mfma_f32_16x16x32_bf16 v[100:103], a[84:87], a[80:83], v[100:103]\n"
  "s_waitcnt lgkmcnt(12)\n"
  "v_mfma_f32_16x16x32_bf16 v[104:107], a[88:91], a[80:83], v[104:107]\n"
  "s_waitcnt lgkmcnt(11)\n"
  "v_mfma_f32_16x16x32_bf16 v[108:111], a[184:187], a[80:83], v[108:111]\n"
  "buffer_load_dwordx4 a[156:159], %0, s[8:11], s39 offen offset:2048 sc1\n"
  "s_waitcnt lgkmcnt(10)\n"
  "v_mfma_f32_16x16x32_bf16 v[112:115], a[188:191], a[80:83], v[112:115]\n"
  "s_waitcnt vmcnt(30)\n"
  "v_mfma_f32_16x16x32_bf16 v[116:119], a[180:183], a[192:195], v[116:119]\n"
  "v_mfma_f32_16x16x32_bf16 v[120:123], a[84:87], a[192:195], v[120:123]\n"
  "v_mfma_f32_16x16x32_bf16 v[124:127], a[88:91], a[192:195], v[124:127]\n"
  "buffer_load_dwordx4 a[24:27], %0, s[8:11], s39 offen offset:3072 sc1\n"
  "v_mfma_f32_16x16x32_bf16 v[128:131], a[184:187], a[192:195], v[128:131]\n"
  "v_mfma_f32_16x16x32_bf16 v[132:135], a[188:191], a[192:195], v[132:135]\n"
  "s_waitcnt vmcnt(30)\n"
  "v_mfma_f32_16x16x32_bf16 v[136:139], a[180:183], a[196:199], v[136:139]\n"
  "v_mfma_f32_16x16x32_bf16 v[140:143], a[84:87], a[196:199], v[140:143]\n"
  "s_add_u32 s39, s20, 12288\n"
  "buffer_load_dwordx4 a[64:67], %0, s[8:11], s39 offen offset:0 sc1\n"
  "v_mfma_f32_16x16x32_bf16 v[144:147], a[88:91], a[196:199], v[144:147]\n"
  "v_mfma_f32_16x16x32_bf16 v[148:151], a[184:187], a[196:199], v[148:151]\n"
  "v_mfma_f32_16x16x32_bf16 v[152:155], a[188:191], a[196:199], v[152:155]\n"
  "s_waitcnt vmcnt(30)\n"
  "v_mfma_f32_16x16x32_bf16 v[156:159], a[180:183], a[92:95], v[156:159]\n"
  "buffer_load_dwordx4 a[160:163], %0, s[8:11], s39 offen offset:1024 sc1\n"
  "v_mfma_f32_16x16x32_bf16 v[160:163], a[84:87], a[92:95], v[160:163]\n"
  "v_mfma_f32_16x16x32_bf16 v[164:167], a[88:91], a[92:95], v[164:167]\n"
  "v_mfma_f32_16x16x32_bf16 v[168:171], a[184:187], a[92:95], v[168:171]\n"
  "buffer_load_dwordx4 a[68:71], %0, s[8:11], s39 offen offset:2048 sc1\n"
  "v_mfma_f32_16x16x32_bf16 v[172:175], a[188:191], a[92:95], v[172:175]\n"
  "s_waitcnt vmcnt(31)\n"
  "s_waitcnt lgkmcnt(9)\n"
  "v_mfma_f32_16x16x32_bf16 v[96:99], a[96:99], a[200:203], v[96:99]\n"
  "s_waitcnt lgkmcnt(8)\n"
  "v_mfma_f32_16x16x32_bf16 v[100:103], a[100:103], a[200:203], v[100:103]\n"
  "s_waitcnt lgkmcnt(7)\n"
  "v_mfma_f32_16x16x32_bf16 v[104:107], a[104:107], a[200:203], v[104:107]\n"
  "buffer_load_dwordx4 a[28:31], v3, s[8:11], s38 offen offset:0 sc1\n"
  "s_waitcnt lgkmcnt(6)\n"
  "v_mfma_f32_16x16x32_bf16 v[108:111], a[204:207], a[200:203], v[108:111]\n"
  "s_waitcnt lgkmcnt(5)\n"
  "v_mfma_f32_16x16x32_bf16 v[112:115], a[208:211], a[200:203], v[112:115]\n"
  "s_waitcnt vmcnt(31)\n"
  "v_mfma_f32_16x16x32_bf16 v[116:119], a[96:99], a[212:215], v[116:119]\n"
  "v_mfma_f32_16x16x32_bf16 v[120:123], a[100:103], a[212:215], v[120:123]\n"
  "buffer_load_dwordx4 a[32:35], v4, s[8:11], s38 offen offset:0 sc1\n"
  "v_mfma_f32_16x16x32_bf16 v[124:127], a[104:107], a[212:215], v[124:127]\n"
  "v_mfma_f32_16x16x32_bf16 v[128:131], a[204:207], a[212:215], v[128:131]\n"
  "v_mfma_f32_16x16x32_bf16 v[132:135], a[208:211], a[212:215], v[132:135]\n"
  "s_waitcnt vmcnt(31)\n"
  "v_mfma_f32_16x16x32_bf16 v[136:139], a[96:99], a[216:219], v[136:139]\n"
  "buffer_load_dwordx4 a[36:39], v5, s[8:11], s38 offen offset:0 sc1\n"
  "v_mfma_f32_16x16x32_bf16 v[140:143], a[100:103], a[216:219], v[140:143]\n"
  "v_mfma_f32_16x16x32_bf16 v[144:147], a[104:107], a[216:219], v[144:147]\n"
  "v_mfma_f32_16x16x32_bf16 v[148:151], a[204:207], a[216:219], v[148:151]\n"
  "v_mfma_f32_16x16x32_bf16 v[152:155], a[208:211], a[216:219], v[152:155]\n"
  "buffer_load_dword v15, off, s[8:11], s40 sc1\n"
  "s_waitcnt vmcnt(32)\n"
  "v_mfma_f32_16x16x32_bf16 v[156:159], a[96:99], a[108:111], v[156:159]\n"
  "v_mfma_f32_16x16x32_bf16 v[160:163], a[100:103], a[108:111], v[160:163]\n"
  "v_mfma_f32_16x16x32_bf16 v[164:167], a[104:107], a[108:111], v[164:167]\n"
  "s_add_u32 s38, s19, 12288\n"
  "buffer_load_dwordx4 a[40:43], v2, s[8:11], s38 offen offset:0 sc1\n"
  "v_mfma_f32_16x16x32_bf16 v[168:171], a[204:207], a[108:111], v[168:171]\n"
  "v_mfma_f32_16x16x32_bf16 v[172:175], a[208:211], a[108:111], v[172:175]\n"
  "s_waitcnt vmcnt(32)\n"
  "s_waitcnt lgkmcnt(4)\n"
  "v_mfma_f32_16x16x32_bf16 v[96:99], a[112:115], a[220:223], v[96:99]\n"
  "s_waitcnt lgkmcnt(3)\n"
  "v_mfma_f32_16x16x32_bf16 v[100:103], a[116:119], a[220:223], v[100:103]\n"
  "buffer_load_dwordx4 a[72:75], %0, s[8:11], s39 offen offset:3072 sc1\n"
  "s_waitcnt lgkmcnt(2)\n"
  "v_mfma_f32_16x16x32_bf16 v[104:107], a[120:123], a[220:223], v[104:107]\n"
  "s_waitcnt lgkmcnt(1)\n"
  "v_mfma_f32_16x16x32_bf16 v[108:111], a[224:227], a[220:223], v[108:111]\n"
  "s_waitcnt lgkmcnt(0)\n"
  "v_mfma_f32_16x16x32_bf16 v[112:115], v[248:251], a[220:223], v[112:115]\n"
  "s_waitcnt vmcnt(32)\n"
  "v_mfma_f32_16x16x32_bf16 v[116:119], a[112:115], v[252:255], v[116:119]\n"
  "s_add_u32 s39, s20, 16384\n"
  "buffer_load_dwordx4 a[44:47], %0, s[8:11], s39 offen offset:0 sc1\n"
  "v_mfma_f32_16x16x32_bf16 v[120:123], a[116:119], v[252:255], v[120:123]\n"
  "v_mfma_f32_16x16x32_bf16 v[124:127], a[120:123], v[252:255], v[124:127]\n"
  "v_mfma_f32_16x16x32_bf16 v[128:131], a[224:227], v[252:255], v[128:131]\n"
  "v_mfma_f32_16x16x32_bf16 v[132:135], v[248:251], v[252:255], v[132:135]\n"
  "buffer_load_dwordx4 a[76:79], %0, s[8:11], s39 offen offset:1024 sc1\n"
  "s_waitcnt vmcnt(33)\n"
  "v_mfma_f32_16x16x32_bf16 v[136:139], a[112:115], a[228:231], v[136:139]\n"
  "v_mfma_f32_16x16x32_bf16 v[140:143], a[116:119], a[228:231], v[140:143]\n"
  "v_mfma_f32_16x16x32_bf16 v[144:147], a[120:123], a[228:231], v[144:147]\n"
  "v_mfma_f32_16x16x32_bf16 v[148:151], a[224:227], a[228:231], v[148:151]\n"
  "buffer_load_dwordx4 a[168:171], %0, s[8:11], s39 offen offset:2048 sc1\n"
  "v_mfma_f32_16x16x32_bf16 v[152:155], v[248:251], a[228:231], v[152:155]\n"
  "s_waitcnt vmcnt(33)\n"
  "v_mfma_f32_16x16x32_bf16 v[156:159], a[112:115], a[232:235], v[156:159]\n"
  "v_mfma_f32_16x16x32_bf16 v[160:163], a[116:119], a[232:235], v[160:163]\n"
  "buffer_load_dwordx4 a[172:175], %0, s[8:11], s39 offen offset:3072 sc1\n"
  "v_mfma_f32_16x16x32_bf16 v[164:167], a[120:123], a[232:235], v[164:167]\n"
  "v_mfma_f32_16x16x32_bf16 v[168:171], a[224:227], a[232:235], v[168:171]\n"
  "v_mfma_f32_16x16x32_bf16 v[172:175], v[248:251], a[232:235], v[172:175]\n"
  "s_waitcnt vmcnt(32)\n"
  "v_mfma_f32_16x16x32_bf16 v[16:19], a[124:127], a[236:239], 0\n"
  "buffer_load_dwordx4 a[48:51], v3, s[8:11], s38 offen offset:0 sc1\n"
  "s_waitcnt vmcnt(32)\n"
  "v_mfma_f32_16x16x32_bf16 v[20:23], a[240:243], a[236:239], 0\n"
  "s_waitcnt vmcnt(31)\n"
  "v_mfma_f32_16x16x32_bf16 v[24:27], a[128:131], a[236:239], 0\n"
  "ds_write_b128 v7, v[116:119] offset:0\n"
  "s_waitcnt vmcnt(30)\n"
  "v_mfma_f32_16x16x32_bf16 v[28:31], a[132:135], a[236:239], 0\n"
  "ds_write_b128 v7, v[120:123] offset:1024\n"
  "s_waitcnt vmcnt(29)\n"
  "v_mfma_f32_16x16x32_bf16 v[32:35], a[136:139], a[236:239], 0\n"
  "buffer_load_dwordx4 a[52:55], v4, s[8:11], s38 offen offset:0 sc1\n"
  "ds_write_b128 v7, v[124:127] offset:2048\n"
  "s_waitcnt vmcnt(29)\n"
  "v_mfma_f32_16x16x32_bf16 v[36:39], a[124:127], a[244:247], 0\n"
  "ds_write_b128 v7, v[128:131] offset:3072\n"
  "v_mfma_f32_16x16x32_bf16 v[40:43], a[240:243], a[244:247], 0\n"
  "ds_write_b128 v7, v[132:135] offset:4096\n"
  "v_mfma_f32_16x16x32_bf16 v[44:47], a[128:131], a[244:247], 0\n"
  "ds_write_b128 v7, v[136:139] offset:5120\n"
  "v_mfma_f32_16x16x32_bf16 v[48:51], a[132:135], a[244:247], 0\n"
  "buffer_load_dwordx4 a[56:59], v5, s[8:11], s38 offen offset:0 sc1\n"
  "ds_write_b128 v7, v[140:143] offset:6144\n"
  "v_mfma_f32_16x16x32_bf16 v[52:55], a[136:139], a[244:247], 0\n"
  "ds_write_b128 v7, v[144:147] offset:7168\n"
  "s_waitcnt vmcnt(29)\n"
  "v_mfma_f32_16x16x32_bf16 v[56:59], a[124:127], a[248:251], 0\n"
  "ds_write_b128 v7, v[148:151] offset:8192\n"
  "v_mfma_f32_16x16x32_bf16 v[60:63], a[240:243], a[248:251], 0\n"
  "ds_write_b128 v7, v[152:155] offset:9216\n"
  "v_mfma_f32_16x16x32_bf16 v[64:67], a[128:131], a[248:251], 0\n"
  "s_add_u32 s38, s19, 16384\n"
  "buffer_load_dwordx4 a[60:63], v2, s[8:11], s38 offen offset:0 sc1\n"
  "ds_write_b128 v7, v[156:159] offset:10240\n"
  "v_mfma_f32_16x16x32_bf16 v[68:71], a[132:135], a[248:251], 0\n"
  "ds_write_b128 v7, v[160:163] offset:11264\n"
  "v_mfma_f32_16x16x32_bf16 v[72:75], a[136:139], a[248:251], 0\n"
  "ds_write_b128 v7, v[164:167] offset:12288\n"
  "s_waitcnt vmcnt(29)\n"
  "v_mfma_f32_16x16x32_bf16 v[76:79], a[124:127], a[252:255], 0\n"
  "s_add_u32 s39, s20, 20480\n"
  "buffer_load_dwordx4 a[176:179], %0, s[8:11], s39 offen offset:0 sc1\n"
  "ds_write_b128 v7, v[168:171] offset:13312\n"
  "v_mfma_f32_16x16x32_bf16 v[80:83], a[240:243], a[252:255], 0\n"
  "ds_write_b128 v7, v[172:175] offset:14336\n"
  "v_mfma_f32_16x16x32_bf16 v[84:87], a[128:131], a[252:255], 0\n"
  "v_mfma_f32_16x16x32_bf16 v[88:91], a[132:135], a[252:255], 0\n"
  "v_mfma_f32_16x16x32_bf16 v[92:95], a[136:139], a[252:255], 0\n"
  "buffer_load_dwordx4 a[164:167], %0, s[8:11], s39 offen offset:1024 sc1\n"
  "s_waitcnt vmcnt(29)\n"
  "v_mfma_f32_16x16x32_bf16 v[16:19], a[140:143], a[0:3], v[16:19]\n"
  "s_waitcnt vmcnt(28)\n"
  "v_mfma_f32_16x16x32_bf16 v[20:23], a[4:7], a[0:3], v[20:23]\n"
  "s_waitcnt vmcnt(27)\n"
  "v_mfma_f32_16x16x32_bf16 v[24:27], a[144:147], a[0:3], v[24:27]\n"
  "s_waitcnt lgkmcnt(0)\n"
  "s_barrier\n"
  "s_waitcnt vmcnt(26)\n"
  "v_mfma_f32_16x16x32_bf16 v[28:31], a[148:151], a[0:3], v[28:31]\n"
  "buffer_load_dwordx4 a[80:83], %0, s[8:11], s39 offen offset:2048 sc1\n"
  "ds_read_b128 v[116:119], v8 offset:0\n"
  "s_waitcnt vmcnt(26)\n"
  "v_mfma_f32_16x16x32_bf16 v[32:35], a[152:155], a[0:3], v[32:35]\n"
  "ds_read_b128 v[120:123], v8 offset:1024\n"
  "s_waitcnt vmcnt(25)\n"
  "v_mfma_f32_16x16x32_bf16 v[36:39], a[140:143], a[8:11], v[36:39]\n"
  "ds_read_b128 v[124:127], v8 offset:2048\n"
  "v_mfma_f32_16x16x32_bf16 v[40:43], a[4:7], a[8:11], v[40:43]\n"
  "ds_read_b128 v[128:131], v8 offset:3072\n"
  "v_mfma_f32_16x16x32_bf16 v[44:47], a[144:147], a[8:11], v[44:47]\n"
  "buffer_load_dwordx4 a[192:195], %0, s[8:11], s39 offen offset:3072 sc1\n"
  "ds_read_b128 v[132:135], v8 offset:4096\n"
  "v_mfma_f32_16x16x32_bf16 v[48:51], a[148:151], a[8:11], v[48:51]\n"
  "ds_read_b128 v[136:139], v9 offset:0\n"
  "v_mfma_f32_16x16x32_bf16 v[52:55], a[152:155], a[8:11], v[52:55]\n"
  "ds_read_b128 v[140:143], v9 offset:1024\n"
  "s_waitcnt vmcnt(25)\n"
  "v_mfma_f32_16x16x32_bf16 v[56:59], a[140:143], a[12:15], v[56:59]\n"
  "ds_read_b128 v[144:147], v9 offset:2048\n"
  "v_mfma_f32_16x16x32_bf16 v[60:63], a[4:7], a[12:15], v[60:63]\n"
  "s_add_u32 s39, s20, 24576\n"
  "buffer_load_dwordx4 a[196:199], %0, s[8:11], s39 offen offset:0 sc1\n"
  "ds_read_b128 v[148:151], v9 offset:3072\n"
  "v_mfma_f32_16x16x32_bf16 v[64:67], a[144:147], a[12:15], v[64:67]\n"
  "ds_read_b128 v[152:155], v9 offset:4096\n"
  "v_mfma_f32_16x16x32_bf16 v[68:71], a[148:151], a[12:15], v[68:71]\n"
  "ds_read_b128 v[156:159], v10 offset:0\n"
  "v_mfma_f32_16x16x32_bf16 v[72:75], a[152:155], a[12:15], v[72:75]\n"
  "buffer_load_dwordx4 a[180:183], v3, s[8:11], s38 offen offset:0 sc1\n"
  "ds_read_b128 v[160:163], v10 offset:1024\n"
  "s_waitcnt vmcnt(26)\n"
  "v_mfma_f32_16x16x32_bf16 v[76:79], a[140:143], a[16:19], v[76:79]\n"
  "ds_read_b128 v[164:167], v10 offset:2048\n"
  "v_mfma_f32_16x16x32_bf16 v[80:83], a[4:7], a[16:19], v[80:83]\n"
  "ds_read_b128 v[168:171], v10 offset:3072\n"
  "v_mfma_f32_16x16x32_bf16 v[84:87], a[144:147], a[16:19], v[84:87]\n"
  "ds_read_b128 v[172:175], v10 offset:4096\n"
  "v_mfma_f32_16x16x32_bf16 v[88:91], a[148:151], a[16:19], v[88:91]\n"
  "buffer_load_dwordx4 a[84:87], v4, s[8:11], s38 offen offset:0 sc1\n"
  "v_mfma_f32_16x16x32_bf16 v[92:95], a[152:155], a[16:19], v[92:95]\n"
  "s_waitcnt vmcnt(25)\n"
  "v_mfma_f32_16x16x32_bf16 v[16:19], a[156:159], a[20:23], v[16:19]\n"
  "s_waitcnt vmcnt(24)\n"
  "v_mfma_f32_16x16x32_bf16 v[20:23], a[24:27], a[20:23], v[20:23]\n"
  "s_waitcnt vmcnt(23)\n"
  "v_mfma_f32_16x16x32_bf16 v[24:27], a[64:67], a[20:23], v[24:27]\n"
  "buffer_load_dwordx4 a[88:91], v5, s[8:11], s38 offen offset:0 sc1\n"
  "s_waitcnt vmcnt(23)\n"
  "v_mfma_f32_16x16x32_bf16 v[28:31], a[160:163], a[20:23], v[28:31]\n"
  "s_waitcnt lgkmcnt(0)\n"
  "s_waitcnt vmcnt(22)\n"
  "v_mfma_f32_16x16x32_bf16 v[32:35], a[68:71], a[20:23], v[32:35]\n"
  "v_add_f32 v96, v96, v116\n"
  "v_add_f32 v97, v97, v117\n"
  "s_waitcnt vmcnt(21)\n"
  "v_mfma_f32_16x16x32_bf16 v[36:39], a[156:159], a[28:31], v[36:39]\n"
  "v_add_f32 v98, v98, v118\n"
  "v_add_f32 v99, v99, v119\n"
  "v_mfma_f32_16x16x32_bf16 v[40:43], a[24:27], a[28:31], v[40:43]\n"
  "v_add_f32 v136, v136, v156\n"
  "v_add_f32 v137, v137, v157\n"
  "v_mfma_f32_16x16x32_bf16 v[44:47], a[64:67], a[28:31], v[44:47]\n"
  "v_add_f32 v138, v138, v158\n"
  "v_add_f32 v139, v139, v159\n"
  "v_mfma_f32_16x16x32_bf16 v[48:51], a[160:163], a[28:31], v[48:51]\n"
  "v_add_f32 v96, v96, v136\n"
  "v_add_f32 v97, v97, v137\n"
  "v_mfma_f32_16x16x32_bf16 v[52:55], a[68:71], a[28:31], v[52:55]\n"
  "v_add_f32 v98, v98, v138\n"
  "v_add_f32 v99, v99, v139\n"
  "s_waitcnt vmcnt(20)\n"
  "v_mfma_f32_16x16x32_bf16 v[56:59], a[156:159], a[32:35], v[56:59]\n"
  "v_add_f32 v100, v100, v120\n"
  "v_add_f32 v101, v101, v121\n"
  "v_mfma_f32_16x16x32_bf16 v[60:63], a[24:27], a[32:35], v[60:63]\n"
  "v_add_f32 v102, v102, v122\n"
  "v_add_f32 v103, v103, v123\n"
  "v_mfma_f32_16x16x32_bf16 v[64:67], a[64:67], a[32:35], v[64:67]\n"
  "v_add_f32 v140, v140, v160\n"
  "v_add_f32 v141, v141, v161\n"
  "v_mfma_f32_16x16x32_bf16 v[68:71], a[160:163], a[32:35], v[68:71]\n"
  "v_add_f32 v142, v142, v162\n"
  "v_add_f32 v143, v143, v163\n"
  "v_mfma_f32_16x16x32_bf16 v[72:75], a[68:71], a[32:35], v[72:75]\n"
  "v_add_f32 v100, v100, v140\n"
  "v_add_f32 v101, v101, v141\n"
  "s_waitcnt vmcnt(19)\n"
  "v_mfma_f32_16x16x32_bf16 v[76:79], a[156:159], a[36:39], v[76:79]\n"
  "v_add_f32 v102, v102, v142\n"
  "v_add_f32 v103, v103, v143\n"
  "v_mfma_f32_16x16x32_bf16 v[80:83], a[24:27], a[36:39], v[80:83]\n"
  "v_add_f32 v104, v104, v124\n"
  "v_add_f32 v105, v105, v125\n"
  "v_mfma_f32_16x16x32_bf16 v[84:87], a[64:67], a[36:39], v[84:87]\n"
  "v_add_f32 v106, v106, v126\n"
  "v_add_f32 v107, v107, v127\n"
  "v_mfma_f32_16x16x32_bf16 v[88:91], a[160:163], a[36:39], v[88:91]\n"
  "v_add_f32 v144, v144, v164\n"
  "v_add_f32 v145, v145, v165\n"
  "v_mfma_f32_16x16x32_bf16 v[92:95], a[68:71], a[36:39], v[92:95]\n"
  "v_add_f32 v146, v146, v166\n"
  "v_add_f32 v147, v147, v167\n"
  "s_waitcnt vmcnt(16)\n"
  "v_mfma_f32_16x16x32_bf16 v[16:19], a[72:75], a[40:43], v[16:19]\n"
  "v_readfirstlane_b32 s26, v15\n"
  "s_cmp_eq_u32 s26, 4\n"
  "s_cbranch_scc0 x_spin%=\n"
  "x_ok%=:\n"
  "v_add_f32 v104, v104, v144\n"
  "v_add_f32 v105, v105, v145\n"
  "s_waitcnt vmcnt(15)\n"
  "v_mfma_f32_16x16x32_bf16 v[20:23], a[44:47], a[40:43], v[20:23]\n"
  "buffer_load_dwordx4 v[196:199], %0, s[8:11], s42 offen offset:0 sc1\n"
  "v_add_f32 v106, v106, v146\n"
  "v_add_f32 v107, v107, v147\n"
  "s_waitcnt vmcnt(15)\n"
  "v_mfma_f32_16x16x32_bf16 v[24:27], a[76:79], a[40:43], v[24:27]\n"
  "v_add_f32 v108, v108, v128\n"
  "v_add_f32 v109, v109, v129\n"
  "s_waitcnt vmcnt(14)\n"
  "v_mfma_f32_16x16x32_bf16 v[28:31], a[168:171], a[40:43], v[28:31]\n"
  "buffer_load_dwordx4 v[176:179], %0, s[8:11], s42 offen offset:1024 sc1\n"
  "v_add_f32 v110, v110, v130\n"
  "v_add_f32 v111, v111, v131\n"
  "s_waitcnt vmcnt(14)\n"
  "v_mfma_f32_16x16x32_bf16 v[32:35], a[172:175], a[40:43], v[32:35]\n"
  "v_add_f32 v148, v148, v168\n"
  "v_add_f32 v149, v149, v169\n"
  "s_waitcnt vmcnt(13)\n"
  "v_mfma_f32_16x16x32_bf16 v[36:39], a[72:75], a[48:51], v[36:39]\n"
  "v_add_f32 v150, v150, v170\n"
  "v_add_f32 v151, v151, v171\n"
  "v_mfma_f32_16x16x32_bf16 v[40:43], a[44:47], a[48:51], v[40:43]\n"
  "v_add_f32 v108, v108, v148\n"
  "v_add_f32 v109, v109, v149\n"
  "v_mfma_f32_16x16x32_bf16 v[44:47], a[76:79], a[48:51], v[44:47]\n"
  "buffer_load_dwordx4 v[180:183], %0, s[8:11], s42 offen offset:2048 sc1\n"
  "v_add_f32 v110, v110, v150\n"
  "v_add_f32 v111, v111, v151\n"
  "v_mfma_f32_16x16x32_bf16 v[48:51], a[168:171], a[48:51], v[48:51]\n"
  "v_add_f32 v112, v112, v132\n"
  "v_add_f32 v113, v113, v133\n"
  "v_mfma_f32_16x16x32_bf16 v[52:55], a[172:175], a[48:51], v[52:55]\n"
  "v_add_f32 v114, v114, v134\n"
  "v_add_f32 v115, v115, v135\n"
  "s_waitcnt vmcnt(13)\n"
  "v_mfma_f32_16x16x32_bf16 v[56:59], a[72:75], a[52:55], v[56:59]\n"
  "v_add_f32 v152, v152, v172\n"
  "v_add_f32 v153, v153, v173\n"
  "v_mfma_f32_16x16x32_bf16 v[60:63], a[44:47], a[52:55], v[60:63]\n"
  "buffer_load_dwordx4 v[184:187], %0, s[8:11], s42 offen offset:3072 sc1\n"
  "v_add_f32 v154, v154, v174\n"
  "v_add_f32 v155, v155, v175\n"
  "v_mfma_f32_16x16x32_bf16 v[64:67], a[76:79], a[52:55], v[64:67]\n"
  "v_add_f32 v112, v112, v152\n"
  "v_add_f32 v113, v113, v153\n"
  "v_mfma_f32_16x16x32_bf16 v[68:71], a[168:171], a[52:55], v[68:71]\n"
  "v_add_f32 v114, v114, v154\n"
  "v_add_f32 v115, v115, v155\n"
  "v_mfma_f32_16x16x32_bf16 v[72:75], a[172:175], a[52:55], v[72:75]\n"
  "s_waitcnt vmcnt(13)\n"
  "v_mfma_f32_16x16x32_bf16 v[76:79], a[72:75], a[56:59], v[76:79]\n"
  "buffer_load_dwordx4 v[188:191], %0, s[8:11], s44 offen offset:0 sc1\n"
  "v_mfma_f32_16x16x32_bf16 v[80:83], a[44:47], a[56:59], v[80:83]\n"
  "v_mfma_f32_16x16x32_bf16 v[84:87], a[76:79], a[56:59], v[84:87]\n"
  "v_mfma_f32_16x16x32_bf16 v[88:91], a[168:171], a[56:59], v[88:91]\n"
  "v_mfma_f32_16x16x32_bf16 v[92:95], a[172:175], a[56:59], v[92:95]\n"
  "s_waitcnt vmcnt(12)\n"
  "v_mfma_f32_16x16x32_bf16 v[16:19], a[176:179], a[60:63], v[16:19]\n"
  "s_waitcnt vmcnt(11)\n"
  "v_mfma_f32_16x16x32_bf16 v[20:23], a[164:167], a[60:63], v[20:23]\n"
  "s_waitcnt vmcnt(10)\n"
  "v_mfma_f32_16x16x32_bf16 v[24:27], a[80:83], a[60:63], v[24:27]\n"
  "s_waitcnt vmcnt(9)\n"
  "v_mfma_f32_16x16x32_bf16 v[28:31], a[192:195], a[60:63], v[28:31]\n"
  "s_waitcnt vmcnt(8)\n"
  "v_mfma_f32_16x16x32_bf16 v[32:35], a[196:199], a[60:63], v[32:35]\n"
  "s_waitcnt vmcnt(7)\n"
  "v_mfma_f32_16x16x32_bf16 v[36:39], a[176:179], a[180:183], v[36:39]\n"
  "v_mfma_f32_16x16x32_bf16 v[40:43], a[164:167], a[180:183], v[40:43]\n"
  "v_mfma_f32_16x16x32_bf16 v[44:47], a[80:83], a[180:183], v[44:47]\n"
  "v_mfma_f32_16x16x32_bf16 v[48:51], a[192:195], a[180:183], v[48:51]\n"
  "v_mfma_f32_16x16x32_bf16 v[52:55], a[196:199], a[180:183], v[52:55]\n"
  "s_waitcnt vmcnt(6)\n"
  "v_mfma_f32_16x16x32_bf16 v[56:59], a[176:179], a[84:87], v[56:59]\n"
  "v_mfma_f32_16x16x32_bf16 v[60:63], a[164:167], a[84:87], v[60:63]\n"
  "v_mfma_f32_16x16x32_bf16 v[64:67], a[80:83], a[84:87], v[64:67]\n"
  "v_mfma_f32_16x16x32_bf16 v[68:71], a[192:195], a[84:87], v[68:71]\n"
  "v_mfma_f32_16x16x32_bf16 v[72:75], a[196:199], a[84:87], v[72:75]\n"
  "s_waitcnt vmcnt(5)\n"
  "v_mfma_f32_16x16x32_bf16 v[76:79], a[176:179], a[88:91], v[76:79]\n"
  "v_mfma_f32_16x16x32_bf16 v[80:83], a[164:167], a[88:91], v[80:83]\n"
  "v_mfma_f32_16x16x32_bf16 v[84:87], a[80:83], a[88:91], v[84:87]\n"
  "v_mfma_f32_16x16x32_bf16 v[88:91], a[192:195], a[88:91], v[88:91]\n"
  "v_mfma_f32_16x16x32_bf16 v[92:95], a[196:199], a[88:91], v[92:95]\n"
  "s_nop 1\n"
  "ds_write_b128 v7, v[36:39] offset:16384\n"
  "ds_write_b128 v7, v[40:43] offset:17408\n"
  "ds_write_b128 v7, v[44:47] offset:18432\n"
  "ds_write_b128 v7, v[48:51] offset:19456\n"
  "ds_write_b128 v7, v[52:55] offset:20480\n"
  "ds_write_b128 v7, v[56:59] offset:21504\n"
  "ds_write_b128 v7, v[60:63] offset:22528\n"
  "ds_write_b128 v7, v[64:67] offset:23552\n"
  "ds_write_b128 v7, v[68:71] offset:24576\n"
  "ds_write_b128 v7, v[72:75] offset:25600\n"
  "ds_write_b128 v7, v[76:79] offset:26624\n"
  "ds_write_b128 v7, v[80:83] offset:27648\n"
  "ds_write_b128 v7, v[84:87] offset:28672\n"
  "ds_write_b128 v7, v[88:91] offset:29696\n"
  "ds_write_b128 v7, v[92:95] offset:30720\n"
  "s_waitcnt vmcnt(4)\n"
  "v_add_f32 v96, v96, v196\n"
  "v_add_f32 v97, v97, v197\n"
  "v_add_f32 v98, v98, v198\n"
  "v_add_f32 v99, v99, v199\n"
  "v_cvt_pk_bf16_f32 v96, v96, v97\n"
  "v_cvt_pk_bf16_f32 v97, v98, v99\n"
  "buffer_store_dwordx2 v[96:97], v6, s[12:15], 0 offen offset:0\n"
  "s_waitcnt vmcnt(4)\n"
  "v_add_f32 v100, v100, v176\n"
  "v_add_f32 v101, v101, v177\n"
  "v_add_f32 v102, v102, v178\n"
  "v_add_f32 v103, v103, v179\n"
  "v_cvt_pk_bf16_f32 v100, v100, v101\n"
  "v_cvt_pk_bf16_f32 v101, v102, v103\n"
  "buffer_store_dwordx2 v[100:101], v6, s[12:15], 0 offen offset:32\n"
  "s_waitcnt lgkmcnt(0)\n"
  "s_barrier\n"
  "ds_read_b128 v[36:39], v8 offset:16384\n"
  "ds_read_b128 v[40:43], v8 offset:17408\n"
  "ds_read_b128 v[44:47], v8 offset:18432\n"
  "ds_read_b128 v[48:51], v8 offset:19456\n"
  "ds_read_b128 v[52:55], v8 offset:20480\n"
  "ds_read_b128 v[56:59], v9 offset:16384\n"
  "ds_read_b128 v[60:63], v9 offset:17408\n"
  "ds_read_b128 v[64:67], v9 offset:18432\n"
  "ds_read_b128 v[68:71], v9 offset:19456\n"
  "ds_read_b128 v[72:75], v9 offset:20480\n"
  "ds_read_b128 v[76:79], v10 offset:16384\n"
  "ds_read_b128 v[80:83], v10 offset:17408\n"
  "ds_read_b128 v[84:87], v10 offset:18432\n"
  "ds_read_b128 v[88:91], v10 offset:19456\n"
  "ds_read_b128 v[92:95], v10 offset:20480\n"
  "s_waitcnt vmcnt(4)\n"
  "v_add_f32 v104, v104, v180\n"
  "v_add_f32 v105, v105, v181\n"
  "v_add_f32 v106, v106, v182\n"
  "v_add_f32 v107, v107, v183\n"
  "v_cvt_pk_bf16_f32 v104, v104, v105\n"
  "v_cvt_pk_bf16_f32 v105, v106, v107\n"
  "buffer_store_dwordx2 v[104:105], v6, s[12:15], 0 offen offset:64\n"
  "s_waitcnt vmcnt(4)\n"
  "v_add_f32 v108, v108, v184\n"
  "v_add_f32 v109, v109, v185\n"
  "v_add_f32 v110, v110, v186\n"
  "v_add_f32 v111, v111, v187\n"
  "v_cvt_pk_bf16_f32 v108, v108, v109\n"
  "v_cvt_pk_bf16_f32 v109, v110, v111\n"
  "buffer_store_dwordx2 v[108:109], v6, s[12:15], 0 offen offset:96\n"
  "s_waitcnt vmcnt(4)\n"
  "v_add_f32 v112, v112, v188\n"
  "v_add_f32 v113, v113, v189\n"
  "v_add_f32 v114, v114, v190\n"
  "v_add_f32 v115, v115, v191\n"
  "v_cvt_pk_bf16_f32 v112, v112, v113\n"
  "v_cvt_pk_bf16_f32 v113, v114, v115\n"
  "buffer_store_dwordx2 v[112:113], v6, s[12:15], 0 offen offset:128\n"
  "s_waitcnt lgkmcnt(0)\n"
  "v_pk_add_f32 v[16:17], v[16:17], v[36:37]\n"
  "v_pk_add_f32 v[18:19], v[18:19], v[38:39]\n"
  "v_pk_add_f32 v[56:57], v[56:57], v[76:77]\n"
  "v_pk_add_f32 v[58:59], v[58:59], v[78:79]\n"
  "v_pk_add_f32 v[16:17], v[16:17], v[56:57]\n"
  "v_pk_add_f32 v[18:19], v[18:19], v[58:59]\n"
  "v_pk_add_f32 v[20:21], v[20:21], v[40:41]\n"
  "v_pk_add_f32 v[22:23], v[22:23], v[42:43]\n"
  "v_pk_add_f32 v[60:61], v[60:61], v[80:81]\n"
  "v_pk_add_f32 v[62:63], v[62:63], v[82:83]\n"
  "v_pk_add_f32 v[20:21], v[20:21], v[60:61]\n"
  "v_pk_add_f32 v[22:23], v[22:23], v[62:63]\n"
  "v_pk_add_f32 v[24:25], v[24:25], v[44:45]\n"
  "v_pk_add_f32 v[26:27], v[26:27], v[46:47]\n"
  "v_pk_add_f32 v[64:65], v[64:65], v[84:85]\n"
  "v_pk_add_f32 v[66:67], v[66:67], v[86:87]\n"
  "v_pk_add_f32 v[24:25], v[24:25], v[64:65]\n"
  "v_pk_add_f32 v[26:27], v[26:27], v[66:67]\n"
  "v_pk_add_f32 v[28:29], v[28:29], v[48:49]\n"
  "v_pk_add_f32 v[30:31], v[30:31], v[50:51]\n"
  "v_pk_add_f32 v[68:69], v[68:69], v[88:89]\n"
  "v_pk_add_f32 v[70:71], v[70:71], v[90:91]\n"
  "v_pk_add_f32 v[28:29], v[28:29], v[68:69]\n"
  "v_pk_add_f32 v[30:31], v[30:31], v[70:71]\n"
  "v_pk_add_f32 v[32:33], v[32:33], v[52:53]\n"
  "v_pk_add_f32 v[34:35], v[34:35], v[54:55]\n"
  "v_pk_add_f32 v[72:73], v[72:73], v[92:93]\n"
  "v_pk_add_f32 v[74:75], v[74:75], v[94:95]\n"
  "v_pk_add_f32 v[32:33], v[32:33], v[72:73]\n"
  "v_pk_add_f32 v[34:35], v[34:35], v[74:75]\n"
  "v_cvt_pk_bf16_f32 v16, v16, v17\n"
  "v_cvt_pk_bf16_f32 v17, v18, v19\n"
  "buffer_store_dwordx2 v[16:17], v14, s[12:15], 0 offen offset:1280\n"
  "v_cvt_pk_bf16_f32 v20, v20, v21\n"
  "v_cvt_pk_bf16_f32 v21, v22, v23\n"
  "buffer_store_dwordx2 v[20:21], v14, s[12:15], 0 offen offset:1312\n"
  "v_cvt_pk_bf16_f32 v24, v24, v25\n"
  "v_cvt_pk_bf16_f32 v25, v26, v27\n"
  "buffer_store_dwordx2 v[24:25], v14, s[12:15], 0 offen offset:1344\n"
  "v_cvt_pk_bf16_f32 v28, v28, v29\n"
  "v_cvt_pk_bf16_f32 v29, v30, v31\n"
  "buffer_store_dwordx2 v[28:29], v14, s[12:15], 0 offen offset:1376\n"
  "v_cvt_pk_bf16_f32 v32, v32, v33\n"
  "v_cvt_pk_bf16_f32 v33, v34, v35\n"
  "buffer_store_dwordx2 v[32:33], v14, s[12:15], 0 offen offset:1408\n"
  "s_branch p3_fin\n"
  "c_spin%=:\n"
  "s_add_u32 s83, s83, 1\n"
  "s_cmp_gt_u32 s83, 0x40000\n"
  "s_cbranch_scc1 c_ok%=\n"
  "s_sleep 2\n"
  "buffer_load_dword v11, off, s[8:11], s23 sc1\n"
  "s_waitcnt vmcnt(0)\n"
  "v_readfirstlane_b32 s26, v11\n"
  "s_cmp_eq_u32 s26, 64\n"
  "s_cbranch_scc0 c_spin%=\n"
  "s_branch c_ok%=\n"
  "b_spin%=:\n"
  "s_add_u32 s83, s83, 1\n"
  "s_cmp_gt_u32 s83, 0x40000\n"
  "s_cbranch_scc1 b_ok%=\n"
  "s_sleep 2\n"
  "buffer_load_dword v11, off, s[8:11], s24 sc1\n"
  "s_waitcnt vmcnt(0)\n"
  "v_readfirstlane_b32 s26, v11\n"
  "s_cmp_eq_u32 s26, 32\n"
  "s_cbranch_scc0 b_spin%=\n"
  "s_branch b_ok%=\n"
  "x_spin%=:\n"
  "s_add_u32 s83, s83, 1\n"
  "s_cmp_gt_u32 s83, 0x40000\n"
  "s_cbranch_scc1 x_ok%=\n"
  "s_sleep 2\n"
  "buffer_load_dword v15, off, s[8:11], s40 sc1\n"
  "s_waitcnt vmcnt(0)\n"
  "v_readfirstlane_b32 s26, v15\n"
  "s_cmp_eq_u32 s26, 4\n"
  "s_cbranch_scc0 x_spin%=\n"
  "s_branch x_ok%=\n"
  "p3_fin:\n"
  :
  : "v"(l16), "s"(ws), "s"(out), "s"(bid), "s"(w) : "v1","v2","v3","v4","v5","v6","v7","v8","v9","v10","v11","v12","v13","v14","v15","v16","v17","v18","v19","v20","v21","v22","v23","v24","v25","v26","v27","v28","v29","v30","v31","v32","v33","v34","v35","v36","v37","v38","v39","v40","v41","v42","v43","v44","v45","v46","v47","v48","v49","v50","v51","v52","v53","v54","v55","v56","v57","v58","v59","v60","v61","v62","v63","v64","v65","v66","v67","v68","v69","v70","v71","v72","v73","v74","v75","v76","v77","v78","v79","v80","v81","v82","v83","v84","v85","v86","v87","v88","v89","v90","v91","v92","v93","v94","v95","v96","v97","v98","v99","v100","v101","v102","v103","v104","v105","v106","v107","v108","v109","v110","v111","v112","v113","v114","v115","v116","v117","v118","v119","v120","v121","v122","v123","v124","v125","v126","v127","v128","v129","v130","v131","v132","v133","v134","v135","v136","v137","v138","v139","v140","v141","v142","v143","v144","v145","v146","v147","v148","v149","v150","v151","v152","v153","v154","v155","v156","v157","v158","v159","v160","v161","v162","v163","v164","v165","v166","v167","v168","v169","v170","v171","v172","v173","v174","v175","v176","v177","v178","v179","v180","v181","v182","v183","v184","v185","v186","v187","v188","v189","v190","v191","v192","v193","v194","v195","v196","v197","v198","v199","v200","v201","v202","v203","v204","v205","v206","v207","v208","v209","v210","v211","v212","v213","v214","v215","v216","v217","v218","v219","v220","v221","v222","v223","v224","v225","v226","v227","v228","v229","v230","v231","v232","v233","v234","v235","v236","v237","v238","v239","v240","v241","v242","v243","v244","v245","v246","v247","v248","v249","v250","v251","v252","v253","v254","v255","a0","a1","a2","a3","a4","a5","a6","a7","a8","a9","a10","a11","a12","a13","a14","a15","a16","a17","a18","a19","a20","a21","a22","a23","a24","a25","a26","a27","a28","a29","a30","a31","a32","a33","a34","a35","a36","a37","a38","a39","a40","a41","a42","a43","a44","a45","a46","a47","a48","a49","a50","a51","a52","a53","a54","a55","a56","a57","a58","a59","a60","a61","a62","a63","a64","a65","a66","a67","a68","a69","a70","a71","a72","a73","a74","a75","a76","a77","a78","a79","a80","a81","a82","a83","a84","a85","a86","a87","a88","a89","a90","a91","a92","a93","a94","a95","a96","a97","a98","a99","a100","a101","a102","a103","a104","a105","a106","a107","a108","a109","a110","a111","a112","a113","a114","a115","a116","a117","a118","a119","a120","a121","a122","a123","a124","a125","a126","a127","a128","a129","a130","a131","a132","a133","a134","a135","a136","a137","a138","a139","a140","a141","a142","a143","a144","a145","a146","a147","a148","a149","a150","a151","a152","a153","a154","a155","a156","a157","a158","a159","a160","a161","a162","a163","a164","a165","a166","a167","a168","a169","a170","a171","a172","a173","a174","a175","a176","a177","a178","a179","a180","a181","a182","a183","a184","a185","a186","a187","a188","a189","a190","a191","a192","a193","a194","a195","a196","a197","a198","a199","a200","a201","a202","a203","a204","a205","a206","a207","a208","a209","a210","a211","a212","a213","a214","a215","a216","a217","a218","a219","a220","a221","a222","a223","a224","a225","a226","a227","a228","a229","a230","a231","a232","a233","a234","a235","a236","a237","a238","a239","a240","a241","a242","a243","a244","a245","a246","a247","a248","a249","a250","a251","a252","a253","a254","a255","s8","s9","s10","s11","s12","s13","s14","s15","s16","s17","s18","s19","s20","s21","s23","s24","s25","s26","s27","s28","s29","s30","s31","s38","s39","s40","s41","s42","s43","s44","s45","s46","s83","vcc","scc","memory");
}

extern "C" void kernel_launch(void* const* d_in, const int* in_sizes, int n_in, void* d_out, int out_size, void* d_ws, size_t ws_size,
                              hipStream_t stream) {
  const char* const* in = (const char* const*)d_in;
  fused<<<256, 256, 0, stream>>>(in[0], in[1], in[2], in[3], in[4], in[5], in[6], in[7], in[8], (char*)d_ws, (char*)d_out);
}
```
